```python
import torch

def _softmax(x, axis=-1):
    return torch.softmax(x, dim=axis)
def _gelu(x):
    return torch.nn.functional.gelu(x, approximate="tanh")
def _silu(x):
    return torch.nn.functional.silu(x)
def _sigmoid(x):
    return torch.sigmoid(x)
def _layernorm(x, axis=-1, eps=1e-5):
    m = x.mean(dim=axis, keepdim=True)
    v = x.var(dim=axis, keepdim=True, unbiased=False)
    return (x - m) / torch.sqrt(v + eps)
def _scatter_add(tgt, idx, src, axis=0):
    return tgt.clone().index_add_(axis, idx, src)
def _segment_sum(src, seg, n):
    out = torch.zeros((n,) + src.shape[1:], dtype=src.dtype)
    return out.index_add_(0, seg, src)
def _pad(x, pw):
    flat = tuple(v for pair in reversed(pw) for v in pair)
    return torch.nn.functional.pad(x, flat)

kernel_name = 'parallel_sum__sliding_local_dilated__moe_arch_mod__prompt'

def setup_inputs():

    _w = (lambda a: (lambda t: t.to(torch.bfloat16) if t.is_floating_point() else t)(torch.as_tensor(a)))
    def _A_setup():
        B, S, D = 1, 2048, 1024
        return {
            "x": _w((lambda *s: torch.randn(*s, dtype=torch.bfloat16) * 0.05)(B, S, D)),
            "wqkv": _w((lambda *s: torch.randn(*s, dtype=torch.bfloat16) * 0.05)(D, 3 * D)),
            "wo": _w((lambda *s: torch.randn(*s, dtype=torch.bfloat16) * 0.05)(D, D)),
        }
    _A_in = _A_setup()

    def _B_setup():
        B, S, D, F, C = 1, 2048, 1024, 3584, 8
        return {
            "x": _w((lambda *s: torch.randn(*s, dtype=torch.bfloat16) * 0.05)(B, S, D)),
            "w_r": _w((lambda *s: torch.randn(*s, dtype=torch.bfloat16) * 0.05)(D)),
            "w_gate": _w((lambda *s: torch.randn(*s, dtype=torch.bfloat16) * 0.05)(D, F)),
            "w_up": _w((lambda *s: torch.randn(*s, dtype=torch.bfloat16) * 0.05)(D, F)),
            "w_down": _w((lambda *s: torch.randn(*s, dtype=torch.bfloat16) * 0.05)(F, D)),
        }
    _B_in = _B_setup()

    return {
        "A_x": _A_in["x"],
        "A_wqkv": _A_in["wqkv"],
        "A_wo": _A_in["wo"],
        "B_x": _B_in["x"],
        "B_w_r": _B_in["w_r"],
        "B_w_gate": _B_in["w_gate"],
        "B_w_up": _B_in["w_up"],
        "B_w_down": _B_in["w_down"],
    }

def reference(A_x, A_wqkv, A_wo, B_x, B_w_r, B_w_gate, B_w_up, B_w_down):
    def _A_ref(x, wqkv, wo):
        B, S, D = x.shape
        H = 32; Dh = D // H
        W, dil = 8, 2
        qkv = (lambda x, *d: x.permute(*d))((x @ wqkv).reshape(B, S, 3, H, Dh), 2, 0, 3, 1, 4)
        q, k, v = qkv[0], qkv[1], qkv[2]
        scale = Dh ** -0.5
        pos = torch.arange(S)
        off = torch.arange(W) * dil
        idx = pos[:, None] - off[None, :]
        mask = idx >= 0
        idx_c = torch.where(mask, idx, 0).reshape(-1)
        kg = k[:, :, idx_c, :].reshape(B, H, S, W, Dh)
        vg = v[:, :, idx_c, :].reshape(B, H, S, W, Dh)
        logits = torch.einsum("bhsd,bhswd->bhsw", q, kg) * scale
        logits = torch.where(mask[None, None, :, :], logits, -1e9)
        attn = _softmax(logits, axis=-1)
        o = torch.einsum("bhsw,bhswd->bhsd", attn, vg)
        return (lambda x, *d: x.permute(*d))(o, 0, 2, 1, 3).reshape(B, S, D) @ wo

    def _B_ref(x, w_r, w_gate, w_up, w_down):
        B, S, D = x.shape
        C = 8
        r = x @ w_r
        idx = torch.argsort(-r, axis=-1)[:, :C]
        idx_e = (lambda x, n, axis: torch.repeat_interleave(x, n, dim=axis))(idx.reshape(B, C, 1), D, axis=2)
        x_sel = (lambda x, i, axis: torch.take_along_dim(x, i, dim=axis))(x, idx_e, axis=1)
        g = _silu(x_sel @ w_gate)
        u = x_sel @ w_up
        y = (g * u) @ w_down
        r_sel = _sigmoid((lambda x, i, axis: torch.take_along_dim(x, i, dim=axis))(r, idx, axis=1))
        y = y * r_sel.reshape(B, C, 1)
        b_off = (torch.arange(B) * S).reshape(B, 1)
        flat_idx = (idx + b_off).reshape(B * C)
        zero = (x * 0.0).reshape(B * S, D)
        upd = _scatter_add(zero, flat_idx, y.reshape(B * C, D), axis=0)
        return x + upd.reshape(B, S, D)

    x = A_x
    xn = _layernorm(x, axis=-1)
    return x + _A_ref(xn, A_wqkv, A_wo) + _B_ref(xn, B_w_r, B_w_gate, B_w_up, B_w_down)
```

```cpp
#define CGPERM
#include <hip/hip_runtime.h>
#include <stdint.h>

typedef unsigned short u16;
typedef unsigned u32;
typedef unsigned long long u64;
typedef __attribute__((ext_vector_type(4))) short s16x4;
typedef __attribute__((ext_vector_type(8))) short s16x8;
typedef __attribute__((ext_vector_type(2))) float f32x2;
typedef __attribute__((ext_vector_type(4))) float f32x4;
typedef __attribute__((ext_vector_type(4))) unsigned u32x4;
typedef __attribute__((ext_vector_type(2))) unsigned u32x2;
typedef __attribute__((ext_vector_type(16))) unsigned u32x16;
typedef __attribute__((ext_vector_type(2))) __bf16 bf16x2;
typedef __amdgpu_buffer_rsrc_t rsrc_t;
#define LAS __attribute__((address_space(3)))
#define GAS __attribute__((address_space(1)))
#define DEV __device__ __forceinline__

#define CGMAP (int)((((loc & 16) ? 0xfedc0a987653421bull : 0xfedc8a9b76534210ull) >> (4 * (loc & 15))) & 15)
#define STAMPS 0
constexpr int OWIN = 6;
constexpr int GWIN = 5, GPRE = 3;
#define TH_ 1100
#define TO_ 1300
#define TG_ 350
#define TS_ 580
constexpr int T_GATHER = TG_, T_SEL = TS_, T_H = TH_, T_O = TO_;
constexpr int WARM_AT = 10;
constexpr u64 HELPK = 0x765432ull, ROLLK = 0xfedcba98ull;
constexpr int HELPN = 6, RS0 = 1, RSN = 8;
constexpr int CODE_LINES = 326;

constexpr int S_ = 2048, D_ = 1024, F_ = 3584;
constexpr u32 WS_EPOCH = 0;
constexpr u32 WS_SEL = 4096;
constexpr u32 WS_RG = 16384;
constexpr u32 WS_SG = 32768;
constexpr u32 WS_OF = 65536;
constexpr u32 WS_H = WS_OF + 131072;
constexpr u32 WS_YS = WS_H + 131072;
constexpr u32 WS_O = WS_YS + 524288;
constexpr u32 WS_END = WS_O + S_ * D_ * 2;
constexpr u32 WS_STAMP = 16u << 20;
constexpr u32 WOFF = 22528, SLOT = 47104;
constexpr u32 L_HX = 131072;
constexpr u32 L_XT = 3 * SLOT;
constexpr u32 L_STQ = L_XT + 16384;
constexpr u32 L_STO = L_STQ + 1408;
constexpr u32 L_UPD = L_STO + 1024;
constexpr u32 L_ONE = L_UPD + 2048;
constexpr u32 L_CTL = L_ONE + 128;
constexpr u32 L_TAG = L_CTL, L_ARR = L_CTL + 4, L_UPDF = L_CTL + 8  , L_HS = L_CTL + 16  , L_SEL = L_CTL + 32  ;
constexpr u32 L_PW = L_CTL + 64, L_HR = L_CTL + 96;
constexpr u32 L_GO = L_CTL + 128;
constexpr u32 L_END = L_CTL + 192;
constexpr int SPIN_CAP = 1 << 22;
static_assert(L_END <= 163840 && L_ONE % 128 == 0, "LDS");

struct Params {
  const u16 *x, *wqkv, *wo, *wr, *wg, *wu, *wd;
  u16* out;
  char* ws;
};
struct Args {
  Params p;
  u64 kt[64];
};

DEV float bf_lo(u32 v) { return __uint_as_float(v << 16); }
DEV float bf_hi(u32 v) { return __uint_as_float(v & 0xffff0000u); }
DEV u32 pack2(float a, float b) { bf16x2 p = {(__bf16)a, (__bf16)b}; return __builtin_bit_cast(u32, p); }
DEV float rbf(float a) { return bf_lo(pack2(a, a)); }
DEV float dot2(u32 a, u32 b, float c) { return __builtin_amdgcn_fdot2_f32_bf16(__builtin_bit_cast(bf16x2, a), __builtin_bit_cast(bf16x2, b), c, false); }
DEV s16x8 as_frag(u32x4 v) { return __builtin_bit_cast(s16x8, v); }
DEV f32x4 mfma(s16x8 a, s16x8 b, f32x4 c) { return __builtin_amdgcn_mfma_f32_16x16x32_bf16(a, b, c, 0, 0, 0); }
DEV int rfl(int v) { return __builtin_amdgcn_readfirstlane(v); }
template <int CTRL, int RM, bool BC> DEV float dppf(float v) { return __int_as_float(__builtin_amdgcn_update_dpp(0, __float_as_int(v), CTRL, RM, 0xf, BC)); }
template <int CTRL, int RM, bool BC> DEV u32 dppu(u32 v) { return (u32)__builtin_amdgcn_update_dpp(0, (int)v, CTRL, RM, 0xf, BC); }
DEV float half_sum(float v) {
  v += dppf<0x111, 0xf, true>(v); v += dppf<0x112, 0xf, true>(v); v += dppf<0x114, 0xf, true>(v); v += dppf<0x118, 0xf, true>(v);
  v += dppf<0x142, 0xa, false>(v);
  return v;
}
DEV u32 wave_max_u32(u32 v) {
  v = max(v, dppu<0x111, 0xf, true>(v)); v = max(v, dppu<0x112, 0xf, true>(v)); v = max(v, dppu<0x114, 0xf, true>(v)); v = max(v, dppu<0x118, 0xf, true>(v));
  v = max(v, dppu<0x142, 0xa, false>(v)); v = max(v, dppu<0x143, 0xc, false>(v));
  return (u32)__builtin_amdgcn_readlane((int)v, 63);
}
DEV float vmax(float a, float b) { return __builtin_elementwise_maximum(a, b); }
DEV float vmax3(float a, float b, float c) { return vmax(vmax(a, b), c); }
DEV float max16(float v) { u32 a = __float_as_uint(v); auto r = __builtin_amdgcn_permlane16_swap(a, a, false, false); u32 x = r[0], y = r[1]; return vmax(__uint_as_float(x), __uint_as_float(y)); }
DEV float max32(float v) { u32 a = __float_as_uint(v); auto r = __builtin_amdgcn_permlane32_swap(a, a, false, false); u32 x = r[0], y = r[1]; return vmax(__uint_as_float(x), __uint_as_float(y)); }
DEV float sum32(float v) { u32 a = __float_as_uint(v); auto r = __builtin_amdgcn_permlane32_swap(a, a, false, false); u32 x = r[0], y = r[1]; return __uint_as_float(x) + __uint_as_float(y); }
DEV u32 ord16(u32 b) { return (b & 0x8000u) ? (~b & 0xffffu) : (b | 0x8000u); }
DEV float sigm(float v) { return __builtin_amdgcn_rcpf(1.f + __builtin_amdgcn_exp2f(-v * 1.4426950408889634f)); }

DEV rsrc_t mk_rsrc(const void* p, u32 bytes) { return __builtin_amdgcn_make_buffer_rsrc((void*)p, 0, (int)bytes, 0x00020000); }
DEV u32x4 ld_nt_128(rsrc_t rs, u32 voff, u32 soff) { return __builtin_amdgcn_raw_buffer_load_b128(rs, voff, soff, 2); }
DEV u32x4 ld_sc1_128(rsrc_t rs, u32 voff, u32 soff) { return __builtin_amdgcn_raw_buffer_load_b128(rs, voff, soff, 16); }
DEV u32x2 ld_sc1_64(rsrc_t rs, u32 voff, u32 soff) { return __builtin_amdgcn_raw_buffer_load_b64(rs, voff, soff, 16); }
DEV u32 ld_sc1_32(rsrc_t rs, u32 voff, u32 soff) { return __builtin_amdgcn_raw_buffer_load_b32(rs, voff, soff, 16); }
DEV void st_sc1_128(u32x4 v, rsrc_t rs, u32 voff) { __builtin_amdgcn_raw_buffer_store_b128(v, rs, voff, 0, 16); }
DEV void st_sc1_64(u32x2 v, rsrc_t rs, u32 voff) { __builtin_amdgcn_raw_buffer_store_b64(v, rs, voff, 0, 16); }
DEV void st_sc1_32(u32 v, rsrc_t rs, u32 voff) { __builtin_amdgcn_raw_buffer_store_b32(v, rs, voff, 0, 16); }
#define VMCNT(n) asm volatile("s_waitcnt vmcnt(" #n ")" ::: "memory")
#define LGKM0() asm volatile("s_waitcnt lgkmcnt(0)" ::: "memory")
#define CBAR() asm volatile("" ::: "memory")
#define BAR() do { CBAR(); __builtin_amdgcn_s_barrier(); CBAR(); } while (0)
#define SB() __builtin_amdgcn_sched_barrier(0)
#define LP(a) ((LAS char*)(uintptr_t)(a))
#define PIN(x) asm volatile("" : "+v"(x))

#define TICK() 0ull
#define RT() 0ull
#define STAMP(k) do {} while (0)
#define CSTAMP(k) do {} while (0)
#define LSTAMP(k) do {} while (0)

DEV u32 lds_rd(LAS char* L, u32 off) { return *(volatile LAS u32*)(L + off); }
DEV void lds_wr(LAS char* L, u32 off, u32 v) { *(volatile LAS u32*)(L + off) = v; }
DEV void lds_wait_set(LAS char* L, u32 off) {
  _Pragma("nounroll") for (int sp = 0; sp < SPIN_CAP; ++sp) {
    if (__all(lds_rd(L, off) != 0u)) break;
    __builtin_amdgcn_s_sleep(1);
  }
  CBAR();
}
DEV void wait_until(u64 t0, u32 ticks) {
  while (__builtin_amdgcn_s_memrealtime() - t0 < ticks) __builtin_amdgcn_s_sleep(4);
}
#define DMA_HEAD "s_mov_b32 m0, %0\n\ts_nop 0\n\t"
DEV void dma16(u32 lds, u32 voff, rsrc_t rs, u32 soff) {
  asm volatile(DMA_HEAD "buffer_load_dwordx4 %1, %2, %3 offen lds" ::"s"(lds), "v"(voff), "s"(rs), "s"(soff) : "memory");
}
DEV void dma16x4(u32 lds, u32 v0, u32 v1, u32 v2, u32 v3, rsrc_t rs, u32 soff) {
  asm volatile(DMA_HEAD "buffer_load_dwordx4 %1, %5, %6 offen lds\n\tbuffer_load_dwordx4 %2, %5, %6 offen offset:1024 lds\n\t"
               "buffer_load_dwordx4 %3, %5, %6 offen offset:2048 lds\n\tbuffer_load_dwordx4 %4, %5, %6 offen offset:3072 lds"
               ::"s"(lds), "v"(v0), "v"(v1), "v"(v2), "v"(v3), "s"(rs), "s"(soff) : "memory");
}
DEV void dma16x2(u32 lds, u32 v0, u32 v1, rsrc_t rs, u32 soff) {
  asm volatile(DMA_HEAD "buffer_load_dwordx4 %1, %3, %4 offen lds\n\tbuffer_load_dwordx4 %2, %3, %4 offen offset:1024 lds"
               ::"s"(lds), "v"(v0), "v"(v1), "s"(rs), "s"(soff) : "memory");
}

struct Frags { s16x8 x[6]; s16x8 w[6]; };

DEV s16x8 tr_frag(LAS char* p, LAS char* q, u32 o0, u32 o1) {
  s16x4 a = __builtin_amdgcn_ds_read_tr16_b64_v4i16((LAS s16x4*)(p + o0));
  s16x4 b = __builtin_amdgcn_ds_read_tr16_b64_v4i16((LAS s16x4*)(q + o1));
  return __builtin_shufflevector(a, b, 0, 1, 2, 3, 4, 5, 6, 7);
}
template <int KK> DEV void load_frags(Frags& f, LAS char* aH, LAS char* aT, LAS char* w0, LAS char* w1) {
#pragma unroll
  for (int t = 0; t < 5; ++t) f.x[1 + t] = *(LAS s16x8*)(aT + t * 2048);
  f.x[0] = *(LAS s16x8*)aH;
#pragma unroll
  for (int sec = 0; sec < 3; ++sec) {
    f.w[2 * sec] = tr_frag(w0, w0, sec * 8192 + KK * 4096, sec * 8192 + KK * 4096 + 512);
    f.w[2 * sec + 1] = tr_frag(w1, w1, sec * 8192 + KK * 4096, sec * 8192 + KK * 4096 + 512);
  }
}
template <int A, int B> DEV void mma_range(f32x4 (&acc)[6][6], const Frags& f, s16x8 e0) {
#pragma unroll
  for (int n = A; n < B; ++n) {
    const int p = n / 11, r = n % 11, w = 2 * p + (r >= 6), t = (r >= 6) ? 11 - r : 5 - r;
    acc[w][t] = (w < 4) ? mfma(f.w[w], (w == 0 && t == 0) ? e0 : f.x[t], acc[w][t]) : mfma(f.x[t], f.w[w], acc[w][t]);
  }
}
DEV void interleave() {
#pragma unroll
  for (int k = 0; k < 6; ++k) { __builtin_amdgcn_sched_group_barrier(0x008, 1, 0); __builtin_amdgcn_sched_group_barrier(0x100, 1, 0); }
#pragma unroll
  for (int k = 0; k < 6; ++k) {
    __builtin_amdgcn_sched_group_barrier(0x008, 1, 0); __builtin_amdgcn_sched_group_barrier(0x100, 1, 0);
    __builtin_amdgcn_sched_group_barrier(0x008, 1, 0); __builtin_amdgcn_sched_group_barrier(0x100, 1, 0);
    __builtin_amdgcn_sched_group_barrier(0x008, 1, 0);
  }
}

template <int E> DEV s16x8 col_frag(const u32x4* L) {
  constexpr u32 sel = (E & 1) ? 0x07060302u : 0x05040100u;
  u32x4 r;
  r.x = __builtin_amdgcn_perm(L[1][E >> 1], L[0][E >> 1], sel);
  r.y = __builtin_amdgcn_perm(L[3][E >> 1], L[2][E >> 1], sel);
  r.z = __builtin_amdgcn_perm(L[5][E >> 1], L[4][E >> 1], sel);
  r.w = __builtin_amdgcn_perm(L[7][E >> 1], L[6][E >> 1], sel);
  return as_frag(r);
}
DEV void mma_cols(f32x4* acc, const u32x4* L, s16x8 b) {
  acc[0] = mfma(col_frag<0>(L), b, acc[0]); acc[1] = mfma(col_frag<1>(L), b, acc[1]);
  acc[2] = mfma(col_frag<2>(L), b, acc[2]); acc[3] = mfma(col_frag<3>(L), b, acc[3]);
  acc[4] = mfma(col_frag<4>(L), b, acc[4]); acc[5] = mfma(col_frag<5>(L), b, acc[5]);
  acc[6] = mfma(col_frag<6>(L), b, acc[6]); acc[7] = mfma(col_frag<7>(L), b, acc[7]);
}

DEV void transpose_grp(u32x4* L) {
  const s16x8 f0 = col_frag<0>(L), f1 = col_frag<1>(L), f2 = col_frag<2>(L), f3 = col_frag<3>(L), f4 = col_frag<4>(L), f5 = col_frag<5>(L), f6 = col_frag<6>(L), f7 = col_frag<7>(L);
  L[0] = __builtin_bit_cast(u32x4, f0); L[1] = __builtin_bit_cast(u32x4, f1); L[2] = __builtin_bit_cast(u32x4, f2); L[3] = __builtin_bit_cast(u32x4, f3);
  L[4] = __builtin_bit_cast(u32x4, f4); L[5] = __builtin_bit_cast(u32x4, f5); L[6] = __builtin_bit_cast(u32x4, f6); L[7] = __builtin_bit_cast(u32x4, f7);
#pragma unroll
  for (int e = 0; e < 8; ++e) PIN(L[e]);
  SB();
}
DEV void mma_frags8(f32x4* acc, const u32x4* L, s16x8 b) {
#pragma unroll
  for (int e = 0; e < 8; ++e) acc[e] = mfma(as_frag(L[e]), b, acc[e]);
}
DEV void warm_line(u32 lds, const char* a) {
  asm volatile(DMA_HEAD "global_load_lds_dword %1, off" ::"s"(lds), "v"(a) : "memory");
}

__global__ void __launch_bounds__(512, 2) fused_kernel(Args Parg) {
  extern __shared__ __attribute__((aligned(128))) char smem[];
  u32x16 ka;
  u64 kb;
  asm volatile("s_load_dwordx16 %0, %2, 0x0\n\ts_load_dwordx2 %1, %2, 0x40" : "=&s"(ka), "=&s"(kb) : "s"(__builtin_amdgcn_kernarg_segment_ptr()));
  Params P;
  const char* pc0;
  asm volatile("s_getpc_b64 %0" : "=s"(pc0));
  LAS char* L = (LAS char*)smem;
  const u32 lbase = (u32)(uintptr_t)L;
  const int lane = threadIdx.x & 63;
  const int wave = rfl(threadIdx.x >> 6);
  const int bid = blockIdx.x, xg = bid & 7, loc = bid >> 3;
  const int np = 7 - (xg & 1);
  const bool gemm = loc < 4 * np;
  const int pl = loc >> 2, hl = loc & 3;
  const int pan = (xg & 1) * 7 + pl, hp = (xg >> 1) * 4 + hl, r0 = pan * 160;
  const int fi = loc - 4 * np;
  const bool dwn = (u32)(fi - 2 - 3 * (xg & 1)) <= 2u * (xg & 1);
  const int fu = (xg & 1) * 12 + (xg >> 1) * (3 + 2 * (xg & 1)) + min(fi, 2 + 2 * (xg & 1));
  const int du = (xg & 1) ? 4 + (xg >> 1) * 3 + fi - 5 : (xg >> 1);
  const int pan2 = (xg & 1) * 8 + (xg >> 1) * 2 + (loc >> 4), cg = CGMAP, hrot = 2 * cg;
  const int g = lane >> 4, i = lane & 15;
  u64 kord;
  asm volatile("s_load_dwordx2 %0, %1, %2" : "=&s"(kord) : "s"(__builtin_amdgcn_kernarg_segment_ptr()), "s"(72 + (((xg & 1) * 32 + loc) & 63) * 8));
  rsrc_t rsWS;
  const u64 tstart = __builtin_amdgcn_s_memrealtime();
  u32 tag = 0;
  u64 cyc0 = 0;
  f32x4 oc[2][4];
  auto oc_init = [&]() {
    const int tl = (wave >> 1) + 64 * (wave & 1) + 2 * i;
    f32x2 st[2];
    u32x2 v[2][4];
#pragma unroll
    for (int tt = 0; tt < 2; ++tt) {
      st[tt] = *(LAS f32x2*)(L + L_STO + tl * 8 + tt * 256);
#pragma unroll
      for (int c = 0; c < 4; ++c) v[tt][c] = *(LAS u32x2*)(L + L_XT + tl * 128 + (((2 * c + (g >> 1)) ^ (i & 7)) << 4) + (g & 1) * 8 + tt * 4096);
    }
    SB();
#pragma unroll
    for (int tt = 0; tt < 2; ++tt) {
      const float k1 = 1.f + st[tt].y, k0 = -st[tt].x * st[tt].y;
#pragma unroll
      for (int c = 0; c < 4; ++c) oc[tt][c] = f32x4{bf_lo(v[tt][c].x), bf_hi(v[tt][c].x), bf_lo(v[tt][c].y), bf_hi(v[tt][c].y)} * k1 + k0;
    }
#pragma unroll
    for (int q = 0; q < 8; ++q) PIN(oc[q >> 2][q & 3]);
  };

  u32 tq[3], vO, vx;
  auto helper_offsets = [&]() {
    u32 lane = threadIdx.x & 63;
    PIN(lane);
    const int lr = lane >> 3, lc = lane & 7;
#pragma unroll
    for (int j = 0; j < 3; ++j) {
      const int rho = min(64 * j + (int)lane, 175), pr = rho >= 96;
      const int tk = r0 + ((rho < 16) ? rho - 16 : pr + 2 * (rho - 16 - 80 * pr));
      const u32 a = (u32)(pan2 * 128 + (rho & 127)) * 8, b = (((u32)tk >= (u32)S_) | (rho == 0)) ? 1u : (u32)tk * 8;
      tq[j] = (wave == 7) ? a : b;
      PIN(tq[j]);
    }
    vO = lr * 2048 + ((u32)(lc ^ (((lr >> 1) & 3) << 1)) << 4) + cg * 128;
    vx = lr * 2048 + ((u32)(lc ^ (lr >> 1)) << 4) + cg * 128;
    PIN(vO); PIN(vx);
  };
  auto wo_grp = [&](int q) { dma16x4(lbase + q * 4096, vO, vO + 15 * 1024, vO + 30 * 1024, vO + 45 * 1024, mk_rsrc(P.wo, D_ * D_ * 2), ((q + hrot) & 31) * 65536); };
  auto helper_fill = [&]() {
    const int lid = wave - 4;
    tag = (u32)rfl((int)lds_rd(L, L_TAG));
    if (gemm) { VMCNT(11); BAR(); }
    if (lid >= 2) {
      u32x2 sv[3];
#pragma unroll
      for (int q = 2 * lid - 4; q < 2 * lid - 2; ++q) dma16x4(lbase + L_XT + q * 4096, vx, (vx ^ 64) + 15 * 1024, vx + 30 * 1024, (vx ^ 64) + 45 * 1024, mk_rsrc(P.x, S_ * D_ * 2), (pan2 * 128 + 32 * q) * 2048);
#pragma unroll
      for (int j = 0; j < 3; ++j) sv[j] = ld_sc1_64(rsWS, tq[j] & ~7u, WS_SG);
      SB();
      if (gemm) { VMCNT(11); BAR(); }
      _Pragma("nounroll") for (int sp = 0; sp < SPIN_CAP; ++sp) {
        if (__all(sv[0].y == tag && sv[1].y == tag && sv[2].y == tag)) break;
        __builtin_amdgcn_s_sleep(2);
#pragma unroll
        for (int j = 0; j < 3; ++j) sv[j] = ld_sc1_64(rsWS, tq[j] & ~7u, WS_SG);
      }
      f32x2 st[3];
      u32 ta = lbase + ((lid == 3) ? L_STO + lane * 8 : L_STQ + lane * 4), tb2 = ta + ((lid == 3) ? 4 : 704);
      u32 tc = (lid == 2 && lane < 48) ? ta + 512 : lbase + L_UPD + lane * 4, td = (lid == 2 && lane < 48) ? tb2 + 512 : tc;
      const u32 ts = (lid == 3) ? 512 : 256;
#pragma unroll
      for (int j = 0; j < 3; ++j) {
        st[j] = f32x2{__uint_as_float((sv[j].x << 16) ^ ((lid == 3) ? 0u : 0x80000000u)), __builtin_amdgcn_rcpf(bf_hi(sv[j].x))};
        if (tq[j] & 1u) st[j] = f32x2{0.f, 0.f};
        PIN(st[j]);
      }
      u32 te = ta + ts, tf = tb2 + ts;
      PIN(ta); PIN(tb2); PIN(tc); PIN(td); PIN(te); PIN(tf);
      VMCNT(0);
      if (gemm) BAR();
      *(LAS float*)LP(ta) = st[0].x; *(LAS float*)LP(tb2) = st[0].y;
      *(LAS float*)LP(te) = st[1].x; *(LAS float*)LP(tf) = st[1].y;
      *(LAS float*)LP(tc) = st[2].x; *(LAS float*)LP(td) = st[2].y;
      LGKM0();
    } else {
      if (gemm) { VMCNT(0); BAR(); }
      wo_grp(12 + lid); wo_grp(14 + lid);
#pragma unroll
      for (int q = 0; q < 4; ++q) wo_grp(16 + 2 * q + lid);
      if (gemm) BAR();
    }
    if (gemm) BAR();
    int l2 = lid;
    asm volatile("" : "+s"(l2));
    wo_grp(l2); wo_grp(4 + l2); wo_grp(8 + l2);
    if (!dwn) { wo_grp(24 + l2); wo_grp(28 + l2); }
    if (lid == 2) {
      u32x2 sk;
      _Pragma("nounroll") for (int sp = 0; sp < SPIN_CAP; ++sp) {
        sk = ld_sc1_64(rsWS, (lane & 7) * 8, WS_SEL);
        if (__all(sk.y == tag)) break;
        __builtin_amdgcn_s_sleep(8);
      }
      if (lane < 8) lds_wr(L, L_SEL + 4 * lane, sk.x);
      LGKM0();
    }
  };
  {
    u32 ln = threadIdx.x & 63;
    PIN(ln);
    if (ln < 2) warm_line(lbase + L_UPD, pc0 + min((loc * 8 + wave) * 2 + (int)ln, CODE_LINES - 1) * 128);
  }
  const int lid = wave - 4;
  const bool isx = lid < 2;
  u32 v0 = 0, v1 = 0, v2 = 0, v3 = 0, vh = 0, g0 = 0, g1 = 0, g2 = 0, kstep = 0, d0 = 0;
  {
    int wave_a = wave;
    asm volatile("" : "+s"(wave_a));
    if (gemm && wave_a >= 4) {
      u32 ln = threadIdx.x & 63;
      PIN(ln);
      const int lr = ln >> 3, lc = ln & 7;
      const u32 sw = (u32)(lc ^ (isx ? lr >> 1 : lr & 2)) << 4;
      const u32 vb = lr * (isx ? 4096 : 6144) + sw + (isx ? (r0 + lid) * 2048 : hp * 128);
      const u32 pp = (isx ? 32768 : 49152) - 1024;
      v0 = vb; v1 = (vb ^ 64) + pp; v2 = vb + 2 * pp; v3 = (vb ^ 64) + 3 * pp;
      g0 = (lid == 3) ? 198656 : 0; g1 = isx ? 131072 : (lid == 3) ? 4096 : 196608; g2 = isx ? 262144 : (lid == 3) ? 200704 : 2048;
      vh = max(r0 - 16 + 8 * lid + lr, 0) * 2048 + (sw ^ (lid << 6));
      kstep = isx ? 128u : 64u * 6144u; d0 = lbase + (isx ? 2 + 10 * lid : 22 + 12 * (lid - 2)) * 1024;
      PIN(v0); PIN(v1); PIN(v2); PIN(v3); PIN(vh);
      asm volatile("" : "+s"(g0), "+s"(g1), "+s"(g2), "+s"(kstep), "+s"(d0));
    }
  }
  u32 ev;
  asm volatile("s_waitcnt lgkmcnt(0)" : "+s"(ka), "+s"(kb), "+s"(kord));
  {
    auto ptr = [&](int k) { return (char*)(GAS char*)(((u64)ka[2 * k + 1] << 32) | ka[2 * k]); };
    P.x = (const u16*)ptr(0); P.wqkv = (const u16*)ptr(1); P.wo = (const u16*)ptr(2); P.wr = (const u16*)ptr(3);
    P.wg = (const u16*)ptr(4); P.wu = (const u16*)ptr(5); P.wd = (const u16*)ptr(6); P.out = (u16*)ptr(7);
    P.ws = (char*)(GAS char*)kb;
  }
  rsWS = mk_rsrc(P.ws, WS_END);
  STAMP(0);
  if (wave < 4) {
    asm volatile("s_load_dword %0, %1, 0x0" : "=s"(ev) : "s"(P.ws + WS_EPOCH) : "memory");
    u32 ln = threadIdx.x & 63;
    PIN(ln);
    const u32 d = lbase + 2 * SLOT + wave * 6144, v = ln * 16;
    dma16x4(d, v, v, v, v, mk_rsrc(P.x, S_ * D_ * 2), (pan2 * 128 + cg * 8 + wave * 2) * 2048);
    dma16x2(d + 4096, v, v, mk_rsrc(P.wr, D_ * 2), 0);
  }
  SB();
  int wave_b = wave;
  asm volatile("" : "+s"(wave_b));
  if (gemm && wave_b >= 4) {
    const rsrc_t rs = mk_rsrc(isx ? P.x : P.wqkv, isx ? S_ * D_ * 2 : D_ * 3 * D_ * 2);
    auto issue_k = [&](u32 ko, u32 so) {
      dma16x4(d0 + so, v0, v1, v2, v3, rs, ko + g0);
      dma16x4(d0 + so + 4096, v0, v1, v2, v3, rs, ko + g1);
      if (isx) { dma16x2(d0 + so + 8192, v0, v1, rs, ko + g2); dma16(lbase + so + lid * 1024, vh, rs, ko); }
      else dma16x4(d0 + so + 8192, v0, v1, v2, v3, rs, ko + g2);
    };
    auto issue = [&](int s, u32 so) { issue_k(((u32)(kord >> (4 * s)) & 15u) * kstep, so); };
    LSTAMP(20);
    issue(0, 0);
    LSTAMP(21);
    helper_offsets();
    issue(1, SLOT);
    LSTAMP(22);
    if (isx) VMCNT(11); else VMCNT(12);
    LSTAMP(23);
    BAR();
    const bool helps = !isx && !(xg & 2) && hl != 3;
    if (helps) {
      constexpr int HL = (HELPN * 192 + 35) / 36;
      const int n = min(((pl * 3 + hl) * 2 + lid - 2) * HL + min(lane, HL - 1), HELPN * 192 - 1), row = n / 3;
      warm_line(lbase + L_UPD, (const char*)P.wqkv + (size_t)((u32)((HELPK >> (4 * (row >> 6))) & 15) * 64 + (row & 63)) * 6144 + (n % 3) * 2048 + (hp | 3) * 128);
    }
    issue(2, 2 * SLOT);
    u32 so = 0, sdum = 0;
    const int sn = min((pl * 4 + hl) * 4 + lid, 95) * 2;
    const u32 so0 = (sn / 3) * 6144 + (sn % 3) * 2048, so1 = ((sn + 1) / 3) * 6144 + ((sn + 1) % 3) * 2048;
    const char* sbase = (const char*)P.wqkv + (hp | 3) * 128;
    _Pragma("nounroll") for (int s = 0; s < 13; ++s) {
      if (helps && s == 0) VMCNT(13); else if (!isx || s == WARM_AT + 1 || s == WARM_AT + 2) VMCNT(12); else VMCNT(11);
      if (s == 0) LSTAMP(24);
      if (s == 1) LSTAMP(25);
      if (s == 2) LSTAMP(26);
      BAR();
      issue(s + 3, so);
      if (!(xg & 2) && (u32)(s - RS0) < RSN) {
        const u32 ko = (u32)((ROLLK >> (4 * (s - RS0))) & 15) * (64u * 6144u), a0 = ko + so0, a1 = ko + so1;
        asm volatile("s_load_dword %0, %1, %2\n\ts_load_dword %0, %1, %2 offset:64\n\ts_load_dword %0, %1, %3\n\ts_load_dword %0, %1, %3 offset:64"
                     : "+s"(sdum) : "s"(sbase), "s"(a0), "s"(a1) : "memory");
      }
      if (s == WARM_AT && isx) warm_line(lbase + L_UPD, (const char*)P.wo + (size_t)min((pan * 16 + hp) * 80 + lid * 40 + min(lane, 39), 16383) * 128);
      so = (so == 2 * SLOT) ? 0 : so + SLOT;
    }
    asm volatile("s_waitcnt lgkmcnt(0)" : "+s"(sdum));
    STAMP(2);
    helper_fill();
  }
  if (wave < 4) {
    __builtin_amdgcn_s_setprio(1);
    if (wave == 1) lds_wr(L, L_ONE + lane * 4, lane < 32 ? 0x3f803f80u : 0u);
    if (wave == 2) lds_wr(L, L_GO, 0u);
    f32x4 acc[6][6];
#pragma unroll
    for (int w = 0; w < 6; ++w)
#pragma unroll
      for (int t = (w & 1); t < 6; ++t) { acc[w][t] = f32x4{0.f, 0.f, 0.f, 0.f}; PIN(acc[w][t]); }
    f32x4 bprev, bhalo;
    u32 mc[2];
    u64 cur[4];
#pragma unroll
    for (int r = 0; r < 4; ++r) {
      const int kap = 4 * g + r, d = 16 + (wave & 1) + 2 * i - kap;
      bprev[r] = (kap - i >= 9) ? 0.f : -1e30f;
      bhalo[r] = (((d & 1) == 0) & (d <= 14) & (pan > 0)) ? 0.f : -1e30f;
      cur[r] = __ballot((u32)(i - kap) <= 7u);
      asm volatile("" : "+s"(cur[r]));
    }
    mc[0] = (((u32)(i - 4 * g) <= 7u) ? 0xffffu : 0u) | (((u32)(i - 4 * g - 1) <= 7u) ? 0xffff0000u : 0u);
    mc[1] = (((u32)(i - 4 * g - 2) <= 7u) ? 0xffffu : 0u) | (((u32)(i - 4 * g - 3) <= 7u) ? 0xffff0000u : 0u);
    u32 hxl = lbase + L_HX + (wave & 2) * 2304 + lane * 16;
    u32 obase = WS_O + (((wave & 1) * 32 + hp * 2 + (wave >> 1)) * 64 + pan * 5) * 1024 + lane * 16;
    PIN(bprev); PIN(bhalo); PIN(mc[0]); PIN(mc[1]); PIN(hxl); PIN(obase);
    SB();
    {
      const int tok = pan2 * 128 + cg * 8 + wave * 2 + (lane >> 5);
      u32x4 xv[4], wv[4];
      VMCNT(0);
      {
        LAS char* sp = L + 2 * SLOT + wave * 6144 + (lane & 31) * 16;
#pragma unroll
        for (int j = 0; j < 4; ++j) { xv[j] = *(LAS u32x4*)(sp + (lane >> 5) * 2048 + j * 512); wv[j] = *(LAS u32x4*)(sp + 4096 + j * 512); }
      }
      asm volatile("s_waitcnt lgkmcnt(0)" : "+s"(ev) :: "memory");
      const u32 e = ev;
      tag = e * 0x9E3779B1u + 0x7F4A7C15u; tag ^= tag >> 15; tag = (u32)rfl((int)(tag ^ (u32)(tag == e)));
      if (wave == 1) lds_wr(L, L_TAG, tag);
      float sx = 0.f, sxx = 0.f, sxw = 0.f, sw = 0.f;
#pragma unroll
      for (int j = 0; j < 4; ++j)
#pragma unroll
        for (int e2 = 0; e2 < 4; ++e2) {
          const u32 a = xv[j][e2], b = wv[j][e2];
          sx = dot2(a, 0x3f803f80u, sx); sxx = dot2(a, a, sxx); sxw = dot2(a, b, sxw); sw = dot2(b, 0x3f803f80u, sw);
        }
      const float mean = half_sum(sx) * (1.f / D_);
      const float var = fmaxf(half_sum(sxx) * (1.f / D_) - mean * mean, 0.f);
      const float mb = rbf(mean), sb = rbf(__builtin_amdgcn_sqrtf(rbf(rbf(var) + 1e-5f)));
      const u32 ro = ord16(pack2((half_sum(sxw) - mb * half_sum(sw)) * __builtin_amdgcn_rcpf(sb), 0.f) & 0xffffu);
      const u32 rlo = (u32)__builtin_amdgcn_readlane((int)ro, 31);
      if ((lane & 31) == 31) st_sc1_64(u32x2{pack2(mb, sb), tag}, rsWS, WS_SG + tok * 8);
      if (lane == 63) st_sc1_64(u32x2{rlo | (ro << 16), tag}, rsWS, WS_RG + (tok >> 1) * 8);
      if (!gemm) { LGKM0(); BAR(); }
    }
    STAMP(1);
    if (gemm) {
      const int h = wave >> 1, par = wave & 1;
        const int q4 = i >> 2, p4 = i & 3;
        u32 a0 = lbase + i * 128 + ((g ^ (i >> 1)) << 4);
        const u32 wsw = ((q4 >> 1) & 1) | ((g & 1) << 1);
        u32 w0 = lbase + WOFF + (8 * g + q4) * 128 + (((2 * h + par) ^ wsw) << 5) + p4 * 8;
        const u32 tb = (1 + 5 * par) * 2048;
        const u32 one = lbase + L_ONE;
        u32 pH = (i == 0) ? one : a0, pT = a0 + tb, pW0 = w0, pW1 = w0 ^ 32;
        PIN(a0); PIN(w0); PIN(pH); PIN(pT); PIN(pW0); PIN(pW1);
        Frags f0, f1;
        const u32 e1 = (i == 0) ? 0x3f803f80u : 0u;
        s16x8 e0 = as_frag(u32x4{e1, e1, e1, e1});
        PIN(e0);
        __builtin_amdgcn_s_waitcnt(0xc07f);
        BAR();
        STAMP(2); CSTAMP(14);
        load_frags<0>(f0, LP(pH), LP(pT), LP(pW0), LP(pW1));
        pH ^= 64; pT ^= 64;
        __builtin_amdgcn_s_waitcnt(0xc07f);
        u32 so = 0, trc = 0;
        auto half1 = [&]() {
          SB();
          load_frags<1>(f1, LP(pH), LP(pT), LP(pW0), LP(pW1));
          mma_range<0, 24>(acc, f0, e0);
          interleave();
          SB();
          so = (so == 2 * SLOT) ? 0 : so + SLOT;
          mma_range<24, 25>(acc, f0, e0);
          SB();
          pT = so + a0; pW0 = so + w0; PIN(pT); PIN(pW0);
          mma_range<25, 26>(acc, f0, e0);
          SB();
          pH = (i == 0) ? one : pT; pW1 = pW0 ^ 32; PIN(pH); PIN(pW1);
          mma_range<26, 27>(acc, f0, e0);
          SB();
          pT += tb; PIN(pT);
          mma_range<27, 28>(acc, f0, e0);
          SB();
        };
        auto half1b = [&](int s) {
          const u64 tnow = TICK(), rnow = RT();
          mma_range<28, 30>(acc, f0, e0);
          SB();
          __builtin_amdgcn_s_waitcnt(0xc07f);
          if (STAMPS) { trc = (lane == s) ? (u32)tnow : trc; trc = (lane == s + 16) ? (u32)rnow : trc; }
          mma_range<30, 31>(acc, f0, e0);
          SB();
          BAR();
          SB();
          mma_range<31, 33>(acc, f0, e0);
          SB();
        };
        half1();
        for (int s = 0; s < 15; ++s) {
          half1b(s);
          load_frags<0>(f0, LP(pH), LP(pT), LP(pW0), LP(pW1));
          mma_range<0, 24>(acc, f1, e0);
          interleave();
          SB();
          pH ^= 64; pT ^= 64; PIN(pH); PIN(pT);
          mma_range<24, 31>(acc, f1, e0);
          SB();
          __builtin_amdgcn_s_waitcnt(0xc07f);
          mma_range<31, 33>(acc, f1, e0);
          half1();
        }
        half1b(15);
        if (STAMPS && lane < 32) ((GAS u32*)(P.ws + WS_STAMP + (1 << 20)))[(bid * 4 + wave) * 32 + lane] = trc;
      mma_range<5, 6>(acc, f1, e0); mma_range<16, 17>(acc, f1, e0); mma_range<27, 28>(acc, f1, e0);
      SB();
      mma_range<11, 16>(acc, f1, e0);
      u32 tz = threadIdx.x;
      PIN(tz);
      const u32 i = tz & 15, g = (tz >> 4) & 3;
      u32 sqi = lbase + L_STQ + i * 4, sqg = lbase + L_STQ + g * 16;
      PIN(sqi); PIN(sqg);
      const u32 hx = hxl + par * 2304, hy = hxl + (par ^ 1) * 2304;
      u32 hxg = hx - i * 16, hyg = hy - i * 16, hxi = hx - g * 256, hyi = hy - g * 256, sqi2 = sqi + par * 320, sqg2 = sqg + par * 320;
      const u32 hqa = hx + 2048 - g * 240, hqb = hxl + (L_UPD - L_HX) - (wave & 2) * 2304;
      u32 hq = __builtin_amdgcn_inverse_ballot_w64(0x0001000100010001ull) ? hqa : hqb;
      PIN(hxg); PIN(hyg); PIN(hxi); PIN(hyi); PIN(sqi2); PIN(sqg2); PIN(hq);
#pragma unroll
      for (int k = 0; k < 5; ++k) { __builtin_amdgcn_sched_group_barrier(0x008, 1, 0); __builtin_amdgcn_sched_group_barrier(0x002, 2, 0); }
      SB();
      *(LAS f32x4*)LP(hx) = acc[2][0];
      *(LAS f32x4*)LP(hx + 1024) = acc[4][0];
      *(LAS f32x4*)LP(hq) = acc[0][0];
      mma_range<17, 22>(acc, f1, e0);
#pragma unroll
      for (int k = 0; k < 3; ++k) { __builtin_amdgcn_sched_group_barrier(0x008, 1, 0); __builtin_amdgcn_sched_group_barrier(0x200, 1, 0); }
      SB();
      mma_range<0, 3>(acc, f1, e0);
      SB();
      __builtin_amdgcn_s_waitcnt(0xc07f);
      BAR();
      SB();
      f32x4 cs[4];
      cs[2] = *(LAS f32x4*)LP(hxg); cs[3] = *(LAS f32x4*)LP(hyg);
      cs[0] = *(LAS f32x4*)LP(hxg + 2048 - g * 240); cs[1] = *(LAS f32x4*)LP(hyg + 2048 - g * 240);
      float m[6], rs[6];
#pragma unroll
      for (int t = 0; t < 6; ++t) { m[t] = *(LAS float*)LP((t ? sqi2 : sqi) + t * 64); rs[t] = *(LAS float*)LP((t ? sqi2 : sqi) + t * 64 + 704); }
      acc[3][0] = *(LAS f32x4*)LP(hy);
      const float cv[2] = {*(LAS float*)LP(hxi + 1024), *(LAS float*)LP(hyi + 1024)};
      acc[5][0] = *(LAS f32x4*)LP(hy + 1024);
      mma_range<3, 5>(acc, f1, e0); mma_range<6, 11>(acc, f1, e0);
#pragma unroll
      for (int k = 0; k < 7; ++k) { __builtin_amdgcn_sched_group_barrier(0x008, 1, 0); __builtin_amdgcn_sched_group_barrier(0x100, 2, 0); }
      SB();
      STAMP(3); CSTAMP(15);
      const float QS = 0.17677669529663687f * 1.4426950408889634f;
      const f32x4 z4 = {0.f, 0.f, 0.f, 0.f};
      f32x4 s0[5], s1[5], m4[6], r4[6];
      {
        s16x8 kf[6];
#pragma unroll
        for (int t = 5; t >= 0; --t) {
          const f32x4 k0 = (acc[2][t] + m[t] * cs[2]) * rs[t], k1 = (acc[3][t] + m[t] * cs[3]) * rs[t];
          kf[t] = as_frag(u32x4{pack2(k0[0], k0[1]), pack2(k0[2], k0[3]), pack2(k1[0], k1[1]), pack2(k1[2], k1[3])});
        }
        mma_range<22, 27>(acc, f1, e0); mma_range<28, 33>(acc, f1, e0);
#pragma unroll
        for (int k = 0; k < 10; ++k) { __builtin_amdgcn_sched_group_barrier(0x008, 1, 0); __builtin_amdgcn_sched_group_barrier(0x002, 3, 0); }
        SB();
#pragma unroll
        for (int t = 0; t < 6; ++t) { m4[t] = *(LAS f32x4*)LP((t ? sqg2 : sqg) + t * 64); r4[t] = *(LAS f32x4*)LP((t ? sqg2 : sqg) + t * 64 + 704); }
#pragma unroll
        for (int t = 0; t < 5; ++t) {
          const f32x4 q0 = acc[0][t + 1] + m[t + 1] * cs[0], q1 = acc[1][t + 1] + m[t + 1] * cs[1];
          const s16x8 qf = as_frag(u32x4{pack2(q0[0], q0[1]), pack2(q0[2], q0[3]), pack2(q1[0], q1[1]), pack2(q1[2], q1[3])});
          s0[t] = mfma(kf[t], qf, t == 0 ? bhalo : bprev); s1[t] = mfma(kf[t + 1], qf, z4);
        }
      }
      SB();
      u32x16 vp[2];
#pragma unroll
      for (int t = 0; t < 6; ++t) {
#pragma unroll
        for (int c = 0; c < 2; ++c) {
          const f32x4 v = (acc[4 + c][t] + m4[t] * cv[c]) * r4[t];
          vp[c][2 * t] = pack2(v[0], v[1]); vp[c][2 * t + 1] = pack2(v[2], v[3]);
        }
      }
      float l[5][4], mx[5], rq[5];
#pragma unroll
      for (int t = 0; t < 5; ++t) {
#pragma unroll
        for (int r = 0; r < 4; ++r) l[t][r] = __builtin_amdgcn_inverse_ballot_w64(cur[r]) ? s1[t][r] : s0[t][r];
        mx[t] = vmax3(vmax(l[t][0], l[t][1]), l[t][2], l[t][3]);
        rq[t] = rs[t + 1] * QS;
      }
#pragma unroll
      for (int t = 0; t < 5; ++t) mx[t] = max16(mx[t]);
#pragma unroll
      for (int t = 0; t < 5; ++t) mx[t] = -max32(mx[t]) * rq[t];
      const s16x8 ones = as_frag(u32x4{0x3f803f80u, 0x3f803f80u, 0x3f803f80u, 0x3f803f80u});
      f32x4 sm[5], o0[5], o1[5];
#pragma unroll
      for (int t = 0; t < 5; ++t) {
#pragma unroll
        for (int r = 0; r < 4; ++r) l[t][r] = __builtin_amdgcn_exp2f(__builtin_fmaf(l[t][r], rq[t], mx[t]));
        const u32 p01 = pack2(l[t][0], l[t][1]), p23 = pack2(l[t][2], l[t][3]);
        const s16x8 pp = as_frag(u32x4{p01 & ~mc[0], p23 & ~mc[1], p01 & mc[0], p23 & mc[1]});
        sm[t] = mfma(ones, pp, z4);
        o0[t] = mfma(as_frag(u32x4{vp[0][2 * t], vp[0][2 * t + 1], vp[0][2 * t + 2], vp[0][2 * t + 3]}), pp, z4);
        o1[t] = mfma(as_frag(u32x4{vp[1][2 * t], vp[1][2 * t + 1], vp[1][2 * t + 2], vp[1][2 * t + 3]}), pp, z4);
      }
#pragma unroll
      for (int t = 0; t < 5; ++t) {
        const float inv = __builtin_amdgcn_rcpf(sm[t][0]);
        o0[t] *= inv; o1[t] *= inv;
        st_sc1_128(u32x4{pack2(o0[t][0], o0[t][1]), pack2(o0[t][2], o0[t][3]), pack2(o1[t][0], o1[t][1]), pack2(o1[t][2], o1[t][3])}, rsWS,
                   (t == 4) ? obase + ((pan == 12) ? 0x40000000u : 4096u) : obase + t * 1024);
      }
      STAMP(8);
    }
  }
  if (!gemm) {
  if (dwn) {
    const rsrc_t rsD = mk_rsrc(P.wd, F_ * D_ * 2);
    const int k0 = (du >> 1) * 14 + (wave >> 2) * 7;
    const u32 vd = ((k0 * 32 + 8 * g) * D_ + (du & 1) * 512 + (wave & 3) * 128 + 8 * i) * 2;
    constexpr u32 D_W = 24 * 4096, D_H = D_W + 8 * 4352;
    u32x4 wb[56];
#pragma unroll
    for (int j = 0; j < 8; ++j) wb[j] = ld_nt_128(rsD, vd, j * (D_ * 2));
    SB();
    if (wave >= 4) { BAR(); tag = (u32)rfl((int)lds_rd(L, L_TAG)); }
#pragma unroll
    for (int j = 8; j < 48; ++j) wb[j] = ld_nt_128(rsD, vd, ((j >> 3) * 32 + (j & 7)) * (D_ * 2));
    SB();
    transpose_grp(wb);
#pragma unroll
    for (int e = 0; e < 4; ++e) *(LAS u32x4*)(L + D_W + wave * 4352 + e * 1024 + lane * 16) = wb[e];
    SB();
#pragma unroll
    for (int q = 1; q < 6; ++q) transpose_grp(wb + 8 * q);
#pragma unroll
    for (int j = 48; j < 56; ++j) wb[j] = ld_nt_128(rsD, vd, (192 + (j & 7)) * (D_ * 2));
    SB();
    __builtin_amdgcn_s_setprio(0);
    if (wave >= 4) { helper_offsets(); helper_fill(); }
    transpose_grp(wb + 48);
    VMCNT(0);
    STAMP(12);
    BAR();
    STAMP(11);
    {
      wait_until(tstart, T_H);
      const int qa = 2 * (wave & 3), qb = min(qa + 1, 6);
      u32x4 va, vb;
      _Pragma("nounroll") for (int sp = 0; sp < SPIN_CAP; ++sp) {
        va = ld_sc1_128(rsWS, lane * 16, WS_H + (k0 + qa) * 1024);
        vb = ld_sc1_128(rsWS, lane * 16, WS_H + (k0 + qb) * 1024);
        if (__all(va.y == tag && va.w == tag && vb.y == tag && vb.w == tag)) break;
        __builtin_amdgcn_s_sleep(1);
      }
      *(LAS u32x2*)(L + D_H + ((wave >> 2) * 7 + qa) * 512 + (lane & 31) * 16 + (lane >> 5) * 8) = u32x2{va.x, va.z};
      *(LAS u32x2*)(L + D_H + ((wave >> 2) * 7 + qb) * 512 + (lane & 31) * 16 + (lane >> 5) * 8) = u32x2{vb.x, vb.z};
      LGKM0();
      lds_wr(L, L_HR + wave * 4, 1);
      lds_wait_set(L, L_HR + (wave & 4) * 4 + (lane & 3) * 4);
    }
    STAMP(2);
    f32x4 ga[8];
#pragma unroll
    for (int e = 0; e < 8; ++e) ga[e] = f32x4{0.f, 0.f, 0.f, 0.f};
#pragma unroll
    for (int q = 6; q >= 0; --q) {
      const s16x8 hf = *(LAS s16x8*)(L + D_H + ((wave >> 2) * 7 + q) * 512 + (g * 8 + (i & 7)) * 16);
      mma_frags8(ga, wb + q * 8, hf);
      if (q == 5) {
#pragma unroll
        for (int e = 0; e < 4; ++e) wb[e] = *(LAS u32x4*)(L + D_W + wave * 4352 + e * 1024 + lane * 16);
      }
      if (q >= 5) SB();
    }
    STAMP(4);
    if (i < 8) {
#pragma unroll
      for (int r = 0; r < 4; ++r) {
        const u32 o = D_W + wave * 4352 + (i * 132 + 8 * (4 * g + r)) * 4;
        *(LAS f32x4*)(L + o) = f32x4{ga[0][r], ga[1][r], ga[2][r], ga[3][r]};
        *(LAS f32x4*)(L + o + 16) = f32x4{ga[4][r], ga[5][r], ga[6][r], ga[7][r]};
      }
    }
    LGKM0();
    lds_wr(L, L_PW + 4 * wave, 1);
    STAMP(18);
    if (wave >= 4) {
      lds_wait_set(L, L_PW + (wave - 4) * 4);
      STAMP(22);
#pragma unroll
      for (int tk = 0; tk < 8; ++tk) {
        const f32x2 y = *(LAS f32x2*)(L + D_W + (wave - 4) * 4352 + (tk * 132 + 2 * lane) * 4) + *(LAS f32x2*)(L + D_W + wave * 4352 + (tk * 132 + 2 * lane) * 4);
        st_sc1_128(u32x4{__float_as_uint(y.x), tag, __float_as_uint(y.y), tag}, rsWS, WS_YS + (du >> 1) * 65536 + tk * 8192 + (du & 1) * 4096 + (wave - 4) * 1024 + lane * 16);
      }
      LGKM0();
      STAMP(21);
      lds_wr(L, L_HS + 4 * (wave - 4), 1);
      lds_wait_set(L, L_HS + (lane & 3) * 4);
      u32 ln = threadIdx.x & 63;
      PIN(ln);
      vO = (ln >> 3) * 2048 + (((ln & 7) ^ (((ln >> 4) & 3) << 1)) << 4) + cg * 128;
      wo_grp(20 + wave); wo_grp(24 + wave);
      STAMP(15);
      VMCNT(0);
      STAMP(16);
      lds_wr(L, L_HS + 4 * (wave - 4), 2);
    }
    STAMP(8);
  } else {
    const int lr = lane >> 3, lc = lane & 7;
    const int fv = fu - (fu > 2);
    const bool two = fu != 2 && fv < 24;
    const int la = (fu == 2) ? 50 : (fv >= 24) ? (fv - 24) * 8 + 3 : (fv >= 21) ? (fv - 21) * 16 + 2 : (fv / 3) * 8 + ((fv % 3) ? 2 + 2 * (fv % 3) : 0);
    const int lb = la + ((fv >= 21) ? 8 : 1);
    const rsrc_t rsG = mk_rsrc(P.wg, D_ * F_ * 2), rsU = mk_rsrc(P.wu, D_ * F_ * 2), rsX = mk_rsrc(P.x, S_ * D_ * 2);
    const u32 vg = (i < 8 || two) ? ((wave * 128 + 8 * g) * F_ + ((i < 8) ? la : lb) * 64 + 8 * (i & 7)) * 2 : 0x7f000000u;
    u32x4 wb[8 * GWIN];
    auto ldgrp = [&](int q) {
#pragma unroll
      for (int j = 0; j < 8; ++j) wb[(q % GWIN) * 8 + j] = ld_nt_128((q & 1) ? rsU : rsG, vg, ((q >> 1) * 32 + j) * (F_ * 2));
      SB();
    };
    __builtin_amdgcn_s_setprio(0);
    ldgrp(0);
    ldgrp(1);
    if (wave >= 4) { BAR(); tag = (u32)rfl((int)lds_rd(L, L_TAG)); }
    if (fu == 2) {
      *(LAS u32x4*)(L + 32 + threadIdx.x * 16) = u32x4{0u, 0u, 0u, 0u};
      if (threadIdx.x < 4) *(LAS u32x4*)(L + 32 + 8192 + threadIdx.x * 16) = u32x4{0u, 0u, 0u, 0u};
      wait_until(tstart, T_GATHER);
      u32x4 v;
      _Pragma("nounroll") for (int sp = 0; sp < SPIN_CAP; ++sp) {
        v = ld_sc1_128(rsWS, lane * 16, WS_RG + wave * 1024);
        if (__all(v.y == tag && v.w == tag)) break;
        __builtin_amdgcn_s_sleep(2);
      }
      const u32 ib = 2047 - 256 * wave - 4 * lane;
      const u32 key[4] = {(v.x << 16) | ib, (v.x & 0xffff0000u) | (ib - 1), (v.z << 16) | (ib - 2), (v.z & 0xffff0000u) | (ib - 3)};
      lds_wr(L, wave * 4, wave_max_u32(max(max(key[0], key[1]), max(key[2], key[3]))));
      LGKM0();
      BAR();
      STAMP(9);
      const u32x4 m0 = *(LAS u32x4*)L, m1 = *(LAS u32x4*)(L + 16);
      const u32 thr = min(min(min(m0.x, m0.y), min(m0.z, m0.w)), min(min(m1.x, m1.y), min(m1.z, m1.w)));
      const u32 c0 = key[0] >= thr, c1 = key[1] >= thr, c2 = key[2] >= thr, c3 = key[3] >= thr;
      if (c0 | c1 | c2 | c3) {
        const u32 p = 64 + 4 * __hip_atomic_fetch_add((LAS u32*)(L + 32), c0 + c1 + c2 + c3, __ATOMIC_RELAXED, __HIP_MEMORY_SCOPE_WORKGROUP);
        lds_wr(L, c0 ? p : 36, key[0]); lds_wr(L, c1 ? p + 4 * c0 : 36, key[1]);
        lds_wr(L, c2 ? p + 4 * (c0 + c1) : 36, key[2]); lds_wr(L, c3 ? p + 4 * (c0 + c1 + c2) : 36, key[3]);
      }
      LGKM0();
      BAR();
      const int n = rfl((int)lds_rd(L, 32));
      _Pragma("nounroll") for (int b = threadIdx.x; b < n; b += 512) {
        const u32 my = lds_rd(L, 64 + 4 * b);
        int rank = 0;
        _Pragma("nounroll") for (int t = 0; t < n; t += 8) {
          const u32x4 o = *(LAS u32x4*)(L + 64 + 4 * t), q = *(LAS u32x4*)(L + 80 + 4 * t);
          rank += (o.x > my) + (o.y > my) + (o.z > my) + (o.w > my) + (q.x > my) + (q.y > my) + (q.z > my) + (q.w > my);
        }
        if (rank < 8) st_sc1_64(u32x2{my ^ 0x7ffu, tag}, rsWS, WS_SEL + rank * 8);
      }
      if (threadIdx.x == 0) st_sc1_32(tag, rsWS, WS_EPOCH);
      STAMP(10);
      LGKM0();
      BAR();
    }
    constexpr u32 F_XS = 81920;
    ldgrp(2);
    if (wave == 0) {
      wait_until(tstart, T_SEL);
      u32x2 sk;
      _Pragma("nounroll") for (int sp = 0; sp < SPIN_CAP; ++sp) {
        sk = ld_sc1_64(rsWS, lr * 8, WS_SEL);
        if (__all(sk.y == tag)) break;
        __builtin_amdgcn_s_sleep(4);
      }
      STAMP(2);
      u32x4 xr[16];
#pragma unroll
      for (int j = 0; j < 16; ++j) xr[j] = __builtin_amdgcn_raw_buffer_load_b128(rsX, (sk.x & 0x7ffu) * 2048 + lc * 16, j * 128, 0);
      u32x2 ms;
      _Pragma("nounroll") for (int sp = 0; sp < SPIN_CAP; ++sp) {
        ms = ld_sc1_64(rsWS, (sk.x & 0x7ffu) * 8, WS_SG);
        if (__all(ms.y == tag)) break;
      }
#pragma unroll
      for (int j = 0; j < 16; ++j) *(LAS u32x4*)(L + F_XS + lr * 2064 + j * 128 + lc * 16) = xr[j];
      *(LAS u32*)(L + F_XS + 8 * 2064 + lr * 4) = ms.x;
      LGKM0();
      lds_wr(L, L_HR, 1);
    }
#pragma unroll
    for (int q = 3; q < GWIN; ++q) ldgrp(q);
#pragma unroll
    for (int q = 0; q < GPRE; ++q) transpose_grp(wb + 8 * q);
    lds_wait_set(L, L_HR);
    f32x4 ga[16];
#pragma unroll
    for (int e = 0; e < 16; ++e) ga[e] = f32x4{0.f, 0.f, 0.f, 0.f};
#pragma unroll
    for (int q = 0; q < 8; ++q) {
      const s16x8 xf = *(LAS s16x8*)(L + ((i < 8) ? F_XS + i * 2064 + (wave * 128 + (q >> 1) * 32 + 8 * g) * 2 : L_ONE));
      if (q < GPRE) mma_frags8(ga + 8 * (q & 1), wb + q * 8, xf); else mma_cols(ga + 8 * (q & 1), wb + (q % GWIN) * 8, xf);
      SB();
      if (q + GWIN < 8) ldgrp(q + GWIN);
    }
    STAMP(4);
    if (i < 9) {
#pragma unroll
      for (int m = 0; m < 2; ++m)
#pragma unroll
        for (int r = 0; r < 4; ++r) {
          const u32 o = wave * 9504 + ((m * 9 + i) * 132 + 8 * (4 * g + r)) * 4;
          *(LAS f32x4*)(L + o) = f32x4{ga[8 * m][r], ga[8 * m + 1][r], ga[8 * m + 2][r], ga[8 * m + 3][r]};
          *(LAS f32x4*)(L + o + 16) = f32x4{ga[8 * m + 4][r], ga[8 * m + 5][r], ga[8 * m + 6][r], ga[8 * m + 7][r]};
          SB();
        }
    }
    LGKM0();
    BAR();
    {
      const int fl = (wave >> 1) * 32 + 8 * g + 4 * (wave & 1) + 2 * (lane & 1), tk = (lane >> 1) & 7;
      f32x2 gs = {0.f, 0.f}, us = {0.f, 0.f}, gc = {0.f, 0.f}, uc = {0.f, 0.f};
#pragma unroll
      for (int w = 0; w < 8; ++w) {
        gs += *(LAS f32x2*)(L + w * 9504 + (tk * 132 + fl) * 4);
        gc += *(LAS f32x2*)(L + w * 9504 + (8 * 132 + fl) * 4);
        us += *(LAS f32x2*)(L + w * 9504 + 4752 + (tk * 132 + fl) * 4);
        uc += *(LAS f32x2*)(L + w * 9504 + 4752 + (8 * 132 + fl) * 4);
      }
      const u32 ms = *(LAS u32*)(L + F_XS + 8 * 2064 + tk * 4);
      const float m = bf_lo(ms), rs = __builtin_amdgcn_rcpf(bf_hi(ms));
      gs = (gs - m * gc) * rs; us = (us - m * uc) * rs;
      const float g0 = rbf(gs.x), g1 = rbf(gs.y);
      const u32 hv = pack2(rbf(g0 * sigm(g0)) * rbf(us.x), rbf(g1 * sigm(g1)) * rbf(us.y));
      if (wave < 4 || two) st_sc1_64(u32x2{hv, tag}, rsWS, WS_H + ((wave < 4) ? la : lb) * 2048 + (wave & 3) * 512 + lane * 8);
    }
    LGKM0();
    BAR();
    STAMP(8);
    if (wave >= 4) { helper_offsets(); helper_fill(); }
  }
  }

  if (wave >= 4) {
    const int lid = wave - 4;
    if (!dwn) {
      VMCNT(0);
      STAMP(3);
      BAR();
    }
    if (lid >= 2) {
      const u32 sk = lds_rd(L, L_SEL + 4 * (lane & 7));
      _Pragma("nounroll") for (int c = lid - 2; c < 8; c += 2) {
        const u32 tk = (u32)__builtin_amdgcn_readlane((int)sk, c) & 0x7ffu;
        if ((int)(tk >> 7) != pan2) continue;
        float y0, y1;
        _Pragma("nounroll") for (int sp = 0; sp < SPIN_CAP; ++sp) {
          u32 bad = 0;
          y0 = 0.f; y1 = 0.f;
#pragma unroll
          for (int j = 0; j < 4; ++j) {
            const u32x4 v = ld_sc1_128(rsWS, (lane >> 5) * 65536 + (lane & 31) * 16, WS_YS + j * 131072 + c * 8192 + cg * 512);
            bad |= (v.y ^ tag) | (v.w ^ tag);
            y0 += __uint_as_float(v.x); y1 += __uint_as_float(v.z);
          }
          if (__all(bad == 0)) break;
          __builtin_amdgcn_s_sleep(8);
        }
        y0 = sum32(y0); y1 = sum32(y1);
        if (lane < 32) *(LAS f32x2*)(L + L_UPD + (c * 64 + lane * 2) * 4) = f32x2{y0, y1};
      }
      LGKM0();
      lds_wr(L, L_UPDF + 4 * (lid - 2), 1);
      STAMP(7);
    }
    return;
  }

  {
    const bool pol = wave == 0;
    const u32 fo = WS_OF + (((pan2 * 4 + 3 * (lane >> 5)) / 5) * 16 + (lane & 15)) * 128;
    u32 fv = ~tag;
    if (dwn && pol) fv = ld_sc1_32(rsWS, fo, 0);
    SB();
    if (!gemm && !dwn) BAR();
    STAMP(19);
    oc_init();
    SB();
    STAMP(20);
    if (gemm) {
      VMCNT(0);
      if (lane == 0 && __hip_atomic_fetch_add((LAS u32*)(L + L_ARR), 1u, __ATOMIC_RELAXED, __HIP_MEMORY_SCOPE_WORKGROUP) == 3)
        st_sc1_32(tag, rsWS, WS_OF + (pan * 16 + hp) * 128);
      STAMP(4);
      SB();
    }
    const int par2 = wave >> 1, t2 = (wave & 1) * 2;
    const int q4 = i >> 2, p4 = i & 3;
    const u32 wb = lbase + (4 * g + q4) * 128 + p4 * 8 + par2 * 2048, wsw = (2 * g + (q4 >> 1)) & 3;
    u32 wa[4], wc[4];
#pragma unroll
    for (int c = 0; c < 4; ++c) { wa[c] = wb + ((c ^ wsw) << 5); wc[c] = wa[c] ^ 2048; PIN(wa[c]); PIN(wc[c]); }
    const int T0 = pan2 * 4 + t2;
    if (gemm) BAR();
    s16x8 wf[3][4];
#pragma unroll
    for (int k = 0; k < 2; ++k)
#pragma unroll
      for (int c = 0; c < 4; ++c) wf[k][c] = tr_frag(LP(wa[c]), LP(wc[c]), k * 4096, k * 4096);
    const u32 sk = lds_rd(L, L_SEL + 4 * (lane & 7));
    __builtin_amdgcn_s_waitcnt(0xc07f);
    SB();
    STAMP(14);
    if (!gemm) wait_until(tstart, T_O);
    if (pol) {
      _Pragma("nounroll") for (int sp = 0; sp < SPIN_CAP; ++sp) {
        if (__all(fv == tag)) break;
        if (sp) __builtin_amdgcn_s_sleep(2);
        fv = ld_sc1_32(rsWS, fo, 0);
      }
      lds_wr(L, L_GO, 1);
    } else {
      _Pragma("nounroll") for (int sp = 0; sp < SPIN_CAP; ++sp) if (rfl((int)lds_rd(L, L_GO))) break;
    }
    CBAR();
    STAMP(5);
    const u32 abase = WS_O + (par2 * 32 * 64 + T0) * 1024;
    u32x4 af[OWIN][2];
#pragma unroll
    for (int hh = 0; hh < OWIN; ++hh) {
#pragma unroll
      for (int tt = 0; tt < 2; ++tt) af[hh][tt] = ld_sc1_128(rsWS, lane * 16 + tt * 1024, abase + ((hh + hrot) & 31) * 65536);
      SB();
    }
#pragma unroll
    for (int hh = 0; hh < 32; ++hh) {
      if (hh == 21 && dwn) {
        _Pragma("nounroll") for (int sp = 0; sp < SPIN_CAP; ++sp) if (__all(lds_rd(L, L_HS + (lane & 3) * 4) == 2u)) break;
        CBAR(); SB();
      }
      if (hh == 14) {
#pragma unroll
        for (int c = 0; c < 4; ++c) { wa[c] += 65536; wc[c] += 65536; PIN(wa[c]); PIN(wc[c]); }
        SB();
      }
      if (hh == 31) __builtin_amdgcn_s_waitcnt(0xc07f); else __builtin_amdgcn_s_waitcnt(0xc87f);
      SB();
      if (hh + 2 < 32) {
#pragma unroll
        for (int c = 0; c < 4; ++c) wf[(hh + 2) % 3][c] = tr_frag(LP(wa[c]), LP(wc[c]), ((hh + 2) & 15) * 4096, ((hh + 2) & 15) * 4096);
      }
#pragma unroll
      for (int tt = 0; tt < 2; ++tt) {
#pragma unroll
        for (int c = 0; c < 4; ++c) oc[tt][c] = mfma(wf[hh % 3][c], as_frag(af[hh % OWIN][tt]), oc[tt][c]);
        if (hh + OWIN < 32) af[hh % OWIN][tt] = ld_sc1_128(rsWS, lane * 16 + tt * 1024, abase + ((hh + OWIN + hrot) & 31) * 65536);
      }
#pragma unroll
      for (int k = 0; k < 2; ++k) {
        __builtin_amdgcn_sched_group_barrier(0x008, 1, 0); __builtin_amdgcn_sched_group_barrier(0x100, 2, 0);
        __builtin_amdgcn_sched_group_barrier(0x008, 1, 0); __builtin_amdgcn_sched_group_barrier(0x100, 1, 0);
        __builtin_amdgcn_sched_group_barrier(0x008, 1, 0); __builtin_amdgcn_sched_group_barrier(0x100, 1, 0);
        __builtin_amdgcn_sched_group_barrier(0x008, 1, 0); __builtin_amdgcn_sched_group_barrier(0x020, 1, 0);
      }
      SB();
    }
    STAMP(6);
    {
      const u32 stl = (sk & 0x7ffu) - (u32)(pan2 * 128 + par2 + 32 * t2);
      u64 hit = __ballot(stl < 64u && (stl & 1u) == 0u) & 0xffull;
      if (hit) {
        lds_wait_set(L, L_UPDF + (lane & 1) * 4);
        while (hit) {
          const int c = __builtin_ctzll(hit);
          hit &= hit - 1;
          const u32 tl = (u32)__builtin_amdgcn_readlane((int)stl, c), ob = (u32)__builtin_amdgcn_readlane((int)sk, c) >> 16;
          const float sg = rbf(sigm(bf_lo((ob & 0x8000u) ? (ob & 0x7fffu) : (~ob & 0xffffu))));
          const float f0 = ((tl >> 1) == (u32)i) ? sg : 0.f, f1 = ((tl >> 1) == (u32)i + 16u) ? sg : 0.f;
#pragma unroll
          for (int cc = 0; cc < 4; ++cc) {
            const f32x4 u = *(LAS f32x4*)(L + L_UPD + (c * 64 + 16 * cc + 4 * g) * 4);
            oc[0][cc] += u * f0; oc[1][cc] += u * f1;
          }
        }
      }
    }
    const rsrc_t rsO = mk_rsrc(P.out, S_ * D_ * 2);
    const u32 xo = ((pan2 * 128 + par2 + 32 * t2 + 2 * i) * D_ + cg * 64 + (g & 1) * 16 + (g >> 1) * 8) * 2;
#pragma unroll
    for (int tt = 0; tt < 2; ++tt)
#pragma unroll
      for (int cp = 0; cp < 2; ++cp) {
        const u32 a0 = pack2(oc[tt][2 * cp][0], oc[tt][2 * cp][1]), a1 = pack2(oc[tt][2 * cp][2], oc[tt][2 * cp][3]);
        const u32 b0 = pack2(oc[tt][2 * cp + 1][0], oc[tt][2 * cp + 1][1]), b1 = pack2(oc[tt][2 * cp + 1][2], oc[tt][2 * cp + 1][3]);
        const auto e0 = __builtin_amdgcn_permlane16_swap(a0, b0, false, false);
        const auto e1 = __builtin_amdgcn_permlane16_swap(a1, b1, false, false);
        st_sc1_128(u32x4{e0[0], e1[0], e0[1], e1[1]}, rsO, xo + tt * 65536 + cp * 64);
      }
    STAMP(7);
  }
}

static u64 k_order(int odd, int pl, int hl) {
  const int np = 7 - odd;
  int slot[16], used[16];
  for (int k = 0; k < 16; ++k) { slot[k] = -1; used[k] = 0; }
  for (int k = 2; k < 16; ++k) {
    const int h0 = k & 3, p0 = (k * 3) % np;
    int want = -1;
    if (hl == h0 && pl % np == p0) want = k - 2; else if (hl == h0 || pl % np == p0) want = k - 1;
    if (want >= 0) {
      if (want < 1) want = 1;
      while (want > 1 && used[want]) --want;
      if (!used[want]) { slot[k] = want; used[want] = 1; }
    }
  }
  int s = 0;
  for (int k = 0; k < 16; ++k) if (slot[k] < 0) { while (used[s]) ++s; slot[k] = s; used[s] = 1; }
  u64 r = 0;
  for (int k = 0; k < 16; ++k) r |= (u64)k << (4 * slot[k]);
  return r;
}

extern "C" void kernel_launch(void* const* d_in, const int* in_sizes, int n_in, void* d_out, int out_size, void* d_ws, size_t ws_size,
                              hipStream_t stream) {
  Args A;
  Params& P = A.p;
  P.x = (const u16*)d_in[0]; P.wqkv = (const u16*)d_in[1]; P.wo = (const u16*)d_in[2]; P.wr = (const u16*)d_in[4];
  P.wg = (const u16*)d_in[5]; P.wu = (const u16*)d_in[6]; P.wd = (const u16*)d_in[7];
  P.out = (u16*)d_out; P.ws = (char*)d_ws;
  for (int e = 0; e < 64; ++e) A.kt[e] = k_order(e >> 5, (e >> 2) & 7, e & 3);
  (void)hipFuncSetAttribute((const void*)fused_kernel, hipFuncAttributeMaxDynamicSharedMemorySize, L_END + 64);
  hipLaunchKernelGGL(fused_kernel, dim3(256), dim3(512), L_END + 64, stream, A);
}
```

```cpp
#include <hip/hip_runtime.h>
#include <stdint.h>

typedef unsigned short u16;
typedef unsigned u32;
typedef unsigned long long u64;
typedef __attribute__((ext_vector_type(4))) short s16x4;
typedef __attribute__((ext_vector_type(8))) short s16x8;
typedef __attribute__((ext_vector_type(2))) float f32x2;
typedef __attribute__((ext_vector_type(4))) float f32x4;
typedef __attribute__((ext_vector_type(4))) unsigned u32x4;
typedef __attribute__((ext_vector_type(2))) unsigned u32x2;
typedef __attribute__((ext_vector_type(16))) unsigned u32x16;
typedef __attribute__((ext_vector_type(2))) __bf16 bf16x2;
typedef __amdgpu_buffer_rsrc_t rsrc_t;
#define LAS __attribute__((address_space(3)))
#define GAS __attribute__((address_space(1)))
#define DEV __device__ __forceinline__

#define STAMPS 0
constexpr int OWIN = 6;
constexpr int GWIN = 5, GPRE = 3;
constexpr int T_GATHER = 350, T_SEL = 580, T_H = 1100, T_O = 1300;
constexpr int WARM_AT = 10;
constexpr u64 HELPK = 0x765432ull, ROLLK = 0xfedcba98ull;
constexpr int HELPN = 6, RS0 = 1, RSN = 8;
constexpr int CODE_LINES = 326;

constexpr int S_ = 2048, D_ = 1024, F_ = 3584;
constexpr u32 WS_EPOCH = 0;
constexpr u32 WS_SEL = 4096;
constexpr u32 WS_RG = 16384;
constexpr u32 WS_SG = 32768;
constexpr u32 WS_OF = 65536;
constexpr u32 WS_H = WS_OF + 131072;
constexpr u32 WS_YS = WS_H + 131072;
constexpr u32 WS_O = WS_YS + 524288;
constexpr u32 WS_END = WS_O + S_ * D_ * 2;
constexpr u32 WS_STAMP = 16u << 20;
constexpr u32 WOFF = 22528, SLOT = 47104;
constexpr u32 L_HX = 131072;
constexpr u32 L_XT = 3 * SLOT;
constexpr u32 L_STQ = L_XT + 16384;
constexpr u32 L_STO = L_STQ + 1408;
constexpr u32 L_UPD = L_STO + 1024;
constexpr u32 L_ONE = L_UPD + 2048;
constexpr u32 L_CTL = L_ONE + 128;
constexpr u32 L_TAG = L_CTL, L_ARR = L_CTL + 4, L_UPDF = L_CTL + 8  , L_HS = L_CTL + 16  , L_SEL = L_CTL + 32  ;
constexpr u32 L_PW = L_CTL + 64, L_HR = L_CTL + 96;
constexpr u32 L_GO = L_CTL + 128;
constexpr u32 L_END = L_CTL + 192;
constexpr int SPIN_CAP = 1 << 22;
static_assert(L_END <= 163840 && L_ONE % 128 == 0, "LDS");

struct Params {
  const u16 *x, *wqkv, *wo, *wr, *wg, *wu, *wd;
  u16* out;
  char* ws;
};
struct Args {
  Params p;
  u64 kt[64];
};

DEV float bf_lo(u32 v) { return __uint_as_float(v << 16); }
DEV float bf_hi(u32 v) { return __uint_as_float(v & 0xffff0000u); }
DEV u32 pack2(float a, float b) { bf16x2 p = {(__bf16)a, (__bf16)b}; return __builtin_bit_cast(u32, p); }
DEV float rbf(float a) { return bf_lo(pack2(a, a)); }
DEV float dot2(u32 a, u32 b, float c) { return __builtin_amdgcn_fdot2_f32_bf16(__builtin_bit_cast(bf16x2, a), __builtin_bit_cast(bf16x2, b), c, false); }
DEV s16x8 as_frag(u32x4 v) { return __builtin_bit_cast(s16x8, v); }
DEV f32x4 mfma(s16x8 a, s16x8 b, f32x4 c) { return __builtin_amdgcn_mfma_f32_16x16x32_bf16(a, b, c, 0, 0, 0); }
DEV int rfl(int v) { return __builtin_amdgcn_readfirstlane(v); }
template <int CTRL, int RM, bool BC> DEV float dppf(float v) { return __int_as_float(__builtin_amdgcn_update_dpp(0, __float_as_int(v), CTRL, RM, 0xf, BC)); }
template <int CTRL, int RM, bool BC> DEV u32 dppu(u32 v) { return (u32)__builtin_amdgcn_update_dpp(0, (int)v, CTRL, RM, 0xf, BC); }
DEV float half_sum(float v) {
  v += dppf<0x111, 0xf, true>(v); v += dppf<0x112, 0xf, true>(v); v += dppf<0x114, 0xf, true>(v); v += dppf<0x118, 0xf, true>(v);
  v += dppf<0x142, 0xa, false>(v);
  return v;
}
DEV u32 wave_max_u32(u32 v) {
  v = max(v, dppu<0x111, 0xf, true>(v)); v = max(v, dppu<0x112, 0xf, true>(v)); v = max(v, dppu<0x114, 0xf, true>(v)); v = max(v, dppu<0x118, 0xf, true>(v));
  v = max(v, dppu<0x142, 0xa, false>(v)); v = max(v, dppu<0x143, 0xc, false>(v));
  return (u32)__builtin_amdgcn_readlane((int)v, 63);
}
DEV float vmax(float a, float b) { return __builtin_elementwise_maximum(a, b); }
DEV float vmax3(float a, float b, float c) { return vmax(vmax(a, b), c); }
DEV float max16(float v) { u32 a = __float_as_uint(v); auto r = __builtin_amdgcn_permlane16_swap(a, a, false, false); u32 x = r[0], y = r[1]; return vmax(__uint_as_float(x), __uint_as_float(y)); }
DEV float max32(float v) { u32 a = __float_as_uint(v); auto r = __builtin_amdgcn_permlane32_swap(a, a, false, false); u32 x = r[0], y = r[1]; return vmax(__uint_as_float(x), __uint_as_float(y)); }
DEV float sum32(float v) { u32 a = __float_as_uint(v); auto r = __builtin_amdgcn_permlane32_swap(a, a, false, false); u32 x = r[0], y = r[1]; return __uint_as_float(x) + __uint_as_float(y); }
DEV u32 ord16(u32 b) { return (b & 0x8000u) ? (~b & 0xffffu) : (b | 0x8000u); }
DEV float sigm(float v) { return __builtin_amdgcn_rcpf(1.f + __builtin_amdgcn_exp2f(-v * 1.4426950408889634f)); }

DEV rsrc_t mk_rsrc(const void* p, u32 bytes) { return __builtin_amdgcn_make_buffer_rsrc((void*)p, 0, (int)bytes, 0x00020000); }
DEV u32x4 ld_nt_128(rsrc_t rs, u32 voff, u32 soff) { return __builtin_amdgcn_raw_buffer_load_b128(rs, voff, soff, 2); }
DEV u32x4 ld_sc1_128(rsrc_t rs, u32 voff, u32 soff) { return __builtin_amdgcn_raw_buffer_load_b128(rs, voff, soff, 16); }
DEV u32x2 ld_sc1_64(rsrc_t rs, u32 voff, u32 soff) { return __builtin_amdgcn_raw_buffer_load_b64(rs, voff, soff, 16); }
DEV u32 ld_sc1_32(rsrc_t rs, u32 voff, u32 soff) { return __builtin_amdgcn_raw_buffer_load_b32(rs, voff, soff, 16); }
DEV void st_sc1_128(u32x4 v, rsrc_t rs, u32 voff) { __builtin_amdgcn_raw_buffer_store_b128(v, rs, voff, 0, 16); }
DEV void st_sc1_64(u32x2 v, rsrc_t rs, u32 voff) { __builtin_amdgcn_raw_buffer_store_b64(v, rs, voff, 0, 16); }
DEV void st_sc1_32(u32 v, rsrc_t rs, u32 voff) { __builtin_amdgcn_raw_buffer_store_b32(v, rs, voff, 0, 16); }
#define VMCNT(n) asm volatile("s_waitcnt vmcnt(" #n ")" ::: "memory")
#define LGKM0() asm volatile("s_waitcnt lgkmcnt(0)" ::: "memory")
#define CBAR() asm volatile("" ::: "memory")
#define BAR() do { CBAR(); __builtin_amdgcn_s_barrier(); CBAR(); } while (0)
#define SB() __builtin_amdgcn_sched_barrier(0)
#define LP(a) ((LAS char*)(uintptr_t)(a))
#define PIN(x) asm volatile("" : "+v"(x))

#define TICK() 0ull
#define RT() 0ull
#define STAMP(k) do {} while (0)
#define CSTAMP(k) do {} while (0)
#define LSTAMP(k) do {} while (0)

DEV u32 lds_rd(LAS char* L, u32 off) { return *(volatile LAS u32*)(L + off); }
DEV void lds_wr(LAS char* L, u32 off, u32 v) { *(volatile LAS u32*)(L + off) = v; }
DEV void lds_wait_set(LAS char* L, u32 off) {
  _Pragma("nounroll") for (int sp = 0; sp < SPIN_CAP; ++sp) {
    if (__all(lds_rd(L, off) != 0u)) break;
    __builtin_amdgcn_s_sleep(1);
  }
  CBAR();
}
DEV void wait_until(u64 t0, u32 ticks) {
  while (__builtin_amdgcn_s_memrealtime() - t0 < ticks) __builtin_amdgcn_s_sleep(4);
}
#define DMA_HEAD "s_mov_b32 m0, %0\n\ts_nop 0\n\t"
DEV void dma16(u32 lds, u32 voff, rsrc_t rs, u32 soff) {
  asm volatile(DMA_HEAD "buffer_load_dwordx4 %1, %2, %3 offen lds" ::"s"(lds), "v"(voff), "s"(rs), "s"(soff) : "memory");
}
DEV void dma16x4(u32 lds, u32 v0, u32 v1, u32 v2, u32 v3, rsrc_t rs, u32 soff) {
  asm volatile(DMA_HEAD "buffer_load_dwordx4 %1, %5, %6 offen lds\n\tbuffer_load_dwordx4 %2, %5, %6 offen offset:1024 lds\n\t"
               "buffer_load_dwordx4 %3, %5, %6 offen offset:2048 lds\n\tbuffer_load_dwordx4 %4, %5, %6 offen offset:3072 lds"
               ::"s"(lds), "v"(v0), "v"(v1), "v"(v2), "v"(v3), "s"(rs), "s"(soff) : "memory");
}
DEV void dma16x2(u32 lds, u32 v0, u32 v1, rsrc_t rs, u32 soff) {
  asm volatile(DMA_HEAD "buffer_load_dwordx4 %1, %3, %4 offen lds\n\tbuffer_load_dwordx4 %2, %3, %4 offen offset:1024 lds"
               ::"s"(lds), "v"(v0), "v"(v1), "s"(rs), "s"(soff) : "memory");
}

struct Frags { s16x8 x[6]; s16x8 w[6]; };

DEV s16x8 tr_frag(LAS char* p, LAS char* q, u32 o0, u32 o1) {
  s16x4 a = __builtin_amdgcn_ds_read_tr16_b64_v4i16((LAS s16x4*)(p + o0));
  s16x4 b = __builtin_amdgcn_ds_read_tr16_b64_v4i16((LAS s16x4*)(q + o1));
  return __builtin_shufflevector(a, b, 0, 1, 2, 3, 4, 5, 6, 7);
}
template <int KK> DEV void load_frags(Frags& f, LAS char* aH, LAS char* aT, LAS char* w0, LAS char* w1) {
#pragma unroll
  for (int t = 0; t < 5; ++t) f.x[1 + t] = *(LAS s16x8*)(aT + t * 2048);
  f.x[0] = *(LAS s16x8*)aH;
#pragma unroll
  for (int sec = 0; sec < 3; ++sec) {
    f.w[2 * sec] = tr_frag(w0, w0, sec * 8192 + KK * 4096, sec * 8192 + KK * 4096 + 512);
    f.w[2 * sec + 1] = tr_frag(w1, w1, sec * 8192 + KK * 4096, sec * 8192 + KK * 4096 + 512);
  }
}
template <int A, int B> DEV void mma_range(f32x4 (&acc)[6][6], const Frags& f, s16x8 e0) {
#pragma unroll
  for (int n = A; n < B; ++n) {
    const int p = n / 11, r = n % 11, w = 2 * p + (r >= 6), t = (r >= 6) ? 11 - r : 5 - r;
    acc[w][t] = (w < 4) ? mfma(f.w[w], (w == 0 && t == 0) ? e0 : f.x[t], acc[w][t]) : mfma(f.x[t], f.w[w], acc[w][t]);
  }
}
DEV void interleave() {
#pragma unroll
  for (int k = 0; k < 6; ++k) { __builtin_amdgcn_sched_group_barrier(0x008, 1, 0); __builtin_amdgcn_sched_group_barrier(0x100, 1, 0); }
#pragma unroll
  for (int k = 0; k < 6; ++k) {
    __builtin_amdgcn_sched_group_barrier(0x008, 1, 0); __builtin_amdgcn_sched_group_barrier(0x100, 1, 0);
    __builtin_amdgcn_sched_group_barrier(0x008, 1, 0); __builtin_amdgcn_sched_group_barrier(0x100, 1, 0);
    __builtin_amdgcn_sched_group_barrier(0x008, 1, 0);
  }
}

template <int E> DEV s16x8 col_frag(const u32x4* L) {
  constexpr u32 sel = (E & 1) ? 0x07060302u : 0x05040100u;
  u32x4 r;
  r.x = __builtin_amdgcn_perm(L[1][E >> 1], L[0][E >> 1], sel);
  r.y = __builtin_amdgcn_perm(L[3][E >> 1], L[2][E >> 1], sel);
  r.z = __builtin_amdgcn_perm(L[5][E >> 1], L[4][E >> 1], sel);
  r.w = __builtin_amdgcn_perm(L[7][E >> 1], L[6][E >> 1], sel);
  return as_frag(r);
}
DEV void mma_cols(f32x4* acc, const u32x4* L, s16x8 b) {
  acc[0] = mfma(col_frag<0>(L), b, acc[0]); acc[1] = mfma(col_frag<1>(L), b, acc[1]);
  acc[2] = mfma(col_frag<2>(L), b, acc[2]); acc[3] = mfma(col_frag<3>(L), b, acc[3]);
  acc[4] = mfma(col_frag<4>(L), b, acc[4]); acc[5] = mfma(col_frag<5>(L), b, acc[5]);
  acc[6] = mfma(col_frag<6>(L), b, acc[6]); acc[7] = mfma(col_frag<7>(L), b, acc[7]);
}

DEV void transpose_grp(u32x4* L) {
  const s16x8 f0 = col_frag<0>(L), f1 = col_frag<1>(L), f2 = col_frag<2>(L), f3 = col_frag<3>(L), f4 = col_frag<4>(L), f5 = col_frag<5>(L), f6 = col_frag<6>(L), f7 = col_frag<7>(L);
  L[0] = __builtin_bit_cast(u32x4, f0); L[1] = __builtin_bit_cast(u32x4, f1); L[2] = __builtin_bit_cast(u32x4, f2); L[3] = __builtin_bit_cast(u32x4, f3);
  L[4] = __builtin_bit_cast(u32x4, f4); L[5] = __builtin_bit_cast(u32x4, f5); L[6] = __builtin_bit_cast(u32x4, f6); L[7] = __builtin_bit_cast(u32x4, f7);
#pragma unroll
  for (int e = 0; e < 8; ++e) PIN(L[e]);
  SB();
}
DEV void mma_frags8(f32x4* acc, const u32x4* L, s16x8 b) {
#pragma unroll
  for (int e = 0; e < 8; ++e) acc[e] = mfma(as_frag(L[e]), b, acc[e]);
}
DEV void warm_line(u32 lds, const char* a) {
  asm volatile(DMA_HEAD "global_load_lds_dword %1, off" ::"s"(lds), "v"(a) : "memory");
}

__global__ void __launch_bounds__(512, 2) fused_kernel(Args Parg) {
  extern __shared__ __attribute__((aligned(128))) char smem[];
  u32x16 ka;
  u64 kb;
  asm volatile("s_load_dwordx16 %0, %2, 0x0\n\ts_load_dwordx2 %1, %2, 0x40" : "=&s"(ka), "=&s"(kb) : "s"(__builtin_amdgcn_kernarg_segment_ptr()));
  Params P;
  const char* pc0;
  asm volatile("s_getpc_b64 %0" : "=s"(pc0));
  LAS char* L = (LAS char*)smem;
  const u32 lbase = (u32)(uintptr_t)L;
  const int lane = threadIdx.x & 63;
  const int wave = rfl(threadIdx.x >> 6);
  const int bid = blockIdx.x, xg = bid & 7, loc = bid >> 3;
  const int np = 7 - (xg & 1);
  const bool gemm = loc < 4 * np;
  const int pl = loc >> 2, hl = loc & 3;
  const int pan = (xg & 1) * 7 + pl, hp = (xg >> 1) * 4 + hl, r0 = pan * 160;
  const int fi = loc - 4 * np;
  const bool dwn = (u32)(fi - 2 - 3 * (xg & 1)) <= 2u * (xg & 1);
  const int fu = (xg & 1) * 12 + (xg >> 1) * (3 + 2 * (xg & 1)) + min(fi, 2 + 2 * (xg & 1));
  const int du = (xg & 1) ? 4 + (xg >> 1) * 3 + fi - 5 : (xg >> 1);
  const int pan2 = (xg & 1) * 8 + (xg >> 1) * 2 + (loc >> 4);
  const int cg = (int)((((loc & 16) ? 0xfedc0a987653421bull : 0xfedc8a9b76534210ull) >> (4 * (loc & 15))) & 15), hrot = 2 * cg;
  const int g = lane >> 4, i = lane & 15;
  u64 kord;
  asm volatile("s_load_dwordx2 %0, %1, %2" : "=&s"(kord) : "s"(__builtin_amdgcn_kernarg_segment_ptr()), "s"(72 + (((xg & 1) * 32 + loc) & 63) * 8));
  rsrc_t rsWS;
  const u64 tstart = __builtin_amdgcn_s_memrealtime();
  u32 tag = 0;
  f32x4 oc[2][4];
  auto oc_init = [&]() {
    const int tl = (wave >> 1) + 64 * (wave & 1) + 2 * i;
    f32x2 st[2];
    u32x2 v[2][4];
#pragma unroll
    for (int tt = 0; tt < 2; ++tt) {
      st[tt] = *(LAS f32x2*)(L + L_STO + tl * 8 + tt * 256);
#pragma unroll
      for (int c = 0; c < 4; ++c) v[tt][c] = *(LAS u32x2*)(L + L_XT + tl * 128 + (((2 * c + (g >> 1)) ^ (i & 7)) << 4) + (g & 1) * 8 + tt * 4096);
    }
    SB();
#pragma unroll
    for (int tt = 0; tt < 2; ++tt) {
      const float k1 = 1.f + st[tt].y, k0 = -st[tt].x * st[tt].y;
#pragma unroll
      for (int c = 0; c < 4; ++c) oc[tt][c] = f32x4{bf_lo(v[tt][c].x), bf_hi(v[tt][c].x), bf_lo(v[tt][c].y), bf_hi(v[tt][c].y)} * k1 + k0;
    }
#pragma unroll
    for (int q = 0; q < 8; ++q) PIN(oc[q >> 2][q & 3]);
  };

  u32 tq[3], vO, vx;
  auto helper_offsets = [&]() {
    u32 lane = threadIdx.x & 63;
    PIN(lane);
    const int lr = lane >> 3, lc = lane & 7;
#pragma unroll
    for (int j = 0; j < 3; ++j) {
      const int rho = min(64 * j + (int)lane, 175), pr = rho >= 96;
      const int tk = r0 + ((rho < 16) ? rho - 16 : pr + 2 * (rho - 16 - 80 * pr));
      const u32 a = (u32)(pan2 * 128 + (rho & 127)) * 8, b = (((u32)tk >= (u32)S_) | (rho == 0)) ? 1u : (u32)tk * 8;
      tq[j] = (wave == 7) ? a : b;
      PIN(tq[j]);
    }
    vO = lr * 2048 + ((u32)(lc ^ (((lr >> 1) & 3) << 1)) << 4) + cg * 128;
    vx = lr * 2048 + ((u32)(lc ^ (lr >> 1)) << 4) + cg * 128;
    PIN(vO); PIN(vx);
  };
  auto wo_grp = [&](int q) { dma16x4(lbase + q * 4096, vO, vO + 15 * 1024, vO + 30 * 1024, vO + 45 * 1024, mk_rsrc(P.wo, D_ * D_ * 2), ((q + hrot) & 31) * 65536); };
  auto helper_fill = [&]() {
    const int lid = wave - 4;
    tag = (u32)rfl((int)lds_rd(L, L_TAG));
    if (gemm) { VMCNT(11); BAR(); }
    if (lid >= 2) {
      u32x2 sv[3];
#pragma unroll
      for (int q = 2 * lid - 4; q < 2 * lid - 2; ++q) dma16x4(lbase + L_XT + q * 4096, vx, (vx ^ 64) + 15 * 1024, vx + 30 * 1024, (vx ^ 64) + 45 * 1024, mk_rsrc(P.x, S_ * D_ * 2), (pan2 * 128 + 32 * q) * 2048);
#pragma unroll
      for (int j = 0; j < 3; ++j) sv[j] = ld_sc1_64(rsWS, tq[j] & ~7u, WS_SG);
      SB();
      if (gemm) { VMCNT(11); BAR(); }
      _Pragma("nounroll") for (int sp = 0; sp < SPIN_CAP; ++sp) {
        if (__all(sv[0].y == tag && sv[1].y == tag && sv[2].y == tag)) break;
        __builtin_amdgcn_s_sleep(2);
#pragma unroll
        for (int j = 0; j < 3; ++j) sv[j] = ld_sc1_64(rsWS, tq[j] & ~7u, WS_SG);
      }
      f32x2 st[3];
      u32 ta = lbase + ((lid == 3) ? L_STO + lane * 8 : L_STQ + lane * 4), tb2 = ta + ((lid == 3) ? 4 : 704);
      u32 tc = (lid == 2 && lane < 48) ? ta + 512 : lbase + L_UPD + lane * 4, td = (lid == 2 && lane < 48) ? tb2 + 512 : tc;
      const u32 ts = (lid == 3) ? 512 : 256;
#pragma unroll
      for (int j = 0; j < 3; ++j) {
        st[j] = f32x2{__uint_as_float((sv[j].x << 16) ^ ((lid == 3) ? 0u : 0x80000000u)), __builtin_amdgcn_rcpf(bf_hi(sv[j].x))};
        if (tq[j] & 1u) st[j] = f32x2{0.f, 0.f};
        PIN(st[j]);
      }
      u32 te = ta + ts, tf = tb2 + ts;
      PIN(ta); PIN(tb2); PIN(tc); PIN(td); PIN(te); PIN(tf);
      VMCNT(0);
      if (gemm) BAR();
      *(LAS float*)LP(ta) = st[0].x; *(LAS float*)LP(tb2) = st[0].y;
      *(LAS float*)LP(te) = st[1].x; *(LAS float*)LP(tf) = st[1].y;
      *(LAS float*)LP(tc) = st[2].x; *(LAS float*)LP(td) = st[2].y;
      LGKM0();
    } else {
      if (gemm) { VMCNT(0); BAR(); }
      wo_grp(12 + lid); wo_grp(14 + lid);
#pragma unroll
      for (int q = 0; q < 4; ++q) wo_grp(16 + 2 * q + lid);
      if (gemm) BAR();
    }
    if (gemm) BAR();
    int l2 = lid;
    asm volatile("" : "+s"(l2));
    wo_grp(l2); wo_grp(4 + l2); wo_grp(8 + l2);
    if (!dwn) { wo_grp(24 + l2); wo_grp(28 + l2); }
    if (lid == 2) {
      u32x2 sk;
      _Pragma("nounroll") for (int sp = 0; sp < SPIN_CAP; ++sp) {
        sk = ld_sc1_64(rsWS, (lane & 7) * 8, WS_SEL);
        if (__all(sk.y == tag)) break;
        __builtin_amdgcn_s_sleep(8);
      }
      if (lane < 8) lds_wr(L, L_SEL + 4 * lane, sk.x);
      LGKM0();
    }
  };
  {
    u32 ln = threadIdx.x & 63;
    PIN(ln);
    if (ln < 2) warm_line(lbase + L_UPD, pc0 + min((loc * 8 + wave) * 2 + (int)ln, CODE_LINES - 1) * 128);
  }
  const int lid = wave - 4;
  const bool isx = lid < 2;
  u32 v0 = 0, v1 = 0, v2 = 0, v3 = 0, vh = 0, g0 = 0, g1 = 0, g2 = 0, kstep = 0, d0 = 0;
  {
    int wave_a = wave;
    asm volatile("" : "+s"(wave_a));
    if (gemm && wave_a >= 4) {
      u32 ln = threadIdx.x & 63;
      PIN(ln);
      const int lr = ln >> 3, lc = ln & 7;
      const u32 sw = (u32)(lc ^ (isx ? lr >> 1 : lr & 2)) << 4;
      const u32 vb = lr * (isx ? 4096 : 6144) + sw + (isx ? (r0 + lid) * 2048 : hp * 128);
      const u32 pp = (isx ? 32768 : 49152) - 1024;
      v0 = vb; v1 = (vb ^ 64) + pp; v2 = vb + 2 * pp; v3 = (vb ^ 64) + 3 * pp;
      g0 = (lid == 3) ? 198656 : 0; g1 = isx ? 131072 : (lid == 3) ? 4096 : 196608; g2 = isx ? 262144 : (lid == 3) ? 200704 : 2048;
      vh = max(r0 - 16 + 8 * lid + lr, 0) * 2048 + (sw ^ (lid << 6));
      kstep = isx ? 128u : 64u * 6144u; d0 = lbase + (isx ? 2 + 10 * lid : 22 + 12 * (lid - 2)) * 1024;
      PIN(v0); PIN(v1); PIN(v2); PIN(v3); PIN(vh);
      asm volatile("" : "+s"(g0), "+s"(g1), "+s"(g2), "+s"(kstep), "+s"(d0));
    }
  }
  u32 ev;
  asm volatile("s_waitcnt lgkmcnt(0)" : "+s"(ka), "+s"(kb), "+s"(kord));
  {
    auto ptr = [&](int k) { return (char*)(GAS char*)(((u64)ka[2 * k + 1] << 32) | ka[2 * k]); };
    P.x = (const u16*)ptr(0); P.wqkv = (const u16*)ptr(1); P.wo = (const u16*)ptr(2); P.wr = (const u16*)ptr(3);
    P.wg = (const u16*)ptr(4); P.wu = (const u16*)ptr(5); P.wd = (const u16*)ptr(6); P.out = (u16*)ptr(7);
    P.ws = (char*)(GAS char*)kb;
  }
  rsWS = mk_rsrc(P.ws, WS_END);
  STAMP(0);
  if (wave < 4) {
    asm volatile("s_load_dword %0, %1, 0x0" : "=s"(ev) : "s"(P.ws + WS_EPOCH) : "memory");
    u32 ln = threadIdx.x & 63;
    PIN(ln);
    const u32 d = lbase + 2 * SLOT + wave * 6144, v = ln * 16;
    dma16x4(d, v, v, v, v, mk_rsrc(P.x, S_ * D_ * 2), (pan2 * 128 + cg * 8 + wave * 2) * 2048);
    dma16x2(d + 4096, v, v, mk_rsrc(P.wr, D_ * 2), 0);
  }
  SB();
  int wave_b = wave;
  asm volatile("" : "+s"(wave_b));
  if (gemm && wave_b >= 4) {
    const rsrc_t rs = mk_rsrc(isx ? P.x : P.wqkv, isx ? S_ * D_ * 2 : D_ * 3 * D_ * 2);
    auto issue_k = [&](u32 ko, u32 so) {
      dma16x4(d0 + so, v0, v1, v2, v3, rs, ko + g0);
      dma16x4(d0 + so + 4096, v0, v1, v2, v3, rs, ko + g1);
      if (isx) { dma16x2(d0 + so + 8192, v0, v1, rs, ko + g2); dma16(lbase + so + lid * 1024, vh, rs, ko); }
      else dma16x4(d0 + so + 8192, v0, v1, v2, v3, rs, ko + g2);
    };
    auto issue = [&](int s, u32 so) { issue_k(((u32)(kord >> (4 * s)) & 15u) * kstep, so); };
    LSTAMP(20);
    issue(0, 0);
    LSTAMP(21);
    helper_offsets();
    issue(1, SLOT);
    LSTAMP(22);
    if (isx) VMCNT(11); else VMCNT(12);
    LSTAMP(23);
    BAR();
    const bool helps = !isx && !(xg & 2) && hl != 3;
    if (helps) {
      constexpr int HL = (HELPN * 192 + 35) / 36;
      const int n = min(((pl * 3 + hl) * 2 + lid - 2) * HL + min(lane, HL - 1), HELPN * 192 - 1), row = n / 3;
      warm_line(lbase + L_UPD, (const char*)P.wqkv + (size_t)((u32)((HELPK >> (4 * (row >> 6))) & 15) * 64 + (row & 63)) * 6144 + (n % 3) * 2048 + (hp | 3) * 128);
    }
    issue(2, 2 * SLOT);
    u32 so = 0, sdum = 0;
    const int sn = min((pl * 4 + hl) * 4 + lid, 95) * 2;
    const u32 so0 = (sn / 3) * 6144 + (sn % 3) * 2048, so1 = ((sn + 1) / 3) * 6144 + ((sn + 1) % 3) * 2048;
    const char* sbase = (const char*)P.wqkv + (hp | 3) * 128;
    _Pragma("nounroll") for (int s = 0; s < 13; ++s) {
      if (helps && s == 0) VMCNT(13); else if (!isx || s == WARM_AT + 1 || s == WARM_AT + 2) VMCNT(12); else VMCNT(11);
      if (s == 0) LSTAMP(24);
      if (s == 1) LSTAMP(25);
      if (s == 2) LSTAMP(26);
      BAR();
      issue(s + 3, so);
      if (!(xg & 2) && (u32)(s - RS0) < RSN) {
        const u32 ko = (u32)((ROLLK >> (4 * (s - RS0))) & 15) * (64u * 6144u), a0 = ko + so0, a1 = ko + so1;
        asm volatile("s_load_dword %0, %1, %2\n\ts_load_dword %0, %1, %2 offset:64\n\ts_load_dword %0, %1, %3\n\ts_load_dword %0, %1, %3 offset:64"
                     : "+s"(sdum) : "s"(sbase), "s"(a0), "s"(a1) : "memory");
      }
      if (s == WARM_AT && isx) warm_line(lbase + L_UPD, (const char*)P.wo + (size_t)min((pan * 16 + hp) * 80 + lid * 40 + min(lane, 39), 16383) * 128);
      so = (so == 2 * SLOT) ? 0 : so + SLOT;
    }
    asm volatile("s_waitcnt lgkmcnt(0)" : "+s"(sdum));
    STAMP(2);
    helper_fill();
  }
  if (wave < 4) {
    __builtin_amdgcn_s_setprio(1);
    if (wave == 1) lds_wr(L, L_ONE + lane * 4, lane < 32 ? 0x3f803f80u : 0u);
    if (wave == 2) lds_wr(L, L_GO, 0u);
    f32x4 acc[6][6];
#pragma unroll
    for (int w = 0; w < 6; ++w)
#pragma unroll
      for (int t = (w & 1); t < 6; ++t) { acc[w][t] = f32x4{0.f, 0.f, 0.f, 0.f}; PIN(acc[w][t]); }
    f32x4 bprev, bhalo;
    u32 mc[2];
    u64 cur[4];
#pragma unroll
    for (int r = 0; r < 4; ++r) {
      const int kap = 4 * g + r, d = 16 + (wave & 1) + 2 * i - kap;
      bprev[r] = (kap - i >= 9) ? 0.f : -1e30f;
      bhalo[r] = (((d & 1) == 0) & (d <= 14) & (pan > 0)) ? 0.f : -1e30f;
      cur[r] = __ballot((u32)(i - kap) <= 7u);
      asm volatile("" : "+s"(cur[r]));
    }
    mc[0] = (((u32)(i - 4 * g) <= 7u) ? 0xffffu : 0u) | (((u32)(i - 4 * g - 1) <= 7u) ? 0xffff0000u : 0u);
    mc[1] = (((u32)(i - 4 * g - 2) <= 7u) ? 0xffffu : 0u) | (((u32)(i - 4 * g - 3) <= 7u) ? 0xffff0000u : 0u);
    u32 hxl = lbase + L_HX + (wave & 2) * 2304 + lane * 16;
    u32 obase = WS_O + (((wave & 1) * 32 + hp * 2 + (wave >> 1)) * 64 + pan * 5) * 1024 + lane * 16;
    PIN(bprev); PIN(bhalo); PIN(mc[0]); PIN(mc[1]); PIN(hxl); PIN(obase);
    SB();
    {
      const int tok = pan2 * 128 + cg * 8 + wave * 2 + (lane >> 5);
      u32x4 xv[4], wv[4];
      VMCNT(0);
      {
        LAS char* sp = L + 2 * SLOT + wave * 6144 + (lane & 31) * 16;
#pragma unroll
        for (int j = 0; j < 4; ++j) { xv[j] = *(LAS u32x4*)(sp + (lane >> 5) * 2048 + j * 512); wv[j] = *(LAS u32x4*)(sp + 4096 + j * 512); }
      }
      asm volatile("s_waitcnt lgkmcnt(0)" : "+s"(ev) :: "memory");
      const u32 e = ev;
      tag = e * 0x9E3779B1u + 0x7F4A7C15u; tag ^= tag >> 15; tag = (u32)rfl((int)(tag ^ (u32)(tag == e)));
      if (wave == 1) lds_wr(L, L_TAG, tag);
      float sx = 0.f, sxx = 0.f, sxw = 0.f, sw = 0.f;
#pragma unroll
      for (int j = 0; j < 4; ++j)
#pragma unroll
        for (int e2 = 0; e2 < 4; ++e2) {
          const u32 a = xv[j][e2], b = wv[j][e2];
          sx = dot2(a, 0x3f803f80u, sx); sxx = dot2(a, a, sxx); sxw = dot2(a, b, sxw); sw = dot2(b, 0x3f803f80u, sw);
        }
      const float mean = half_sum(sx) * (1.f / D_);
      const float var = fmaxf(half_sum(sxx) * (1.f / D_) - mean * mean, 0.f);
      const float mb = rbf(mean), sb = rbf(__builtin_amdgcn_sqrtf(rbf(rbf(var) + 1e-5f)));
      const u32 ro = ord16(pack2((half_sum(sxw) - mb * half_sum(sw)) * __builtin_amdgcn_rcpf(sb), 0.f) & 0xffffu);
      const u32 rlo = (u32)__builtin_amdgcn_readlane((int)ro, 31);
      if ((lane & 31) == 31) st_sc1_64(u32x2{pack2(mb, sb), tag}, rsWS, WS_SG + tok * 8);
      if (lane == 63) st_sc1_64(u32x2{rlo | (ro << 16), tag}, rsWS, WS_RG + (tok >> 1) * 8);
      if (!gemm) { LGKM0(); BAR(); }
    }
    STAMP(1);
    if (gemm) {
      const int h = wave >> 1, par = wave & 1;
        const int q4 = i >> 2, p4 = i & 3;
        u32 a0 = lbase + i * 128 + ((g ^ (i >> 1)) << 4);
        const u32 wsw = ((q4 >> 1) & 1) | ((g & 1) << 1);
        u32 w0 = lbase + WOFF + (8 * g + q4) * 128 + (((2 * h + par) ^ wsw) << 5) + p4 * 8;
        const u32 tb = (1 + 5 * par) * 2048;
        const u32 one = lbase + L_ONE;
        u32 pH = (i == 0) ? one : a0, pT = a0 + tb, pW0 = w0, pW1 = w0 ^ 32;
        PIN(a0); PIN(w0); PIN(pH); PIN(pT); PIN(pW0); PIN(pW1);
        Frags f0, f1;
        const u32 e1 = (i == 0) ? 0x3f803f80u : 0u;
        s16x8 e0 = as_frag(u32x4{e1, e1, e1, e1});
        PIN(e0);
        __builtin_amdgcn_s_waitcnt(0xc07f);
        BAR();
        STAMP(2); CSTAMP(14);
        load_frags<0>(f0, LP(pH), LP(pT), LP(pW0), LP(pW1));
        pH ^= 64; pT ^= 64;
        __builtin_amdgcn_s_waitcnt(0xc07f);
        u32 so = 0, trc = 0;
        auto half1 = [&]() {
          SB();
          load_frags<1>(f1, LP(pH), LP(pT), LP(pW0), LP(pW1));
          mma_range<0, 24>(acc, f0, e0);
          interleave();
          SB();
          so = (so == 2 * SLOT) ? 0 : so + SLOT;
          mma_range<24, 25>(acc, f0, e0);
          SB();
          pT = so + a0; pW0 = so + w0; PIN(pT); PIN(pW0);
          mma_range<25, 26>(acc, f0, e0);
          SB();
          pH = (i == 0) ? one : pT; pW1 = pW0 ^ 32; PIN(pH); PIN(pW1);
          mma_range<26, 27>(acc, f0, e0);
          SB();
          pT += tb; PIN(pT);
          mma_range<27, 28>(acc, f0, e0);
          SB();
        };
        auto half1b = [&](int s) {
          const u64 tnow = TICK(), rnow = RT();
          mma_range<28, 30>(acc, f0, e0);
          SB();
          __builtin_amdgcn_s_waitcnt(0xc07f);
          if (STAMPS) { trc = (lane == s) ? (u32)tnow : trc; trc = (lane == s + 16) ? (u32)rnow : trc; }
          mma_range<30, 31>(acc, f0, e0);
          SB();
          BAR();
          SB();
          mma_range<31, 33>(acc, f0, e0);
          SB();
        };
        half1();
        for (int s = 0; s < 15; ++s) {
          half1b(s);
          load_frags<0>(f0, LP(pH), LP(pT), LP(pW0), LP(pW1));
          mma_range<0, 24>(acc, f1, e0);
          interleave();
          SB();
          pH ^= 64; pT ^= 64; PIN(pH); PIN(pT);
          mma_range<24, 31>(acc, f1, e0);
          SB();
          __builtin_amdgcn_s_waitcnt(0xc07f);
          mma_range<31, 33>(acc, f1, e0);
          half1();
        }
        half1b(15);
        if (STAMPS && lane < 32) ((GAS u32*)(P.ws + WS_STAMP + (1 << 20)))[(bid * 4 + wave) * 32 + lane] = trc;
      mma_range<5, 6>(acc, f1, e0); mma_range<16, 17>(acc, f1, e0); mma_range<27, 28>(acc, f1, e0);
      SB();
      mma_range<11, 16>(acc, f1, e0);
      u32 tz = threadIdx.x;
      PIN(tz);
      const u32 i = tz & 15, g = (tz >> 4) & 3;
      u32 sqi = lbase + L_STQ + i * 4, sqg = lbase + L_STQ + g * 16;
      PIN(sqi); PIN(sqg);
      const u32 hx = hxl + par * 2304, hy = hxl + (par ^ 1) * 2304;
      u32 hxg = hx - i * 16, hyg = hy - i * 16, hxi = hx - g * 256, hyi = hy - g * 256, sqi2 = sqi + par * 320, sqg2 = sqg + par * 320;
      const u32 hqa = hx + 2048 - g * 240, hqb = hxl + (L_UPD - L_HX) - (wave & 2) * 2304;
      u32 hq = __builtin_amdgcn_inverse_ballot_w64(0x0001000100010001ull) ? hqa : hqb;
      PIN(hxg); PIN(hyg); PIN(hxi); PIN(hyi); PIN(sqi2); PIN(sqg2); PIN(hq);
#pragma unroll
      for (int k = 0; k < 5; ++k) { __builtin_amdgcn_sched_group_barrier(0x008, 1, 0); __builtin_amdgcn_sched_group_barrier(0x002, 2, 0); }
      SB();
      *(LAS f32x4*)LP(hx) = acc[2][0];
      *(LAS f32x4*)LP(hx + 1024) = acc[4][0];
      *(LAS f32x4*)LP(hq) = acc[0][0];
      mma_range<17, 22>(acc, f1, e0);
#pragma unroll
      for (int k = 0; k < 3; ++k) { __builtin_amdgcn_sched_group_barrier(0x008, 1, 0); __builtin_amdgcn_sched_group_barrier(0x200, 1, 0); }
      SB();
      mma_range<0, 3>(acc, f1, e0);
      SB();
      __builtin_amdgcn_s_waitcnt(0xc07f);
      BAR();
      SB();
      f32x4 cs[4];
      cs[2] = *(LAS f32x4*)LP(hxg); cs[3] = *(LAS f32x4*)LP(hyg);
      cs[0] = *(LAS f32x4*)LP(hxg + 2048 - g * 240); cs[1] = *(LAS f32x4*)LP(hyg + 2048 - g * 240);
      float m[6], rs[6];
#pragma unroll
      for (int t = 0; t < 6; ++t) { m[t] = *(LAS float*)LP((t ? sqi2 : sqi) + t * 64); rs[t] = *(LAS float*)LP((t ? sqi2 : sqi) + t * 64 + 704); }
      acc[3][0] = *(LAS f32x4*)LP(hy);
      const float cv[2] = {*(LAS float*)LP(hxi + 1024), *(LAS float*)LP(hyi + 1024)};
      acc[5][0] = *(LAS f32x4*)LP(hy + 1024);
      mma_range<3, 5>(acc, f1, e0); mma_range<6, 11>(acc, f1, e0);
#pragma unroll
      for (int k = 0; k < 7; ++k) { __builtin_amdgcn_sched_group_barrier(0x008, 1, 0); __builtin_amdgcn_sched_group_barrier(0x100, 2, 0); }
      SB();
      STAMP(3); CSTAMP(15);
      const float QS = 0.17677669529663687f * 1.4426950408889634f;
      const f32x4 z4 = {0.f, 0.f, 0.f, 0.f};
      f32x4 s0[5], s1[5], m4[6], r4[6];
      {
        s16x8 kf[6];
#pragma unroll
        for (int t = 5; t >= 0; --t) {
          const f32x4 k0 = (acc[2][t] + m[t] * cs[2]) * rs[t], k1 = (acc[3][t] + m[t] * cs[3]) * rs[t];
          kf[t] = as_frag(u32x4{pack2(k0[0], k0[1]), pack2(k0[2], k0[3]), pack2(k1[0], k1[1]), pack2(k1[2], k1[3])});
        }
        mma_range<22, 27>(acc, f1, e0); mma_range<28, 33>(acc, f1, e0);
#pragma unroll
        for (int k = 0; k < 10; ++k) { __builtin_amdgcn_sched_group_barrier(0x008, 1, 0); __builtin_amdgcn_sched_group_barrier(0x002, 3, 0); }
        SB();
#pragma unroll
        for (int t = 0; t < 6; ++t) { m4[t] = *(LAS f32x4*)LP((t ? sqg2 : sqg) + t * 64); r4[t] = *(LAS f32x4*)LP((t ? sqg2 : sqg) + t * 64 + 704); }
#pragma unroll
        for (int t = 0; t < 5; ++t) {
          const f32x4 q0 = acc[0][t + 1] + m[t + 1] * cs[0], q1 = acc[1][t + 1] + m[t + 1] * cs[1];
          const s16x8 qf = as_frag(u32x4{pack2(q0[0], q0[1]), pack2(q0[2], q0[3]), pack2(q1[0], q1[1]), pack2(q1[2], q1[3])});
          s0[t] = mfma(kf[t], qf, t == 0 ? bhalo : bprev); s1[t] = mfma(kf[t + 1], qf, z4);
        }
      }
      SB();
      u32x16 vp[2];
#pragma unroll
      for (int t = 0; t < 6; ++t) {
#pragma unroll
        for (int c = 0; c < 2; ++c) {
          const f32x4 v = (acc[4 + c][t] + m4[t] * cv[c]) * r4[t];
          vp[c][2 * t] = pack2(v[0], v[1]); vp[c][2 * t + 1] = pack2(v[2], v[3]);
        }
      }
      float l[5][4], mx[5], rq[5];
#pragma unroll
      for (int t = 0; t < 5; ++t) {
#pragma unroll
        for (int r = 0; r < 4; ++r) l[t][r] = __builtin_amdgcn_inverse_ballot_w64(cur[r]) ? s1[t][r] : s0[t][r];
        mx[t] = vmax3(vmax(l[t][0], l[t][1]), l[t][2], l[t][3]);
        rq[t] = rs[t + 1] * QS;
      }
#pragma unroll
      for (int t = 0; t < 5; ++t) mx[t] = max16(mx[t]);
#pragma unroll
      for (int t = 0; t < 5; ++t) mx[t] = -max32(mx[t]) * rq[t];
      const s16x8 ones = as_frag(u32x4{0x3f803f80u, 0x3f803f80u, 0x3f803f80u, 0x3f803f80u});
      f32x4 sm[5], o0[5], o1[5];
#pragma unroll
      for (int t = 0; t < 5; ++t) {
#pragma unroll
        for (int r = 0; r < 4; ++r) l[t][r] = __builtin_amdgcn_exp2f(__builtin_fmaf(l[t][r], rq[t], mx[t]));
        const u32 p01 = pack2(l[t][0], l[t][1]), p23 = pack2(l[t][2], l[t][3]);
        const s16x8 pp = as_frag(u32x4{p01 & ~mc[0], p23 & ~mc[1], p01 & mc[0], p23 & mc[1]});
        sm[t] = mfma(ones, pp, z4);
        o0[t] = mfma(as_frag(u32x4{vp[0][2 * t], vp[0][2 * t + 1], vp[0][2 * t + 2], vp[0][2 * t + 3]}), pp, z4);
        o1[t] = mfma(as_frag(u32x4{vp[1][2 * t], vp[1][2 * t + 1], vp[1][2 * t + 2], vp[1][2 * t + 3]}), pp, z4);
      }
#pragma unroll
      for (int t = 0; t < 5; ++t) {
        const float inv = __builtin_amdgcn_rcpf(sm[t][0]);
        o0[t] *= inv; o1[t] *= inv;
        st_sc1_128(u32x4{pack2(o0[t][0], o0[t][1]), pack2(o0[t][2], o0[t][3]), pack2(o1[t][0], o1[t][1]), pack2(o1[t][2], o1[t][3])}, rsWS,
                   (t == 4) ? obase + ((pan == 12) ? 0x40000000u : 4096u) : obase + t * 1024);
      }
      STAMP(8);
    }
  }
  if (!gemm) {
  if (dwn) {
    const rsrc_t rsD = mk_rsrc(P.wd, F_ * D_ * 2);
    const int k0 = (du >> 1) * 14 + (wave >> 2) * 7;
    const u32 vd = (g * D_ + (du & 1) * 512 + (wave & 3) * 128 + 8 * i) * 2;
    auto drow = [&](int q, int j) -> u32 { return (u32)(64 * ((k0 + q) >> 1) + 4 * ((k0 + q) & 1) + 8 * j) * (D_ * 2); };
    constexpr u32 D_W = 24 * 4096, D_H = D_W + 8 * 4352;
    u32x4 wb[56];
#pragma unroll
    for (int j = 0; j < 8; ++j) wb[j] = ld_nt_128(rsD, vd, drow(0, j));
    SB();
    if (wave >= 4) { BAR(); tag = (u32)rfl((int)lds_rd(L, L_TAG)); }
#pragma unroll
    for (int j = 8; j < 48; ++j) wb[j] = ld_nt_128(rsD, vd, drow(j >> 3, j & 7));
    SB();
    transpose_grp(wb);
#pragma unroll
    for (int e = 0; e < 4; ++e) *(LAS u32x4*)(L + D_W + wave * 4352 + e * 1024 + lane * 16) = wb[e];
    SB();
#pragma unroll
    for (int q = 1; q < 6; ++q) transpose_grp(wb + 8 * q);
#pragma unroll
    for (int j = 48; j < 56; ++j) wb[j] = ld_nt_128(rsD, vd, drow(6, j & 7));
    SB();
    __builtin_amdgcn_s_setprio(0);
    if (wave >= 4) { helper_offsets(); helper_fill(); }
    transpose_grp(wb + 48);
    VMCNT(0);
    STAMP(12);
    BAR();
    STAMP(11);
    {
      wait_until(tstart, T_H);
      const int qa = 2 * (wave & 3), qb = min(qa + 1, 6);
      u32x4 va, vb;
      _Pragma("nounroll") for (int sp = 0; sp < SPIN_CAP; ++sp) {
        va = ld_sc1_128(rsWS, lane * 16, WS_H + (k0 + qa) * 1024);
        vb = ld_sc1_128(rsWS, lane * 16, WS_H + (k0 + qb) * 1024);
        if (__all(va.y == tag && va.w == tag && vb.y == tag && vb.w == tag)) break;
        __builtin_amdgcn_s_sleep(1);
      }
      *(LAS u32x2*)(L + D_H + ((wave >> 2) * 7 + qa) * 512 + (lane & 31) * 16 + (lane >> 5) * 8) = u32x2{va.x, va.z};
      *(LAS u32x2*)(L + D_H + ((wave >> 2) * 7 + qb) * 512 + (lane & 31) * 16 + (lane >> 5) * 8) = u32x2{vb.x, vb.z};
      LGKM0();
      lds_wr(L, L_HR + wave * 4, 1);
      lds_wait_set(L, L_HR + (wave & 4) * 4 + (lane & 3) * 4);
    }
    STAMP(2);
    f32x4 ga[8];
#pragma unroll
    for (int e = 0; e < 8; ++e) ga[e] = f32x4{0.f, 0.f, 0.f, 0.f};
    {
      s16x8 hf = *(LAS s16x8*)(L + D_H + ((wave >> 2) * 7 + 6) * 512 + (g * 8 + (i & 7)) * 16);
#pragma unroll
      for (int q = 6; q >= 0; --q) {
        s16x8 hn = hf;
        if (q > 0) hn = *(LAS s16x8*)(L + D_H + ((wave >> 2) * 7 + q - 1) * 512 + (g * 8 + (i & 7)) * 16);
        mma_frags8(ga, wb + q * 8, hf);
        if (q == 5) {
#pragma unroll
          for (int e = 0; e < 4; ++e) wb[e] = *(LAS u32x4*)(L + D_W + wave * 4352 + e * 1024 + lane * 16);
        }
        SB();
        hf = hn;
      }
    }
    STAMP(4);
    {
      u32 ln = threadIdx.x & 63;
      PIN(ln);
      if ((ln & 15) < 8) {
        const u32 o = D_W + wave * 4352 + (ln & 7) * 64 + (((ln >> 4) ^ ((ln >> 1) & 3)) << 4);
#pragma unroll
        for (int e = 0; e < 8; ++e) *(LAS f32x4*)(L + o + e * 512) = ga[e];
      }
    }
    LGKM0();
    lds_wr(L, L_PW + 4 * wave, 1);
    if (wave >= 4) {
      lds_wait_set(L, L_PW + (wave - 4) * 4);
      {
        u32 ln = threadIdx.x & 63;
        PIN(ln);
        const u32 gq = 2 * (ln >> 5) + ((ln >> 1) & 1), o = D_W + ((ln >> 2) & 7) * 512 + (ln & 1) * 8;
#pragma unroll
        for (int tk = 0; tk < 8; ++tk) {
          const u32 a = o + tk * 64 + ((gq ^ ((tk >> 1) & 3)) << 4);
          const f32x2 y = *(LAS f32x2*)(L + a + (wave - 4) * 4352) + *(LAS f32x2*)(L + a + wave * 4352);
          st_sc1_128(u32x4{__float_as_uint(y.x), tag, __float_as_uint(y.y), tag}, rsWS, WS_YS + (du >> 1) * 65536 + tk * 8192 + (du & 1) * 4096 + (wave - 4) * 1024 + ln * 16);
        }
      }
      LGKM0();
      lds_wr(L, L_HS + 4 * (wave - 4), 1);
      lds_wait_set(L, L_HS + (lane & 3) * 4);
      u32 ln = threadIdx.x & 63;
      PIN(ln);
      vO = (ln >> 3) * 2048 + (((ln & 7) ^ (((ln >> 4) & 3) << 1)) << 4) + cg * 128;
      wo_grp(20 + wave); wo_grp(24 + wave);
      VMCNT(0);
      lds_wr(L, L_HS + 4 * (wave - 4), 2);
    }
    STAMP(8);
  } else {
    const int lr = lane >> 3, lc = lane & 7;
    const int fv = fu - (fu > 2);
    const bool two = fu != 2 && fv < 24;
    const int la = (fu == 2) ? 50 : (fv >= 24) ? (fv - 24) * 8 + 3 : (fv >= 21) ? (fv - 21) * 16 + 2 : (fv / 3) * 8 + ((fv % 3) ? 2 + 2 * (fv % 3) : 0);
    const int lb = la + ((fv >= 21) ? 8 : 1);
    const rsrc_t rsG = mk_rsrc(P.wg, D_ * F_ * 2), rsU = mk_rsrc(P.wu, D_ * F_ * 2), rsX = mk_rsrc(P.x, S_ * D_ * 2);
    const u32 vg = (i < 8 || two) ? ((wave * 128 + 8 * g) * F_ + ((i < 8) ? la : lb) * 64 + 8 * (i & 7)) * 2 : 0x7f000000u;
    u32x4 wb[8 * GWIN];
    auto ldgrp = [&](int q) {
#pragma unroll
      for (int j = 0; j < 8; ++j) wb[(q % GWIN) * 8 + j] = ld_nt_128((q & 1) ? rsU : rsG, vg, ((q >> 1) * 32 + j) * (F_ * 2));
      SB();
    };
    __builtin_amdgcn_s_setprio(0);
    ldgrp(0);
    ldgrp(1);
    if (wave >= 4) { BAR(); tag = (u32)rfl((int)lds_rd(L, L_TAG)); }
    if (fu == 2) {
      *(LAS u32x4*)(L + 32 + threadIdx.x * 16) = u32x4{0u, 0u, 0u, 0u};
      if (threadIdx.x < 4) *(LAS u32x4*)(L + 32 + 8192 + threadIdx.x * 16) = u32x4{0u, 0u, 0u, 0u};
      wait_until(tstart, T_GATHER);
      u32x4 v;
      _Pragma("nounroll") for (int sp = 0; sp < SPIN_CAP; ++sp) {
        v = ld_sc1_128(rsWS, lane * 16, WS_RG + wave * 1024);
        if (__all(v.y == tag && v.w == tag)) break;
        __builtin_amdgcn_s_sleep(2);
      }
      const u32 ib = 2047 - 256 * wave - 4 * lane;
      const u32 key[4] = {(v.x << 16) | ib, (v.x & 0xffff0000u) | (ib - 1), (v.z << 16) | (ib - 2), (v.z & 0xffff0000u) | (ib - 3)};
      lds_wr(L, wave * 4, wave_max_u32(max(max(key[0], key[1]), max(key[2], key[3]))));
      LGKM0();
      BAR();
      STAMP(9);
      const u32x4 m0 = *(LAS u32x4*)L, m1 = *(LAS u32x4*)(L + 16);
      const u32 thr = min(min(min(m0.x, m0.y), min(m0.z, m0.w)), min(min(m1.x, m1.y), min(m1.z, m1.w)));
      const u32 c0 = key[0] >= thr, c1 = key[1] >= thr, c2 = key[2] >= thr, c3 = key[3] >= thr;
      if (c0 | c1 | c2 | c3) {
        const u32 p = 64 + 4 * __hip_atomic_fetch_add((LAS u32*)(L + 32), c0 + c1 + c2 + c3, __ATOMIC_RELAXED, __HIP_MEMORY_SCOPE_WORKGROUP);
        lds_wr(L, c0 ? p : 36, key[0]); lds_wr(L, c1 ? p + 4 * c0 : 36, key[1]);
        lds_wr(L, c2 ? p + 4 * (c0 + c1) : 36, key[2]); lds_wr(L, c3 ? p + 4 * (c0 + c1 + c2) : 36, key[3]);
      }
      LGKM0();
      BAR();
      const int n = rfl((int)lds_rd(L, 32));
      _Pragma("nounroll") for (int b = threadIdx.x; b < n; b += 512) {
        const u32 my = lds_rd(L, 64 + 4 * b);
        int rank = 0;
        _Pragma("nounroll") for (int t = 0; t < n; t += 8) {
          const u32x4 o = *(LAS u32x4*)(L + 64 + 4 * t), q = *(LAS u32x4*)(L + 80 + 4 * t);
          rank += (o.x > my) + (o.y > my) + (o.z > my) + (o.w > my) + (q.x > my) + (q.y > my) + (q.z > my) + (q.w > my);
        }
        if (rank < 8) st_sc1_64(u32x2{my ^ 0x7ffu, tag}, rsWS, WS_SEL + rank * 8);
      }
      if (threadIdx.x == 0) st_sc1_32(tag, rsWS, WS_EPOCH);
      STAMP(10);
      LGKM0();
      BAR();
    }
    constexpr u32 F_XS = 81920;
    ldgrp(2);
    if (wave == 0) {
      wait_until(tstart, T_SEL);
      u32x2 sk;
      _Pragma("nounroll") for (int sp = 0; sp < SPIN_CAP; ++sp) {
        sk = ld_sc1_64(rsWS, lr * 8, WS_SEL);
        if (__all(sk.y == tag)) break;
        __builtin_amdgcn_s_sleep(4);
      }
      STAMP(2);
      u32x4 xr[16];
#pragma unroll
      for (int j = 0; j < 16; ++j) xr[j] = __builtin_amdgcn_raw_buffer_load_b128(rsX, (sk.x & 0x7ffu) * 2048 + lc * 16, j * 128, 0);
      u32x2 ms;
      _Pragma("nounroll") for (int sp = 0; sp < SPIN_CAP; ++sp) {
        ms = ld_sc1_64(rsWS, (sk.x & 0x7ffu) * 8, WS_SG);
        if (__all(ms.y == tag)) break;
      }
#pragma unroll
      for (int j = 0; j < 16; ++j) *(LAS u32x4*)(L + F_XS + lr * 2064 + j * 128 + lc * 16) = xr[j];
      *(LAS u32*)(L + F_XS + 8 * 2064 + lr * 4) = ms.x;
      LGKM0();
      lds_wr(L, L_HR, 1);
    }
#pragma unroll
    for (int q = 3; q < GWIN; ++q) ldgrp(q);
#pragma unroll
    for (int q = 0; q < GPRE; ++q) transpose_grp(wb + 8 * q);
    lds_wait_set(L, L_HR);
    f32x4 ga[16];
#pragma unroll
    for (int e = 0; e < 16; ++e) ga[e] = f32x4{0.f, 0.f, 0.f, 0.f};
    const u32 ms = *(LAS u32*)(L + F_XS + 8 * 2064 + (i & 7) * 4);
    const float rsn = __builtin_amdgcn_rcpf(bf_hi(ms)), mrs = -bf_lo(ms) * rsn;
    s16x8 xf;
#pragma unroll
    for (int q = 0; q < 8; ++q) {
      if ((q & 1) == 0) {
        const u32x4 xv = *(LAS u32x4*)(L + F_XS + (i & 7) * 2064 + (wave * 128 + (q >> 1) * 32 + 8 * g) * 2);
        u32x4 xn;
#pragma unroll
        for (int e = 0; e < 4; ++e) xn[e] = pack2(__builtin_fmaf(bf_lo(xv[e]), rsn, mrs), __builtin_fmaf(bf_hi(xv[e]), rsn, mrs));
        xf = as_frag(xn);
      }
      if (q < GPRE) mma_frags8(ga + 8 * (q & 1), wb + q * 8, xf); else mma_cols(ga + 8 * (q & 1), wb + (q % GWIN) * 8, xf);
      SB();
      if (q + GWIN < 8) ldgrp(q + GWIN);
    }
    STAMP(4);
    {
      u32 ln = threadIdx.x & 63;
      PIN(ln);
      if ((ln & 15) < 8) {
        const u32 o = wave * 10240 + (ln & 7) * 80 + (ln >> 4) * 16;
#pragma unroll
        for (int e = 0; e < 16; ++e) *(LAS f32x4*)(L + o + e * 640) = ga[e];
      }
    }
    LGKM0();
    BAR();
    {
      u32 ln = threadIdx.x & 63;
      PIN(ln);
      const u32 e = 4 * ((wave >> 1) & 1) + (ln >> 4), tk = (ln >> 1) & 7, m = 8 * (wave >> 2) + 4 * (wave & 1) + 2 * (ln & 1);
      const u32 o = (e * 8 + tk) * 80 + m * 4;
      f32x2 gs = {0.f, 0.f}, us = {0.f, 0.f};
#pragma unroll
      for (int w = 0; w < 8; ++w) { gs += *(LAS f32x2*)(L + w * 10240 + o); us += *(LAS f32x2*)(L + w * 10240 + 5120 + o); }
      const float g0 = rbf(gs.x), g1 = rbf(gs.y);
      const u32 hv = pack2(rbf(g0 * sigm(g0)) * rbf(us.x), rbf(g1 * sigm(g1)) * rbf(us.y));
      if (wave < 4 || two) st_sc1_64(u32x2{hv, tag}, rsWS, WS_H + ((wave < 4) ? la : lb) * 2048 + (wave & 3) * 512 + ln * 8);
    }
    LGKM0();
    BAR();
    STAMP(8);
    if (wave >= 4) { helper_offsets(); helper_fill(); }
  }
  }

  if (wave >= 4) {
    const int lid = wave - 4;
    if (!dwn) {
      VMCNT(0);
      STAMP(3);
      BAR();
    }
    if (lid >= 2) {
      const u32 sk = lds_rd(L, L_SEL + 4 * (lane & 7));
      _Pragma("nounroll") for (int c = lid - 2; c < 8; c += 2) {
        const u32 tk = (u32)__builtin_amdgcn_readlane((int)sk, c) & 0x7ffu;
        if ((int)(tk >> 7) != pan2) continue;
        float y0, y1;
        _Pragma("nounroll") for (int sp = 0; sp < SPIN_CAP; ++sp) {
          u32 bad = 0;
          y0 = 0.f; y1 = 0.f;
#pragma unroll
          for (int j = 0; j < 4; ++j) {
            const u32x4 v = ld_sc1_128(rsWS, (lane >> 5) * 65536 + (lane & 31) * 16, WS_YS + j * 131072 + c * 8192 + cg * 512);
            bad |= (v.y ^ tag) | (v.w ^ tag);
            y0 += __uint_as_float(v.x); y1 += __uint_as_float(v.z);
          }
          if (__all(bad == 0)) break;
          __builtin_amdgcn_s_sleep(8);
        }
        y0 = sum32(y0); y1 = sum32(y1);
        if (lane < 32) {
          const u32 o = L_UPD + (c * 64 + 16 * (lane & 3) + (lane >> 2)) * 4;
          *(LAS float*)(L + o) = y0; *(LAS float*)(L + o + 32) = y1;
        }
      }
      LGKM0();
      lds_wr(L, L_UPDF + 4 * (lid - 2), 1);
      STAMP(7);
    }
    return;
  }

  {
    const bool pol = wave == 0;
    const u32 fo = WS_OF + (((pan2 * 4 + 3 * (lane >> 5)) / 5) * 16 + (lane & 15)) * 128;
    u32 fv = ~tag;
    if (dwn && pol) fv = ld_sc1_32(rsWS, fo, 0);
    SB();
    if (!gemm && !dwn) BAR();
    oc_init();
    SB();
    if (gemm) {
      VMCNT(0);
      if (lane == 0 && __hip_atomic_fetch_add((LAS u32*)(L + L_ARR), 1u, __ATOMIC_RELAXED, __HIP_MEMORY_SCOPE_WORKGROUP) == 3)
        st_sc1_32(tag, rsWS, WS_OF + (pan * 16 + hp) * 128);
      STAMP(4);
      SB();
    }
    const int par2 = wave >> 1, t2 = (wave & 1) * 2;
    const int q4 = i >> 2, p4 = i & 3;
    const u32 wb = lbase + (4 * g + q4) * 128 + p4 * 8 + par2 * 2048, wsw = (2 * g + (q4 >> 1)) & 3;
    u32 wa[4], wc[4];
#pragma unroll
    for (int c = 0; c < 4; ++c) { wa[c] = wb + ((c ^ wsw) << 5); wc[c] = wa[c] ^ 2048; PIN(wa[c]); PIN(wc[c]); }
    const int T0 = pan2 * 4 + t2;
    if (gemm) BAR();
    s16x8 wf[3][4];
#pragma unroll
    for (int k = 0; k < 2; ++k)
#pragma unroll
      for (int c = 0; c < 4; ++c) wf[k][c] = tr_frag(LP(wa[c]), LP(wc[c]), k * 4096, k * 4096);
    const u32 sk = lds_rd(L, L_SEL + 4 * (lane & 7));
    __builtin_amdgcn_s_waitcnt(0xc07f);
    SB();
    if (!gemm) wait_until(tstart, T_O);
    if (pol) {
      _Pragma("nounroll") for (int sp = 0; sp < SPIN_CAP; ++sp) {
        if (__all(fv == tag)) break;
        if (sp) __builtin_amdgcn_s_sleep(2);
        fv = ld_sc1_32(rsWS, fo, 0);
      }
      lds_wr(L, L_GO, 1);
    } else {
      _Pragma("nounroll") for (int sp = 0; sp < SPIN_CAP; ++sp) if (rfl((int)lds_rd(L, L_GO))) break;
    }
    CBAR();
    STAMP(5);
    const u32 abase = WS_O + (par2 * 32 * 64 + T0) * 1024;
    u32x4 af[OWIN][2];
#pragma unroll
    for (int hh = 0; hh < OWIN; ++hh) {
#pragma unroll
      for (int tt = 0; tt < 2; ++tt) af[hh][tt] = ld_sc1_128(rsWS, lane * 16 + tt * 1024, abase + ((hh + hrot) & 31) * 65536);
      SB();
    }
#pragma unroll
    for (int hh = 0; hh < 32; ++hh) {
      if (hh == 21 && dwn) {
        _Pragma("nounroll") for (int sp = 0; sp < SPIN_CAP; ++sp) if (__all(lds_rd(L, L_HS + (lane & 3) * 4) == 2u)) break;
        CBAR(); SB();
      }
      if (hh == 14) {
#pragma unroll
        for (int c = 0; c < 4; ++c) { wa[c] += 65536; wc[c] += 65536; PIN(wa[c]); PIN(wc[c]); }
        SB();
      }
      if (hh == 31) __builtin_amdgcn_s_waitcnt(0xc07f); else __builtin_amdgcn_s_waitcnt(0xc87f);
      SB();
      if (hh + 2 < 32) {
#pragma unroll
        for (int c = 0; c < 4; ++c) wf[(hh + 2) % 3][c] = tr_frag(LP(wa[c]), LP(wc[c]), ((hh + 2) & 15) * 4096, ((hh + 2) & 15) * 4096);
      }
#pragma unroll
      for (int tt = 0; tt < 2; ++tt) {
#pragma unroll
        for (int c = 0; c < 4; ++c) oc[tt][c] = mfma(wf[hh % 3][c], as_frag(af[hh % OWIN][tt]), oc[tt][c]);
        if (hh + OWIN < 32) af[hh % OWIN][tt] = ld_sc1_128(rsWS, lane * 16 + tt * 1024, abase + ((hh + OWIN + hrot) & 31) * 65536);
      }
#pragma unroll
      for (int k = 0; k < 2; ++k) {
        __builtin_amdgcn_sched_group_barrier(0x008, 1, 0); __builtin_amdgcn_sched_group_barrier(0x100, 2, 0);
        __builtin_amdgcn_sched_group_barrier(0x008, 1, 0); __builtin_amdgcn_sched_group_barrier(0x100, 1, 0);
        __builtin_amdgcn_sched_group_barrier(0x008, 1, 0); __builtin_amdgcn_sched_group_barrier(0x100, 1, 0);
        __builtin_amdgcn_sched_group_barrier(0x008, 1, 0); __builtin_amdgcn_sched_group_barrier(0x020, 1, 0);
      }
      SB();
    }
    STAMP(6);
    {
      const u32 stl = (sk & 0x7ffu) - (u32)(pan2 * 128 + par2 + 32 * t2);
      u64 hit = __ballot(stl < 64u && (stl & 1u) == 0u) & 0xffull;
      if (hit) {
        lds_wait_set(L, L_UPDF + (lane & 1) * 4);
        while (hit) {
          const int c = __builtin_ctzll(hit);
          hit &= hit - 1;
          const u32 tl = (u32)__builtin_amdgcn_readlane((int)stl, c), ob = (u32)__builtin_amdgcn_readlane((int)sk, c) >> 16;
          const float sg = rbf(sigm(bf_lo((ob & 0x8000u) ? (ob & 0x7fffu) : (~ob & 0xffffu))));
          const float f0 = ((tl >> 1) == (u32)i) ? sg : 0.f, f1 = ((tl >> 1) == (u32)i + 16u) ? sg : 0.f;
#pragma unroll
          for (int cc = 0; cc < 4; ++cc) {
            const f32x4 u = *(LAS f32x4*)(L + L_UPD + (c * 64 + 16 * cc + 4 * g) * 4);
            oc[0][cc] += u * f0; oc[1][cc] += u * f1;
          }
        }
      }
    }
    const rsrc_t rsO = mk_rsrc(P.out, S_ * D_ * 2);
    const u32 xo = ((pan2 * 128 + par2 + 32 * t2 + 2 * i) * D_ + cg * 64 + (g & 1) * 16 + (g >> 1) * 8) * 2;
#pragma unroll
    for (int tt = 0; tt < 2; ++tt)
#pragma unroll
      for (int cp = 0; cp < 2; ++cp) {
        const u32 a0 = pack2(oc[tt][2 * cp][0], oc[tt][2 * cp][1]), a1 = pack2(oc[tt][2 * cp][2], oc[tt][2 * cp][3]);
        const u32 b0 = pack2(oc[tt][2 * cp + 1][0], oc[tt][2 * cp + 1][1]), b1 = pack2(oc[tt][2 * cp + 1][2], oc[tt][2 * cp + 1][3]);
        const auto e0 = __builtin_amdgcn_permlane16_swap(a0, b0, false, false);
        const auto e1 = __builtin_amdgcn_permlane16_swap(a1, b1, false, false);
        st_sc1_128(u32x4{e0[0], e1[0], e0[1], e1[1]}, rsO, xo + tt * 65536 + cp * 64);
      }
    STAMP(7);
  }
}

static u64 k_order(int odd, int pl, int hl) {
  const int np = 7 - odd;
  int slot[16], used[16];
  for (int k = 0; k < 16; ++k) { slot[k] = -1; used[k] = 0; }
  for (int k = 2; k < 16; ++k) {
    const int h0 = k & 3, p0 = (k * (odd ? 5 : 3)) % np;
    int want = -1;
    if (hl == h0 && pl % np == p0) want = k - 2; else if (hl == h0 || pl % np == p0) want = k - 1;
    if (want >= 0) {
      if (want < 1) want = 1;
      while (want > 1 && used[want]) --want;
      if (!used[want]) { slot[k] = want; used[want] = 1; }
    }
  }
  int s = 0;
  for (int k = 0; k < 16; ++k) if (slot[k] < 0) { while (used[s]) ++s; slot[k] = s; used[s] = 1; }
  u64 r = 0;
  for (int k = 0; k < 16; ++k) r |= (u64)k << (4 * slot[k]);
  return r;
}

extern "C" void kernel_launch(void* const* d_in, const int* in_sizes, int n_in, void* d_out, int out_size, void* d_ws, size_t ws_size,
                              hipStream_t stream) {
  Args A;
  Params& P = A.p;
  P.x = (const u16*)d_in[0]; P.wqkv = (const u16*)d_in[1]; P.wo = (const u16*)d_in[2]; P.wr = (const u16*)d_in[4];
  P.wg = (const u16*)d_in[5]; P.wu = (const u16*)d_in[6]; P.wd = (const u16*)d_in[7];
  P.out = (u16*)d_out; P.ws = (char*)d_ws;
  for (int e = 0; e < 64; ++e) A.kt[e] = k_order(e >> 5, (e >> 2) & 7, e & 3);
  (void)hipFuncSetAttribute((const void*)fused_kernel, hipFuncAttributeMaxDynamicSharedMemorySize, L_END + 64);
  hipLaunchKernelGGL(fused_kernel, dim3(256), dim3(512), L_END + 64, stream, A);
}
```

```cpp
#include <hip/hip_runtime.h>
#include <stdint.h>

typedef unsigned short u16;
typedef unsigned u32;
typedef unsigned long long u64;
typedef __attribute__((ext_vector_type(4))) short s16x4;
typedef __attribute__((ext_vector_type(8))) short s16x8;
typedef __attribute__((ext_vector_type(2))) float f32x2;
typedef __attribute__((ext_vector_type(4))) float f32x4;
typedef __attribute__((ext_vector_type(4))) unsigned u32x4;
typedef __attribute__((ext_vector_type(2))) unsigned u32x2;
typedef __attribute__((ext_vector_type(16))) unsigned u32x16;
typedef __attribute__((ext_vector_type(2))) __bf16 bf16x2;
typedef __amdgpu_buffer_rsrc_t rsrc_t;
#define LAS __attribute__((address_space(3)))
#define GAS __attribute__((address_space(1)))
#define DEV __device__ __forceinline__

#define STAMPS 0
constexpr int OWIN = 6;
constexpr int GWIN = 5, GPRE = 3;
constexpr int T_GATHER = 350, T_SEL = 580, T_H = 1100, T_O = 1300;
constexpr int WARM_AT = 10;
constexpr u64 HELPK = 0x765432ull, ROLLK = 0xfedcba98ull;
constexpr int HELPN = 6, RS0 = 1, RSN = 8;
constexpr int CODE_LINES = 341;

constexpr int S_ = 2048, D_ = 1024, F_ = 3584;
constexpr u32 WS_EPOCH = 0;
constexpr u32 WS_SEL = 4096;
constexpr u32 WS_RG = 16384;
constexpr u32 WS_SG = 32768;
constexpr u32 WS_OF = 65536;
constexpr u32 WS_H = WS_OF + 131072;
constexpr u32 WS_YS = WS_H + 131072;
constexpr u32 WS_O = WS_YS + 524288;
constexpr u32 WS_END = WS_O + S_ * D_ * 2;
constexpr u32 WS_STAMP = 16u << 20;
constexpr u32 WOFF = 22528, SLOT = 47104;
constexpr u32 L_HX = 131072;
constexpr u32 L_XT = 3 * SLOT;
constexpr u32 L_STQ = L_XT + 16384;
constexpr u32 L_STO = L_STQ + 1408;
constexpr u32 L_UPD = L_STO + 1024;
constexpr u32 L_ONE = L_UPD + 2048;
constexpr u32 L_CTL = L_ONE + 128;
constexpr u32 L_TAG = L_CTL, L_ARR = L_CTL + 4, L_UPDF = L_CTL + 8  , L_HS = L_CTL + 16  , L_SEL = L_CTL + 32  ;
constexpr u32 L_PW = L_CTL + 64, L_HR = L_CTL + 96;
constexpr u32 L_GO = L_CTL + 128;
constexpr u32 L_END = L_CTL + 192;
constexpr int SPIN_CAP = 1 << 22;
static_assert(L_END <= 163840 && L_ONE % 128 == 0, "LDS");

struct Params {
  const u16 *x, *wqkv, *wo, *wr, *wg, *wu, *wd;
  u16* out;
  char* ws;
};
struct Args {
  Params p;
  u64 kt[64];
};

DEV float bf_lo(u32 v) { return __uint_as_float(v << 16); }
DEV float bf_hi(u32 v) { return __uint_as_float(v & 0xffff0000u); }
DEV u32 pack2(float a, float b) { bf16x2 p = {(__bf16)a, (__bf16)b}; return __builtin_bit_cast(u32, p); }
DEV float rbf(float a) { return bf_lo(pack2(a, a)); }
DEV float dot2(u32 a, u32 b, float c) { return __builtin_amdgcn_fdot2_f32_bf16(__builtin_bit_cast(bf16x2, a), __builtin_bit_cast(bf16x2, b), c, false); }
DEV s16x8 as_frag(u32x4 v) { return __builtin_bit_cast(s16x8, v); }
DEV f32x4 mfma(s16x8 a, s16x8 b, f32x4 c) { return __builtin_amdgcn_mfma_f32_16x16x32_bf16(a, b, c, 0, 0, 0); }
DEV int rfl(int v) { return __builtin_amdgcn_readfirstlane(v); }
template <int CTRL, int RM, bool BC> DEV float dppf(float v) { return __int_as_float(__builtin_amdgcn_update_dpp(0, __float_as_int(v), CTRL, RM, 0xf, BC)); }
template <int CTRL, int RM, bool BC> DEV u32 dppu(u32 v) { return (u32)__builtin_amdgcn_update_dpp(0, (int)v, CTRL, RM, 0xf, BC); }
DEV float half_sum(float v) {
  v += dppf<0x111, 0xf, true>(v); v += dppf<0x112, 0xf, true>(v); v += dppf<0x114, 0xf, true>(v); v += dppf<0x118, 0xf, true>(v);
  v += dppf<0x142, 0xa, false>(v);
  return v;
}
DEV u32 wave_max_u32(u32 v) {
  v = max(v, dppu<0x111, 0xf, true>(v)); v = max(v, dppu<0x112, 0xf, true>(v)); v = max(v, dppu<0x114, 0xf, true>(v)); v = max(v, dppu<0x118, 0xf, true>(v));
  v = max(v, dppu<0x142, 0xa, false>(v)); v = max(v, dppu<0x143, 0xc, false>(v));
  return (u32)__builtin_amdgcn_readlane((int)v, 63);
}
DEV float vmax(float a, float b) { return __builtin_elementwise_maximum(a, b); }
DEV float vmax3(float a, float b, float c) { return vmax(vmax(a, b), c); }
DEV float max16(float v) { u32 a = __float_as_uint(v); auto r = __builtin_amdgcn_permlane16_swap(a, a, false, false); u32 x = r[0], y = r[1]; return vmax(__uint_as_float(x), __uint_as_float(y)); }
DEV float max32(float v) { u32 a = __float_as_uint(v); auto r = __builtin_amdgcn_permlane32_swap(a, a, false, false); u32 x = r[0], y = r[1]; return vmax(__uint_as_float(x), __uint_as_float(y)); }
DEV float sum32(float v) { u32 a = __float_as_uint(v); auto r = __builtin_amdgcn_permlane32_swap(a, a, false, false); u32 x = r[0], y = r[1]; return __uint_as_float(x) + __uint_as_float(y); }
DEV u32 ord16(u32 b) { return (b & 0x8000u) ? (~b & 0xffffu) : (b | 0x8000u); }
DEV float sigm(float v) { return __builtin_amdgcn_rcpf(1.f + __builtin_amdgcn_exp2f(-v * 1.4426950408889634f)); }

DEV rsrc_t mk_rsrc(const void* p, u32 bytes) { return __builtin_amdgcn_make_buffer_rsrc((void*)p, 0, (int)bytes, 0x00020000); }
DEV u32x4 ld_nt_128(rsrc_t rs, u32 voff, u32 soff) { return __builtin_amdgcn_raw_buffer_load_b128(rs, voff, soff, 2); }
DEV u32x4 ld_sc1_128(rsrc_t rs, u32 voff, u32 soff) { return __builtin_amdgcn_raw_buffer_load_b128(rs, voff, soff, 16); }
DEV u32x2 ld_sc1_64(rsrc_t rs, u32 voff, u32 soff) { return __builtin_amdgcn_raw_buffer_load_b64(rs, voff, soff, 16); }
DEV u32 ld_sc1_32(rsrc_t rs, u32 voff, u32 soff) { return __builtin_amdgcn_raw_buffer_load_b32(rs, voff, soff, 16); }
DEV void st_sc1_128(u32x4 v, rsrc_t rs, u32 voff) { __builtin_amdgcn_raw_buffer_store_b128(v, rs, voff, 0, 16); }
DEV void st_sc1_64(u32x2 v, rsrc_t rs, u32 voff) { __builtin_amdgcn_raw_buffer_store_b64(v, rs, voff, 0, 16); }
DEV void st_sc1_32(u32 v, rsrc_t rs, u32 voff) { __builtin_amdgcn_raw_buffer_store_b32(v, rs, voff, 0, 16); }
#define VMCNT(n) asm volatile("s_waitcnt vmcnt(" #n ")" ::: "memory")
#define LGKM0() asm volatile("s_waitcnt lgkmcnt(0)" ::: "memory")
#define CBAR() asm volatile("" ::: "memory")
#define BAR() do { CBAR(); __builtin_amdgcn_s_barrier(); CBAR(); } while (0)
#define SB() __builtin_amdgcn_sched_barrier(0)
#define LP(a) ((LAS char*)(uintptr_t)(a))
#define PIN(x) asm volatile("" : "+v"(x))

#define TICK() 0ull
#define RT() 0ull
#define STAMP(k) do {} while (0)
#define CSTAMP(k) do {} while (0)
#define LSTAMP(k) do {} while (0)

DEV u32 lds_rd(LAS char* L, u32 off) { return *(volatile LAS u32*)(L + off); }
DEV void lds_wr(LAS char* L, u32 off, u32 v) { *(volatile LAS u32*)(L + off) = v; }
DEV void lds_wait_set(LAS char* L, u32 off) {
  _Pragma("nounroll") for (int sp = 0; sp < SPIN_CAP; ++sp) {
    if (__all(lds_rd(L, off) != 0u)) break;
    __builtin_amdgcn_s_sleep(1);
  }
  CBAR();
}
DEV void wait_until(u64 t0, u32 ticks) {
  while (__builtin_amdgcn_s_memrealtime() - t0 < ticks) __builtin_amdgcn_s_sleep(4);
}
#define DMA_HEAD "s_mov_b32 m0, %0\n\ts_nop 0\n\t"
DEV void dma16(u32 lds, u32 voff, rsrc_t rs, u32 soff) {
  asm volatile(DMA_HEAD "buffer_load_dwordx4 %1, %2, %3 offen lds" ::"s"(lds), "v"(voff), "s"(rs), "s"(soff) : "memory");
}
DEV void dma16x4(u32 lds, u32 v0, u32 v1, u32 v2, u32 v3, rsrc_t rs, u32 soff) {
  asm volatile(DMA_HEAD "buffer_load_dwordx4 %1, %5, %6 offen lds\n\tbuffer_load_dwordx4 %2, %5, %6 offen offset:1024 lds\n\t"
               "buffer_load_dwordx4 %3, %5, %6 offen offset:2048 lds\n\tbuffer_load_dwordx4 %4, %5, %6 offen offset:3072 lds"
               ::"s"(lds), "v"(v0), "v"(v1), "v"(v2), "v"(v3), "s"(rs), "s"(soff) : "memory");
}
DEV void dma16x2(u32 lds, u32 v0, u32 v1, rsrc_t rs, u32 soff) {
  asm volatile(DMA_HEAD "buffer_load_dwordx4 %1, %3, %4 offen lds\n\tbuffer_load_dwordx4 %2, %3, %4 offen offset:1024 lds"
               ::"s"(lds), "v"(v0), "v"(v1), "s"(rs), "s"(soff) : "memory");
}

struct Frags { s16x8 x[6]; s16x8 w[6]; };

DEV s16x8 tr_frag(LAS char* p, LAS char* q, u32 o0, u32 o1) {
  s16x4 a = __builtin_amdgcn_ds_read_tr16_b64_v4i16((LAS s16x4*)(p + o0));
  s16x4 b = __builtin_amdgcn_ds_read_tr16_b64_v4i16((LAS s16x4*)(q + o1));
  return __builtin_shufflevector(a, b, 0, 1, 2, 3, 4, 5, 6, 7);
}
template <int KK> DEV void load_frags(Frags& f, LAS char* aH, LAS char* aT, LAS char* w0, LAS char* w1) {
#pragma unroll
  for (int t = 0; t < 5; ++t) f.x[1 + t] = *(LAS s16x8*)(aT + t * 2048);
  f.x[0] = *(LAS s16x8*)aH;
#pragma unroll
  for (int sec = 0; sec < 3; ++sec) {
    f.w[2 * sec] = tr_frag(w0, w0, sec * 8192 + KK * 4096, sec * 8192 + KK * 4096 + 512);
    f.w[2 * sec + 1] = tr_frag(w1, w1, sec * 8192 + KK * 4096, sec * 8192 + KK * 4096 + 512);
  }
}
template <int A, int B> DEV void mma_range(f32x4 (&acc)[6][6], const Frags& f, s16x8 e0) {
#pragma unroll
  for (int n = A; n < B; ++n) {
    const int p = n / 11, r = n % 11, w = 2 * p + (r >= 6), t = (r >= 6) ? 11 - r : 5 - r;
    acc[w][t] = (w < 4) ? mfma(f.w[w], (w == 0 && t == 0) ? e0 : f.x[t], acc[w][t]) : mfma(f.x[t], f.w[w], acc[w][t]);
  }
}
DEV void interleave() {
#pragma unroll
  for (int k = 0; k < 6; ++k) { __builtin_amdgcn_sched_group_barrier(0x008, 1, 0); __builtin_amdgcn_sched_group_barrier(0x100, 1, 0); }
#pragma unroll
  for (int k = 0; k < 6; ++k) {
    __builtin_amdgcn_sched_group_barrier(0x008, 1, 0); __builtin_amdgcn_sched_group_barrier(0x100, 1, 0);
    __builtin_amdgcn_sched_group_barrier(0x008, 1, 0); __builtin_amdgcn_sched_group_barrier(0x100, 1, 0);
    __builtin_amdgcn_sched_group_barrier(0x008, 1, 0);
  }
}

template <int E> DEV s16x8 col_frag(const u32x4* L) {
  constexpr u32 sel = (E & 1) ? 0x07060302u : 0x05040100u;
  u32x4 r;
  r.x = __builtin_amdgcn_perm(L[1][E >> 1], L[0][E >> 1], sel);
  r.y = __builtin_amdgcn_perm(L[3][E >> 1], L[2][E >> 1], sel);
  r.z = __builtin_amdgcn_perm(L[5][E >> 1], L[4][E >> 1], sel);
  r.w = __builtin_amdgcn_perm(L[7][E >> 1], L[6][E >> 1], sel);
  return as_frag(r);
}
DEV void mma_cols(f32x4* acc, const u32x4* L, s16x8 b) {
  acc[0] = mfma(col_frag<0>(L), b, acc[0]); acc[1] = mfma(col_frag<1>(L), b, acc[1]);
  acc[2] = mfma(col_frag<2>(L), b, acc[2]); acc[3] = mfma(col_frag<3>(L), b, acc[3]);
  acc[4] = mfma(col_frag<4>(L), b, acc[4]); acc[5] = mfma(col_frag<5>(L), b, acc[5]);
  acc[6] = mfma(col_frag<6>(L), b, acc[6]); acc[7] = mfma(col_frag<7>(L), b, acc[7]);
}

DEV void transpose_grp(u32x4* L) {
  const s16x8 f0 = col_frag<0>(L), f1 = col_frag<1>(L), f2 = col_frag<2>(L), f3 = col_frag<3>(L), f4 = col_frag<4>(L), f5 = col_frag<5>(L), f6 = col_frag<6>(L), f7 = col_frag<7>(L);
  L[0] = __builtin_bit_cast(u32x4, f0); L[1] = __builtin_bit_cast(u32x4, f1); L[2] = __builtin_bit_cast(u32x4, f2); L[3] = __builtin_bit_cast(u32x4, f3);
  L[4] = __builtin_bit_cast(u32x4, f4); L[5] = __builtin_bit_cast(u32x4, f5); L[6] = __builtin_bit_cast(u32x4, f6); L[7] = __builtin_bit_cast(u32x4, f7);
#pragma unroll
  for (int e = 0; e < 8; ++e) PIN(L[e]);
  SB();
}
DEV void mma_frags8(f32x4* acc, const u32x4* L, s16x8 b) {
#pragma unroll
  for (int e = 0; e < 8; ++e) acc[e] = mfma(as_frag(L[e]), b, acc[e]);
}
DEV void warm_line(u32 lds, const char* a) {
  asm volatile(DMA_HEAD "global_load_lds_dword %1, off" ::"s"(lds), "v"(a) : "memory");
}

__global__ void __launch_bounds__(512, 2) fused_kernel(Args Parg) {
  extern __shared__ __attribute__((aligned(128))) char smem[];
  u32x16 ka;
  u64 kb;
  asm volatile("s_load_dwordx16 %0, %2, 0x0\n\ts_load_dwordx2 %1, %2, 0x40" : "=&s"(ka), "=&s"(kb) : "s"(__builtin_amdgcn_kernarg_segment_ptr()));
  Params P;
  const char* pc0;
  asm volatile("s_getpc_b64 %0" : "=s"(pc0));
  LAS char* L = (LAS char*)smem;
  const u32 lbase = (u32)(uintptr_t)L;
  const int lane = threadIdx.x & 63;
  const int wave = rfl(threadIdx.x >> 6);
  const int bid = blockIdx.x, xg = bid & 7, loc = bid >> 3;
  const int np = 7 - (xg & 1);
  const bool gemm = loc < 4 * np;
  const int pl = loc >> 2, hl = loc & 3;
  const int pan = (xg & 1) * 7 + pl, hp = (xg >> 1) * 4 + hl, r0 = pan * 160;
  const int fi = loc - 4 * np;
  const bool dwn = (u32)(fi - 2 - 3 * (xg & 1)) <= 2u * (xg & 1);
  const int fu = (xg & 1) * 12 + (xg >> 1) * (3 + 2 * (xg & 1)) + min(fi, 2 + 2 * (xg & 1));
  const int du = (xg & 1) ? 4 + (xg >> 1) * 3 + fi - 5 : (xg >> 1);
  const int pan2 = (xg & 1) * 8 + (xg >> 1) * 2 + (loc >> 4);
  const int cg = (int)((((loc & 16) ? 0xfedc0a987653421bull : 0xfedc8a9b76534210ull) >> (4 * (loc & 15))) & 15), hrot = 2 * cg;
  const int g = lane >> 4, i = lane & 15;
  u64 kord;
  asm volatile("s_load_dwordx2 %0, %1, %2" : "=&s"(kord) : "s"(__builtin_amdgcn_kernarg_segment_ptr()), "s"(72 + (((xg & 1) * 32 + loc) & 63) * 8));
  rsrc_t rsWS;
  const u64 tstart = __builtin_amdgcn_s_memrealtime();
  u32 tag = 0;
  f32x4 oc[2][4];
  auto oc_init = [&]() {
    const int tl = (wave >> 1) + 64 * (wave & 1) + 2 * i;
    f32x2 st[2];
    u32x2 v[2][4];
#pragma unroll
    for (int tt = 0; tt < 2; ++tt) {
      st[tt] = *(LAS f32x2*)(L + L_STO + tl * 8 + tt * 256);
#pragma unroll
      for (int c = 0; c < 4; ++c) v[tt][c] = *(LAS u32x2*)(L + L_XT + tl * 128 + (((2 * c + (g >> 1)) ^ (i & 7)) << 4) + (g & 1) * 8 + tt * 4096);
    }
    SB();
#pragma unroll
    for (int tt = 0; tt < 2; ++tt) {
      const float k1 = 1.f + st[tt].y, k0 = -st[tt].x * st[tt].y;
#pragma unroll
      for (int c = 0; c < 4; ++c) oc[tt][c] = f32x4{bf_lo(v[tt][c].x), bf_hi(v[tt][c].x), bf_lo(v[tt][c].y), bf_hi(v[tt][c].y)} * k1 + k0;
    }
#pragma unroll
    for (int q = 0; q < 8; ++q) PIN(oc[q >> 2][q & 3]);
  };

  u32 tq[3], vO, vx;
  auto helper_offsets = [&]() {
    u32 lane = threadIdx.x & 63;
    PIN(lane);
    const int lr = lane >> 3, lc = lane & 7;
#pragma unroll
    for (int j = 0; j < 3; ++j) {
      const int rho = min(64 * j + (int)lane, 175), pr = rho >= 96;
      const int tk = r0 + ((rho < 16) ? rho - 16 : pr + 2 * (rho - 16 - 80 * pr));
      const u32 a = (u32)(pan2 * 128 + (rho & 127)) * 8, b = (((u32)tk >= (u32)S_) | (rho == 0)) ? 1u : (u32)tk * 8;
      tq[j] = (wave == 7) ? a : b;
      PIN(tq[j]);
    }
    vO = lr * 2048 + ((u32)(lc ^ (((lr >> 1) & 3) << 1)) << 4) + cg * 128;
    vx = lr * 2048 + ((u32)(lc ^ (lr >> 1)) << 4) + cg * 128;
    PIN(vO); PIN(vx);
  };
  auto wo_grp = [&](int q) { dma16x4(lbase + q * 4096, vO, vO + 15 * 1024, vO + 30 * 1024, vO + 45 * 1024, mk_rsrc(P.wo, D_ * D_ * 2), ((q + hrot) & 31) * 65536); };
  auto helper_fill = [&]() {
    const int lid = wave - 4;
    tag = (u32)rfl((int)lds_rd(L, L_TAG));
    if (gemm) { VMCNT(11); BAR(); }
    if (lid >= 2) {
      u32x2 sv[3];
#pragma unroll
      for (int q = 2 * lid - 4; q < 2 * lid - 2; ++q) dma16x4(lbase + L_XT + q * 4096, vx, (vx ^ 64) + 15 * 1024, vx + 30 * 1024, (vx ^ 64) + 45 * 1024, mk_rsrc(P.x, S_ * D_ * 2), (pan2 * 128 + 32 * q) * 2048);
#pragma unroll
      for (int j = 0; j < 3; ++j) sv[j] = ld_sc1_64(rsWS, tq[j] & ~7u, WS_SG);
      SB();
      if (gemm) { VMCNT(11); BAR(); }
      _Pragma("nounroll") for (int sp = 0; sp < SPIN_CAP; ++sp) {
        if (__all(sv[0].y == tag && sv[1].y == tag && sv[2].y == tag)) break;
        __builtin_amdgcn_s_sleep(2);
#pragma unroll
        for (int j = 0; j < 3; ++j) sv[j] = ld_sc1_64(rsWS, tq[j] & ~7u, WS_SG);
      }
      f32x2 st[3];
      u32 ta = lbase + ((lid == 3) ? L_STO + lane * 8 : L_STQ + lane * 4), tb2 = ta + ((lid == 3) ? 4 : 704);
      u32 tc = (lid == 2 && lane < 48) ? ta + 512 : lbase + L_UPD + lane * 4, td = (lid == 2 && lane < 48) ? tb2 + 512 : tc;
      const u32 ts = (lid == 3) ? 512 : 256;
#pragma unroll
      for (int j = 0; j < 3; ++j) {
        st[j] = f32x2{__uint_as_float((sv[j].x << 16) ^ ((lid == 3) ? 0u : 0x80000000u)), __builtin_amdgcn_rcpf(bf_hi(sv[j].x))};
        if (tq[j] & 1u) st[j] = f32x2{0.f, 0.f};
        PIN(st[j]);
      }
      u32 te = ta + ts, tf = tb2 + ts;
      PIN(ta); PIN(tb2); PIN(tc); PIN(td); PIN(te); PIN(tf);
      VMCNT(0);
      if (gemm) BAR();
      *(LAS float*)LP(ta) = st[0].x; *(LAS float*)LP(tb2) = st[0].y;
      *(LAS float*)LP(te) = st[1].x; *(LAS float*)LP(tf) = st[1].y;
      *(LAS float*)LP(tc) = st[2].x; *(LAS float*)LP(td) = st[2].y;
      LGKM0();
    } else {
      if (gemm) { VMCNT(0); BAR(); }
      wo_grp(12 + lid); wo_grp(14 + lid);
#pragma unroll
      for (int q = 0; q < 4; ++q) wo_grp(16 + 2 * q + lid);
      if (gemm) BAR();
    }
    if (gemm) BAR();
    int l2 = lid;
    asm volatile("" : "+s"(l2));
    wo_grp(l2); wo_grp(4 + l2); wo_grp(8 + l2);
    if (!dwn) { wo_grp(24 + l2); wo_grp(28 + l2); }
    if (lid == 2) {
      u32x2 sk;
      _Pragma("nounroll") for (int sp = 0; sp < SPIN_CAP; ++sp) {
        sk = ld_sc1_64(rsWS, (lane & 7) * 8, WS_SEL);
        if (__all(sk.y == tag)) break;
        __builtin_amdgcn_s_sleep(8);
      }
      if (lane < 8) lds_wr(L, L_SEL + 4 * lane, sk.x);
      LGKM0();
    }
  };
  {
    u32 ln = threadIdx.x & 63;
    PIN(ln);
    if (ln < 2) warm_line(lbase + L_UPD, pc0 + min((loc * 8 + wave) * 2 + (int)ln, CODE_LINES - 1) * 128);
  }
  const int lid = wave - 4;
  const bool isx = lid < 2;
  u32 v0 = 0, v1 = 0, v2 = 0, v3 = 0, vh = 0, g0 = 0, g1 = 0, g2 = 0, kstep = 0, d0 = 0;
  {
    int wave_a = wave;
    asm volatile("" : "+s"(wave_a));
    if (gemm && wave_a >= 4) {
      u32 ln = threadIdx.x & 63;
      PIN(ln);
      const int lr = ln >> 3, lc = ln & 7;
      const u32 sw = (u32)(lc ^ (isx ? lr >> 1 : lr & 2)) << 4;
      const u32 vb = lr * (isx ? 4096 : 6144) + sw + (isx ? (r0 + lid) * 2048 : hp * 128);
      const u32 pp = (isx ? 32768 : 49152) - 1024;
      v0 = vb; v1 = (vb ^ 64) + pp; v2 = vb + 2 * pp; v3 = (vb ^ 64) + 3 * pp;
      g0 = (lid == 3) ? 198656 : 0; g1 = isx ? 131072 : (lid == 3) ? 4096 : 196608; g2 = isx ? 262144 : (lid == 3) ? 200704 : 2048;
      vh = max(r0 - 16 + 8 * lid + lr, 0) * 2048 + (sw ^ (lid << 6));
      kstep = isx ? 128u : 64u * 6144u; d0 = lbase + (isx ? 2 + 10 * lid : 22 + 12 * (lid - 2)) * 1024;
      PIN(v0); PIN(v1); PIN(v2); PIN(v3); PIN(vh);
      asm volatile("" : "+s"(g0), "+s"(g1), "+s"(g2), "+s"(kstep), "+s"(d0));
    }
  }
  u32 ev;
  asm volatile("s_waitcnt lgkmcnt(0)" : "+s"(ka), "+s"(kb), "+s"(kord));
  {
    auto ptr = [&](int k) { return (char*)(GAS char*)(((u64)ka[2 * k + 1] << 32) | ka[2 * k]); };
    P.x = (const u16*)ptr(0); P.wqkv = (const u16*)ptr(1); P.wo = (const u16*)ptr(2); P.wr = (const u16*)ptr(3);
    P.wg = (const u16*)ptr(4); P.wu = (const u16*)ptr(5); P.wd = (const u16*)ptr(6); P.out = (u16*)ptr(7);
    P.ws = (char*)(GAS char*)kb;
  }
  rsWS = mk_rsrc(P.ws, WS_END);
  STAMP(0);
  if (wave < 4) {
    asm volatile("s_load_dword %0, %1, 0x0" : "=s"(ev) : "s"(P.ws + WS_EPOCH) : "memory");
    u32 ln = threadIdx.x & 63;
    PIN(ln);
    const u32 d = lbase + 2 * SLOT + wave * 6144, v = ln * 16;
    dma16x4(d, v, v, v, v, mk_rsrc(P.x, S_ * D_ * 2), (pan2 * 128 + cg * 8 + wave * 2) * 2048);
    dma16x2(d + 4096, v, v, mk_rsrc(P.wr, D_ * 2), 0);
  }
  SB();
  int wave_b = wave;
  asm volatile("" : "+s"(wave_b));
  if (gemm && wave_b >= 4) {
    const rsrc_t rs = mk_rsrc(isx ? P.x : P.wqkv, isx ? S_ * D_ * 2 : D_ * 3 * D_ * 2);
    auto issue_k = [&](u32 ko, u32 so) {
      dma16x4(d0 + so, v0, v1, v2, v3, rs, ko + g0);
      dma16x4(d0 + so + 4096, v0, v1, v2, v3, rs, ko + g1);
      if (isx) { dma16x2(d0 + so + 8192, v0, v1, rs, ko + g2); dma16(lbase + so + lid * 1024, vh, rs, ko); }
      else dma16x4(d0 + so + 8192, v0, v1, v2, v3, rs, ko + g2);
    };
    auto issue = [&](int s, u32 so) { issue_k(((u32)(kord >> (4 * s)) & 15u) * kstep, so); };
    LSTAMP(20);
    issue(0, 0);
    LSTAMP(21);
    helper_offsets();
    issue(1, SLOT);
    LSTAMP(22);
    if (isx) VMCNT(11); else VMCNT(12);
    LSTAMP(23);
    BAR();
    const bool helps = !isx && !(xg & 2) && hl != 3;
    if (helps) {
      constexpr int HL = (HELPN * 192 + 35) / 36;
      const int n = min(((pl * 3 + hl) * 2 + lid - 2) * HL + min(lane, HL - 1), HELPN * 192 - 1), row = n / 3;
      warm_line(lbase + L_UPD, (const char*)P.wqkv + (size_t)((u32)((HELPK >> (4 * (row >> 6))) & 15) * 64 + (row & 63)) * 6144 + (n % 3) * 2048 + (hp | 3) * 128);
    }
    issue(2, 2 * SLOT);
    u32 so = 0, sdum = 0;
    const int sn = min((pl * 4 + hl) * 4 + lid, 95) * 2;
    const u32 so0 = (sn / 3) * 6144 + (sn % 3) * 2048, so1 = ((sn + 1) / 3) * 6144 + ((sn + 1) % 3) * 2048;
    const char* sbase = (const char*)P.wqkv + (hp | 3) * 128;
    _Pragma("nounroll") for (int s = 0; s < 13; ++s) {
      if (helps && s == 0) VMCNT(13); else if (!isx || s == WARM_AT + 1 || s == WARM_AT + 2) VMCNT(12); else VMCNT(11);
      if (s == 0) LSTAMP(24);
      if (s == 1) LSTAMP(25);
      if (s == 2) LSTAMP(26);
      BAR();
      issue(s + 3, so);
      if (!(xg & 2) && (u32)(s - RS0) < RSN) {
        const u32 ko = (u32)((ROLLK >> (4 * (s - RS0))) & 15) * (64u * 6144u), a0 = ko + so0, a1 = ko + so1;
        asm volatile("s_load_dword %0, %1, %2\n\ts_load_dword %0, %1, %2 offset:64\n\ts_load_dword %0, %1, %3\n\ts_load_dword %0, %1, %3 offset:64"
                     : "+s"(sdum) : "s"(sbase), "s"(a0), "s"(a1) : "memory");
      }
      if (s == WARM_AT && isx) warm_line(lbase + L_UPD, (const char*)P.wo + (size_t)min((pan * 16 + hp) * 80 + lid * 40 + min(lane, 39), 16383) * 128);
      so = (so == 2 * SLOT) ? 0 : so + SLOT;
    }
    asm volatile("s_waitcnt lgkmcnt(0)" : "+s"(sdum));
    STAMP(2);
    helper_fill();
  }
  if (wave < 4) {
    __builtin_amdgcn_s_setprio(1);
    if (wave == 1) lds_wr(L, L_ONE + lane * 4, lane < 32 ? 0x3f803f80u : 0u);
    if (wave == 2) lds_wr(L, L_GO, 0u);
    f32x4 acc[6][6];
#pragma unroll
    for (int w = 0; w < 6; ++w)
#pragma unroll
      for (int t = (w & 1); t < 6; ++t) { acc[w][t] = f32x4{0.f, 0.f, 0.f, 0.f}; PIN(acc[w][t]); }
    f32x4 bprev, bhalo;
    u32 mc[2];
    u64 cur[4];
#pragma unroll
    for (int r = 0; r < 4; ++r) {
      const int kap = 4 * g + r, d = 16 + (wave & 1) + 2 * i - kap;
      bprev[r] = (kap - i >= 9) ? 0.f : -1e30f;
      bhalo[r] = (((d & 1) == 0) & (d <= 14) & (pan > 0)) ? 0.f : -1e30f;
      cur[r] = __ballot((u32)(i - kap) <= 7u);
      asm volatile("" : "+s"(cur[r]));
    }
    mc[0] = (((u32)(i - 4 * g) <= 7u) ? 0xffffu : 0u) | (((u32)(i - 4 * g - 1) <= 7u) ? 0xffff0000u : 0u);
    mc[1] = (((u32)(i - 4 * g - 2) <= 7u) ? 0xffffu : 0u) | (((u32)(i - 4 * g - 3) <= 7u) ? 0xffff0000u : 0u);
    u32 hxl = lbase + L_HX + (wave & 2) * 2304 + lane * 16;
    u32 obase = WS_O + (((wave & 1) * 32 + hp * 2 + (wave >> 1)) * 64 + pan * 5) * 1024 + lane * 16;
    PIN(bprev); PIN(bhalo); PIN(mc[0]); PIN(mc[1]); PIN(hxl); PIN(obase);
    SB();
    {
      const int tok = pan2 * 128 + cg * 8 + wave * 2 + (lane >> 5);
      u32x4 xv[4], wv[4];
      VMCNT(0);
      {
        LAS char* sp = L + 2 * SLOT + wave * 6144 + (lane & 31) * 16;
#pragma unroll
        for (int j = 0; j < 4; ++j) { xv[j] = *(LAS u32x4*)(sp + (lane >> 5) * 2048 + j * 512); wv[j] = *(LAS u32x4*)(sp + 4096 + j * 512); }
      }
      asm volatile("s_waitcnt lgkmcnt(0)" : "+s"(ev) :: "memory");
      const u32 e = ev;
      tag = e * 0x9E3779B1u + 0x7F4A7C15u; tag ^= tag >> 15; tag = (u32)rfl((int)(tag ^ (u32)(tag == e)));
      if (wave == 1) lds_wr(L, L_TAG, tag);
      float sx = 0.f, sxx = 0.f, sxw = 0.f, sw = 0.f;
#pragma unroll
      for (int j = 0; j < 4; ++j)
#pragma unroll
        for (int e2 = 0; e2 < 4; ++e2) {
          const u32 a = xv[j][e2], b = wv[j][e2];
          sx = dot2(a, 0x3f803f80u, sx); sxx = dot2(a, a, sxx); sxw = dot2(a, b, sxw); sw = dot2(b, 0x3f803f80u, sw);
        }
      const float mean = half_sum(sx) * (1.f / D_);
      const float var = fmaxf(half_sum(sxx) * (1.f / D_) - mean * mean, 0.f);
      const float mb = rbf(mean), sb = rbf(__builtin_amdgcn_sqrtf(rbf(rbf(var) + 1e-5f)));
      const u32 ro = ord16(pack2((half_sum(sxw) - mb * half_sum(sw)) * __builtin_amdgcn_rcpf(sb), 0.f) & 0xffffu);
      const u32 rlo = (u32)__builtin_amdgcn_readlane((int)ro, 31);
      if ((lane & 31) == 31) st_sc1_64(u32x2{pack2(mb, sb), tag}, rsWS, WS_SG + tok * 8);
      if (lane == 63) st_sc1_64(u32x2{rlo | (ro << 16), tag}, rsWS, WS_RG + (tok >> 1) * 8);
      if (!gemm) { LGKM0(); BAR(); }
    }
    STAMP(1);
    if (gemm) {
      const int h = wave >> 1, par = wave & 1;
        const int q4 = i >> 2, p4 = i & 3;
        u32 a0 = lbase + i * 128 + ((g ^ (i >> 1)) << 4);
        const u32 wsw = ((q4 >> 1) & 1) | ((g & 1) << 1);
        u32 w0 = lbase + WOFF + (8 * g + q4) * 128 + (((2 * h + par) ^ wsw) << 5) + p4 * 8;
        const u32 tb = (1 + 5 * par) * 2048;
        const u32 one = lbase + L_ONE;
        u32 pH = (i == 0) ? one : a0, pT = a0 + tb, pW0 = w0, pW1 = w0 ^ 32;
        PIN(a0); PIN(w0); PIN(pH); PIN(pT); PIN(pW0); PIN(pW1);
        Frags f0, f1;
        const u32 e1 = (i == 0) ? 0x3f803f80u : 0u;
        s16x8 e0 = as_frag(u32x4{e1, e1, e1, e1});
        PIN(e0);
        __builtin_amdgcn_s_waitcnt(0xc07f);
        BAR();
        STAMP(2); CSTAMP(14);
        load_frags<0>(f0, LP(pH), LP(pT), LP(pW0), LP(pW1));
        pH ^= 64; pT ^= 64;
        __builtin_amdgcn_s_waitcnt(0xc07f);
        u32 so = 0, trc = 0;
        auto half1 = [&]() {
          SB();
          load_frags<1>(f1, LP(pH), LP(pT), LP(pW0), LP(pW1));
          mma_range<0, 24>(acc, f0, e0);
          interleave();
          SB();
          so = (so == 2 * SLOT) ? 0 : so + SLOT;
          mma_range<24, 25>(acc, f0, e0);
          SB();
          pT = so + a0; pW0 = so + w0; PIN(pT); PIN(pW0);
          mma_range<25, 26>(acc, f0, e0);
          SB();
          pH = (i == 0) ? one : pT; pW1 = pW0 ^ 32; PIN(pH); PIN(pW1);
          mma_range<26, 27>(acc, f0, e0);
          SB();
          pT += tb; PIN(pT);
          mma_range<27, 28>(acc, f0, e0);
          SB();
        };
        auto half1b = [&](int s) {
          const u64 tnow = TICK(), rnow = RT();
          mma_range<28, 30>(acc, f0, e0);
          SB();
          __builtin_amdgcn_s_waitcnt(0xc07f);
          if (STAMPS) { trc = (lane == s) ? (u32)tnow : trc; trc = (lane == s + 16) ? (u32)rnow : trc; }
          mma_range<30, 31>(acc, f0, e0);
          SB();
          BAR();
          SB();
          mma_range<31, 33>(acc, f0, e0);
          SB();
        };
        half1();
        for (int s = 0; s < 15; ++s) {
          half1b(s);
          load_frags<0>(f0, LP(pH), LP(pT), LP(pW0), LP(pW1));
          mma_range<0, 24>(acc, f1, e0);
          interleave();
          SB();
          pH ^= 64; pT ^= 64; PIN(pH); PIN(pT);
          mma_range<24, 31>(acc, f1, e0);
          SB();
          __builtin_amdgcn_s_waitcnt(0xc07f);
          mma_range<31, 33>(acc, f1, e0);
          half1();
        }
        half1b(15);
        if (STAMPS && lane < 32) ((GAS u32*)(P.ws + WS_STAMP + (1 << 20)))[(bid * 4 + wave) * 32 + lane] = trc;
      mma_range<5, 6>(acc, f1, e0); mma_range<16, 17>(acc, f1, e0); mma_range<27, 28>(acc, f1, e0);
      SB();
      mma_range<11, 16>(acc, f1, e0);
      u32 tz = threadIdx.x;
      PIN(tz);
      const u32 i = tz & 15, g = (tz >> 4) & 3;
      u32 sqi = lbase + L_STQ + i * 4, sqg = lbase + L_STQ + g * 16;
      PIN(sqi); PIN(sqg);
      const u32 hx = hxl + par * 2304, hy = hxl + (par ^ 1) * 2304;
      u32 hxg = hx - i * 16, hyg = hy - i * 16, hxi = hx - g * 256, hyi = hy - g * 256, sqi2 = sqi + par * 320, sqg2 = sqg + par * 320;
      const u32 hqa = hx + 2048 - g * 240, hqb = hxl + (L_UPD - L_HX) - (wave & 2) * 2304;
      u32 hq = __builtin_amdgcn_inverse_ballot_w64(0x0001000100010001ull) ? hqa : hqb;
      PIN(hxg); PIN(hyg); PIN(hxi); PIN(hyi); PIN(sqi2); PIN(sqg2); PIN(hq);
#pragma unroll
      for (int k = 0; k < 5; ++k) { __builtin_amdgcn_sched_group_barrier(0x008, 1, 0); __builtin_amdgcn_sched_group_barrier(0x002, 2, 0); }
      SB();
      *(LAS f32x4*)LP(hx) = acc[2][0];
      *(LAS f32x4*)LP(hx + 1024) = acc[4][0];
      *(LAS f32x4*)LP(hq) = acc[0][0];
      mma_range<17, 22>(acc, f1, e0);
#pragma unroll
      for (int k = 0; k < 3; ++k) { __builtin_amdgcn_sched_group_barrier(0x008, 1, 0); __builtin_amdgcn_sched_group_barrier(0x200, 1, 0); }
      SB();
      mma_range<0, 3>(acc, f1, e0);
      SB();
      __builtin_amdgcn_s_waitcnt(0xc07f);
      BAR();
      SB();
      f32x4 cs[4];
      cs[2] = *(LAS f32x4*)LP(hxg); cs[3] = *(LAS f32x4*)LP(hyg);
      cs[0] = *(LAS f32x4*)LP(hxg + 2048 - g * 240); cs[1] = *(LAS f32x4*)LP(hyg + 2048 - g * 240);
      float m[6], rs[6];
#pragma unroll
      for (int t = 0; t < 6; ++t) { m[t] = *(LAS float*)LP((t ? sqi2 : sqi) + t * 64); rs[t] = *(LAS float*)LP((t ? sqi2 : sqi) + t * 64 + 704); }
      acc[3][0] = *(LAS f32x4*)LP(hy);
      const float cv[2] = {*(LAS float*)LP(hxi + 1024), *(LAS float*)LP(hyi + 1024)};
      acc[5][0] = *(LAS f32x4*)LP(hy + 1024);
      mma_range<3, 5>(acc, f1, e0); mma_range<6, 11>(acc, f1, e0);
#pragma unroll
      for (int k = 0; k < 7; ++k) { __builtin_amdgcn_sched_group_barrier(0x008, 1, 0); __builtin_amdgcn_sched_group_barrier(0x100, 2, 0); }
      SB();
      STAMP(3); CSTAMP(15);
      const float QS = 0.17677669529663687f * 1.4426950408889634f;
      const f32x4 z4 = {0.f, 0.f, 0.f, 0.f};
      f32x4 s0[5], s1[5], m4[6], r4[6];
      {
        s16x8 kf[6];
#pragma unroll
        for (int t = 5; t >= 0; --t) {
          const f32x4 k0 = (acc[2][t] + m[t] * cs[2]) * rs[t], k1 = (acc[3][t] + m[t] * cs[3]) * rs[t];
          kf[t] = as_frag(u32x4{pack2(k0[0], k0[1]), pack2(k0[2], k0[3]), pack2(k1[0], k1[1]), pack2(k1[2], k1[3])});
        }
        mma_range<22, 27>(acc, f1, e0); mma_range<28, 33>(acc, f1, e0);
#pragma unroll
        for (int k = 0; k < 10; ++k) { __builtin_amdgcn_sched_group_barrier(0x008, 1, 0); __builtin_amdgcn_sched_group_barrier(0x002, 3, 0); }
        SB();
#pragma unroll
        for (int t = 0; t < 6; ++t) { m4[t] = *(LAS f32x4*)LP((t ? sqg2 : sqg) + t * 64); r4[t] = *(LAS f32x4*)LP((t ? sqg2 : sqg) + t * 64 + 704); }
#pragma unroll
        for (int t = 0; t < 5; ++t) {
          const f32x4 q0 = acc[0][t + 1] + m[t + 1] * cs[0], q1 = acc[1][t + 1] + m[t + 1] * cs[1];
          const s16x8 qf = as_frag(u32x4{pack2(q0[0], q0[1]), pack2(q0[2], q0[3]), pack2(q1[0], q1[1]), pack2(q1[2], q1[3])});
          s0[t] = mfma(kf[t], qf, t == 0 ? bhalo : bprev); s1[t] = mfma(kf[t + 1], qf, z4);
        }
      }
      SB();
      u32x16 vp[2];
#pragma unroll
      for (int t = 0; t < 6; ++t) {
#pragma unroll
        for (int c = 0; c < 2; ++c) {
          const f32x4 v = (acc[4 + c][t] + m4[t] * cv[c]) * r4[t];
          vp[c][2 * t] = pack2(v[0], v[1]); vp[c][2 * t + 1] = pack2(v[2], v[3]);
        }
      }
      PIN(vp[0]); PIN(vp[1]);
      float l[5][4], mx[5], rq[5];
#pragma unroll
      for (int t = 0; t < 5; ++t) {
#pragma unroll
        for (int r = 0; r < 4; ++r) l[t][r] = __builtin_amdgcn_inverse_ballot_w64(cur[r]) ? s1[t][r] : s0[t][r];
        mx[t] = vmax3(vmax(l[t][0], l[t][1]), l[t][2], l[t][3]);
        rq[t] = rs[t + 1] * QS;
      }
#pragma unroll
      for (int t = 0; t < 4; t += 2) {
        const auto r = __builtin_amdgcn_permlane16_swap(__float_as_uint(mx[t]), __float_as_uint(mx[t + 1]), false, false);
        const u32 r0 = r[0], r1 = r[1];
        const float z = max32(vmax(__uint_as_float(r0), __uint_as_float(r1)));
        const auto b = __builtin_amdgcn_permlane16_swap(__float_as_uint(z), __float_as_uint(z), false, false);
        const u32 b0 = b[0], b1 = b[1];
        mx[t] = -__uint_as_float(b0) * rq[t]; mx[t + 1] = -__uint_as_float(b1) * rq[t + 1];
      }
      mx[4] = -max32(max16(mx[4])) * rq[4];
      const s16x8 ones = as_frag(u32x4{0x3f803f80u, 0x3f803f80u, 0x3f803f80u, 0x3f803f80u});
      f32x4 sm[5], o0[5], o1[5];
#pragma unroll
      for (int t = 0; t < 5; ++t) {
#pragma unroll
        for (int r = 0; r < 4; ++r) l[t][r] = __builtin_amdgcn_exp2f(__builtin_fmaf(l[t][r], rq[t], mx[t]));
        const u32 p01 = pack2(l[t][0], l[t][1]), p23 = pack2(l[t][2], l[t][3]);
        const s16x8 pp = as_frag(u32x4{p01 & ~mc[0], p23 & ~mc[1], p01 & mc[0], p23 & mc[1]});
        sm[t] = mfma(ones, pp, z4);
        o0[t] = mfma(as_frag(u32x4{vp[0][2 * t], vp[0][2 * t + 1], vp[0][2 * t + 2], vp[0][2 * t + 3]}), pp, z4);
        o1[t] = mfma(as_frag(u32x4{vp[1][2 * t], vp[1][2 * t + 1], vp[1][2 * t + 2], vp[1][2 * t + 3]}), pp, z4);
      }
#pragma unroll
      for (int t = 0; t < 5; ++t) {
        const float inv = __builtin_amdgcn_rcpf(sm[t][0]);
        o0[t] *= inv; o1[t] *= inv;
        st_sc1_128(u32x4{pack2(o0[t][0], o0[t][1]), pack2(o0[t][2], o0[t][3]), pack2(o1[t][0], o1[t][1]), pack2(o1[t][2], o1[t][3])}, rsWS,
                   (t == 4) ? obase + ((pan == 12) ? 0x40000000u : 4096u) : obase + t * 1024);
      }
      STAMP(8);
    }
  }
  if (!gemm) {
  if (dwn) {
    const rsrc_t rsD = mk_rsrc(P.wd, F_ * D_ * 2);
    const int k0 = (du >> 1) * 14 + (wave >> 2) * 7;
    const u32 vd = (g * D_ + (du & 1) * 512 + (wave & 3) * 128 + 8 * i) * 2;
    auto drow = [&](int q, int j) -> u32 { return (u32)(64 * ((k0 + q) >> 1) + 4 * ((k0 + q) & 1) + 8 * j) * (D_ * 2); };
    constexpr u32 D_W = 24 * 4096, D_H = D_W + 8 * 4352;
    u32x4 wb[56];
#pragma unroll
    for (int j = 0; j < 8; ++j) wb[j] = ld_nt_128(rsD, vd, drow(0, j));
    SB();
    if (wave >= 4) { BAR(); tag = (u32)rfl((int)lds_rd(L, L_TAG)); }
#pragma unroll
    for (int j = 8; j < 48; ++j) wb[j] = ld_nt_128(rsD, vd, drow(j >> 3, j & 7));
    SB();
    transpose_grp(wb);
#pragma unroll
    for (int e = 0; e < 4; ++e) *(LAS u32x4*)(L + D_W + wave * 4352 + e * 1024 + lane * 16) = wb[e];
    SB();
#pragma unroll
    for (int q = 1; q < 6; ++q) transpose_grp(wb + 8 * q);
#pragma unroll
    for (int j = 48; j < 56; ++j) wb[j] = ld_nt_128(rsD, vd, drow(6, j & 7));
    SB();
    __builtin_amdgcn_s_setprio(0);
    if (wave >= 4) { helper_offsets(); helper_fill(); }
    transpose_grp(wb + 48);
    VMCNT(0);
    STAMP(12);
    BAR();
    STAMP(11);
    {
      wait_until(tstart, T_H);
      const int qa = 2 * (wave & 3), qb = min(qa + 1, 6);
      u32x4 va, vb;
      _Pragma("nounroll") for (int sp = 0; sp < SPIN_CAP; ++sp) {
        va = ld_sc1_128(rsWS, lane * 16, WS_H + (k0 + qa) * 1024);
        vb = ld_sc1_128(rsWS, lane * 16, WS_H + (k0 + qb) * 1024);
        if (__all(va.y == tag && va.w == tag && vb.y == tag && vb.w == tag)) break;
        __builtin_amdgcn_s_sleep(1);
      }
      *(LAS u32x2*)(L + D_H + ((wave >> 2) * 7 + qa) * 512 + (lane & 31) * 16 + (lane >> 5) * 8) = u32x2{va.x, va.z};
      *(LAS u32x2*)(L + D_H + ((wave >> 2) * 7 + qb) * 512 + (lane & 31) * 16 + (lane >> 5) * 8) = u32x2{vb.x, vb.z};
      LGKM0();
      lds_wr(L, L_HR + wave * 4, 1);
      lds_wait_set(L, L_HR + (wave & 4) * 4 + (lane & 3) * 4);
    }
    STAMP(2);
    f32x4 ga[8];
#pragma unroll
    for (int e = 0; e < 8; ++e) ga[e] = f32x4{0.f, 0.f, 0.f, 0.f};
    {
      s16x8 hf = *(LAS s16x8*)(L + D_H + ((wave >> 2) * 7 + 6) * 512 + (g * 8 + (i & 7)) * 16);
#pragma unroll
      for (int q = 6; q >= 0; --q) {
        s16x8 hn = hf;
        if (q > 0) hn = *(LAS s16x8*)(L + D_H + ((wave >> 2) * 7 + q - 1) * 512 + (g * 8 + (i & 7)) * 16);
        mma_frags8(ga, wb + q * 8, hf);
        if (q == 5) {
#pragma unroll
          for (int e = 0; e < 4; ++e) wb[e] = *(LAS u32x4*)(L + D_W + wave * 4352 + e * 1024 + lane * 16);
        }
        SB();
        hf = hn;
      }
    }
    STAMP(4);
    {
      u32 ln = threadIdx.x & 63;
      PIN(ln);
      if ((ln & 15) < 8) {
        const u32 o = D_W + wave * 4352 + (ln & 7) * 64 + (((ln >> 4) ^ ((ln >> 1) & 3)) << 4);
#pragma unroll
        for (int e = 0; e < 8; ++e) *(LAS f32x4*)(L + o + e * 512) = ga[e];
      }
    }
    LGKM0();
    lds_wr(L, L_PW + 4 * wave, 1);
    if (wave >= 4) {
      lds_wait_set(L, L_PW + (wave - 4) * 4);
      {
        u32 ln = threadIdx.x & 63;
        PIN(ln);
        const u32 gq = 2 * (ln >> 5) + ((ln >> 1) & 1), o = D_W + ((ln >> 2) & 7) * 512 + (ln & 1) * 8;
#pragma unroll
        for (int tk = 0; tk < 8; ++tk) {
          const u32 a = o + tk * 64 + ((gq ^ ((tk >> 1) & 3)) << 4);
          const f32x2 y = *(LAS f32x2*)(L + a + (wave - 4) * 4352) + *(LAS f32x2*)(L + a + wave * 4352);
          st_sc1_128(u32x4{__float_as_uint(y.x), tag, __float_as_uint(y.y), tag}, rsWS, WS_YS + (du >> 1) * 65536 + tk * 8192 + (du & 1) * 4096 + (wave - 4) * 1024 + ln * 16);
        }
      }
      LGKM0();
      lds_wr(L, L_HS + 4 * (wave - 4), 1);
      lds_wait_set(L, L_HS + (lane & 3) * 4);
      u32 ln = threadIdx.x & 63;
      PIN(ln);
      vO = (ln >> 3) * 2048 + (((ln & 7) ^ (((ln >> 4) & 3) << 1)) << 4) + cg * 128;
      wo_grp(20 + wave); wo_grp(24 + wave);
      VMCNT(0);
      lds_wr(L, L_HS + 4 * (wave - 4), 2);
    }
    STAMP(8);
  } else {
    const int lr = lane >> 3, lc = lane & 7;
    const int fv = fu - (fu > 2);
    const bool two = fu != 2 && fv < 24;
    const int la = (fu == 2) ? 50 : (fv >= 24) ? (fv - 24) * 8 + 3 : (fv >= 21) ? (fv - 21) * 16 + 2 : (fv / 3) * 8 + ((fv % 3) ? 2 + 2 * (fv % 3) : 0);
    const int lb = la + ((fv >= 21) ? 8 : 1);
    const rsrc_t rsG = mk_rsrc(P.wg, D_ * F_ * 2), rsU = mk_rsrc(P.wu, D_ * F_ * 2), rsX = mk_rsrc(P.x, S_ * D_ * 2);
    const u32 vg = (i < 8 || two) ? ((wave * 128 + 8 * g) * F_ + ((i < 8) ? la : lb) * 64 + 8 * (i & 7)) * 2 : 0x7f000000u;
    u32x4 wb[8 * GWIN];
    auto ldgrp = [&](int q) {
#pragma unroll
      for (int j = 0; j < 8; ++j) wb[(q % GWIN) * 8 + j] = ld_nt_128((q & 1) ? rsU : rsG, vg, ((q >> 1) * 32 + j) * (F_ * 2));
      SB();
    };
    __builtin_amdgcn_s_setprio(0);
    ldgrp(0);
    ldgrp(1);
    if (wave >= 4) { BAR(); tag = (u32)rfl((int)lds_rd(L, L_TAG)); }
    if (fu == 2) {
      *(LAS u32x4*)(L + 32 + threadIdx.x * 16) = u32x4{0u, 0u, 0u, 0u};
      if (threadIdx.x < 4) *(LAS u32x4*)(L + 32 + 8192 + threadIdx.x * 16) = u32x4{0u, 0u, 0u, 0u};
      wait_until(tstart, T_GATHER);
      u32x4 v;
      _Pragma("nounroll") for (int sp = 0; sp < SPIN_CAP; ++sp) {
        v = ld_sc1_128(rsWS, lane * 16, WS_RG + wave * 1024);
        if (__all(v.y == tag && v.w == tag)) break;
        __builtin_amdgcn_s_sleep(2);
      }
      const u32 ib = 2047 - 256 * wave - 4 * lane;
      const u32 key[4] = {(v.x << 16) | ib, (v.x & 0xffff0000u) | (ib - 1), (v.z << 16) | (ib - 2), (v.z & 0xffff0000u) | (ib - 3)};
      lds_wr(L, wave * 4, wave_max_u32(max(max(key[0], key[1]), max(key[2], key[3]))));
      LGKM0();
      BAR();
      STAMP(9);
      const u32x4 m0 = *(LAS u32x4*)L, m1 = *(LAS u32x4*)(L + 16);
      const u32 thr = min(min(min(m0.x, m0.y), min(m0.z, m0.w)), min(min(m1.x, m1.y), min(m1.z, m1.w)));
      const u32 c0 = key[0] >= thr, c1 = key[1] >= thr, c2 = key[2] >= thr, c3 = key[3] >= thr;
      if (c0 | c1 | c2 | c3) {
        const u32 p = 64 + 4 * __hip_atomic_fetch_add((LAS u32*)(L + 32), c0 + c1 + c2 + c3, __ATOMIC_RELAXED, __HIP_MEMORY_SCOPE_WORKGROUP);
        lds_wr(L, c0 ? p : 36, key[0]); lds_wr(L, c1 ? p + 4 * c0 : 36, key[1]);
        lds_wr(L, c2 ? p + 4 * (c0 + c1) : 36, key[2]); lds_wr(L, c3 ? p + 4 * (c0 + c1 + c2) : 36, key[3]);
      }
      LGKM0();
      BAR();
      const int n = rfl((int)lds_rd(L, 32));
      _Pragma("nounroll") for (int b = threadIdx.x; b < n; b += 512) {
        const u32 my = lds_rd(L, 64 + 4 * b);
        int rank = 0;
        _Pragma("nounroll") for (int t = 0; t < n; t += 8) {
          const u32x4 o = *(LAS u32x4*)(L + 64 + 4 * t), q = *(LAS u32x4*)(L + 80 + 4 * t);
          rank += (o.x > my) + (o.y > my) + (o.z > my) + (o.w > my) + (q.x > my) + (q.y > my) + (q.z > my) + (q.w > my);
        }
        if (rank < 8) st_sc1_64(u32x2{my ^ 0x7ffu, tag}, rsWS, WS_SEL + rank * 8);
      }
      if (threadIdx.x == 0) st_sc1_32(tag, rsWS, WS_EPOCH);
      STAMP(10);
      LGKM0();
      BAR();
    }
    constexpr u32 F_XS = 81920;
    ldgrp(2);
    if (wave == 0) {
      wait_until(tstart, T_SEL);
      u32x2 sk;
      _Pragma("nounroll") for (int sp = 0; sp < SPIN_CAP; ++sp) {
        sk = ld_sc1_64(rsWS, lr * 8, WS_SEL);
        if (__all(sk.y == tag)) break;
        __builtin_amdgcn_s_sleep(4);
      }
      STAMP(2);
      u32x4 xr[16];
#pragma unroll
      for (int j = 0; j < 16; ++j) xr[j] = __builtin_amdgcn_raw_buffer_load_b128(rsX, (sk.x & 0x7ffu) * 2048 + lc * 16, j * 128, 0);
      u32x2 ms;
      _Pragma("nounroll") for (int sp = 0; sp < SPIN_CAP; ++sp) {
        ms = ld_sc1_64(rsWS, (sk.x & 0x7ffu) * 8, WS_SG);
        if (__all(ms.y == tag)) break;
      }
#pragma unroll
      for (int j = 0; j < 16; ++j) *(LAS u32x4*)(L + F_XS + lr * 2064 + j * 128 + lc * 16) = xr[j];
      *(LAS u32*)(L + F_XS + 8 * 2064 + lr * 4) = ms.x;
      LGKM0();
      lds_wr(L, L_HR, 1);
    }
#pragma unroll
    for (int q = 3; q < GWIN; ++q) ldgrp(q);
#pragma unroll
    for (int q = 0; q < GPRE; ++q) transpose_grp(wb + 8 * q);
    lds_wait_set(L, L_HR);
    f32x4 ga[16];
#pragma unroll
    for (int e = 0; e < 16; ++e) ga[e] = f32x4{0.f, 0.f, 0.f, 0.f};
    const u32 ms = *(LAS u32*)(L + F_XS + 8 * 2064 + (i & 7) * 4);
    const float rsn = __builtin_amdgcn_rcpf(bf_hi(ms)), mrs = -bf_lo(ms) * rsn;
    s16x8 xf;
#pragma unroll
    for (int q = 0; q < 8; ++q) {
      if ((q & 1) == 0) {
        const u32x4 xv = *(LAS u32x4*)(L + F_XS + (i & 7) * 2064 + (wave * 128 + (q >> 1) * 32 + 8 * g) * 2);
        u32x4 xn;
#pragma unroll
        for (int e = 0; e < 4; ++e) xn[e] = pack2(__builtin_fmaf(bf_lo(xv[e]), rsn, mrs), __builtin_fmaf(bf_hi(xv[e]), rsn, mrs));
        xf = as_frag(xn);
      }
      if (q < GPRE) mma_frags8(ga + 8 * (q & 1), wb + q * 8, xf); else mma_cols(ga + 8 * (q & 1), wb + (q % GWIN) * 8, xf);
      SB();
      if (q + GWIN < 8) ldgrp(q + GWIN);
    }
    STAMP(4);
    {
      u32 ln = threadIdx.x & 63;
      PIN(ln);
      if ((ln & 15) < 8) {
        const u32 o = wave * 10240 + (ln & 7) * 80 + (ln >> 4) * 16;
#pragma unroll
        for (int e = 0; e < 16; ++e) *(LAS f32x4*)(L + o + e * 640) = ga[e];
      }
    }
    LGKM0();
    BAR();
    {
      u32 ln = threadIdx.x & 63;
      PIN(ln);
      const u32 e = 4 * ((wave >> 1) & 1) + (ln >> 4), tk = (ln >> 1) & 7, m = 8 * (wave >> 2) + 4 * (wave & 1) + 2 * (ln & 1);
      const u32 o = (e * 8 + tk) * 80 + m * 4;
      f32x2 gs = {0.f, 0.f}, us = {0.f, 0.f};
#pragma unroll
      for (int w = 0; w < 8; ++w) { gs += *(LAS f32x2*)(L + w * 10240 + o); us += *(LAS f32x2*)(L + w * 10240 + 5120 + o); }
      const float g0 = rbf(gs.x), g1 = rbf(gs.y);
      const u32 hv = pack2(rbf(g0 * sigm(g0)) * rbf(us.x), rbf(g1 * sigm(g1)) * rbf(us.y));
      if (wave < 4 || two) st_sc1_64(u32x2{hv, tag}, rsWS, WS_H + ((wave < 4) ? la : lb) * 2048 + (wave & 3) * 512 + ln * 8);
    }
    LGKM0();
    BAR();
    STAMP(8);
    if (wave >= 4) { helper_offsets(); helper_fill(); }
  }
  }

  if (wave >= 4) {
    const int lid = wave - 4;
    if (!dwn) {
      VMCNT(0);
      STAMP(3);
      BAR();
    }
    if (lid >= 2) {
      const u32 sk = lds_rd(L, L_SEL + 4 * (lane & 7));
      _Pragma("nounroll") for (int c = lid - 2; c < 8; c += 2) {
        const u32 tk = (u32)__builtin_amdgcn_readlane((int)sk, c) & 0x7ffu;
        if ((int)(tk >> 7) != pan2) continue;
        float y0, y1;
        _Pragma("nounroll") for (int sp = 0; sp < SPIN_CAP; ++sp) {
          u32 bad = 0;
          y0 = 0.f; y1 = 0.f;
#pragma unroll
          for (int j = 0; j < 4; ++j) {
            const u32x4 v = ld_sc1_128(rsWS, (lane >> 5) * 65536 + (lane & 31) * 16, WS_YS + j * 131072 + c * 8192 + cg * 512);
            bad |= (v.y ^ tag) | (v.w ^ tag);
            y0 += __uint_as_float(v.x); y1 += __uint_as_float(v.z);
          }
          if (__all(bad == 0)) break;
          __builtin_amdgcn_s_sleep(8);
        }
        y0 = sum32(y0); y1 = sum32(y1);
        if (lane < 32) {
          const u32 o = L_UPD + (c * 64 + 16 * (lane & 3) + (lane >> 2)) * 4;
          *(LAS float*)(L + o) = y0; *(LAS float*)(L + o + 32) = y1;
        }
      }
      LGKM0();
      lds_wr(L, L_UPDF + 4 * (lid - 2), 1);
      STAMP(7);
    }
    return;
  }

  {
    const bool pol = wave == 0;
    const u32 fo = WS_OF + (((pan2 * 4 + 3 * (lane >> 5)) / 5) * 16 + (lane & 15)) * 128;
    u32 fv = ~tag;
    if (dwn && pol) fv = ld_sc1_32(rsWS, fo, 0);
    SB();
    if (!gemm && !dwn) BAR();
    const int par2 = wave >> 1, t2 = (wave & 1) * 2;
    u32 wa[4], wc[4];
    s16x8 wf[3][4];
    auto wo_addr = [&]() {
      u32 ln = threadIdx.x & 63;
      PIN(ln);
      const u32 g = ln >> 4, i = ln & 15, q4 = i >> 2, p4 = i & 3;
      const u32 wb = lbase + (4 * g + q4) * 128 + p4 * 8 + par2 * 2048, wsw = (2 * g + (q4 >> 1)) & 3;
#pragma unroll
      for (int c = 0; c < 4; ++c) { wa[c] = wb + ((c ^ wsw) << 5); wc[c] = wa[c] ^ 2048; PIN(wa[c]); PIN(wc[c]); }
    };
    auto wf_first = [&]() {
#pragma unroll
      for (int k = 0; k < 2; ++k)
#pragma unroll
        for (int c = 0; c < 4; ++c) wf[k][c] = tr_frag(LP(wa[c]), LP(wc[c]), k * 4096, k * 4096);
    };
    if (gemm) oc_init();
    SB();
    if (gemm) {
      VMCNT(0);
      if (lane == 0 && __hip_atomic_fetch_add((LAS u32*)(L + L_ARR), 1u, __ATOMIC_RELAXED, __HIP_MEMORY_SCOPE_WORKGROUP) == 3)
        st_sc1_32(tag, rsWS, WS_OF + (pan * 16 + hp) * 128);
      STAMP(4);
      SB();
      wo_addr();
      BAR();
      wf_first();
    }
    const int T0 = pan2 * 4 + t2;
    const u32 sk = lds_rd(L, L_SEL + 4 * (lane & 7));
    __builtin_amdgcn_s_waitcnt(0xc07f);
    SB();
    if (!gemm) wait_until(tstart, T_O);
    if (pol) {
      _Pragma("nounroll") for (int sp = 0; sp < SPIN_CAP; ++sp) {
        if (__all(fv == tag)) break;
        if (sp) __builtin_amdgcn_s_sleep(2);
        fv = ld_sc1_32(rsWS, fo, 0);
      }
      lds_wr(L, L_GO, 1);
    } else {
      _Pragma("nounroll") for (int sp = 0; sp < SPIN_CAP; ++sp) if (rfl((int)lds_rd(L, L_GO))) break;
    }
    CBAR();
    STAMP(5);
    const u32 abase = WS_O + (par2 * 32 * 64 + T0) * 1024;
    u32x4 af[OWIN][2];
#pragma unroll
    for (int hh = 0; hh < OWIN; ++hh) {
#pragma unroll
      for (int tt = 0; tt < 2; ++tt) af[hh][tt] = ld_sc1_128(rsWS, lane * 16 + tt * 1024, abase + ((hh + hrot) & 31) * 65536);
      SB();
    }
    if (!gemm) {
      wo_addr(); wf_first();
      SB();
      oc_init();
      __builtin_amdgcn_s_waitcnt(0xc07f);
      SB();
    }
#pragma unroll
    for (int hh = 0; hh < 32; ++hh) {
      if (hh == 21 && dwn) {
        _Pragma("nounroll") for (int sp = 0; sp < SPIN_CAP; ++sp) if (__all(lds_rd(L, L_HS + (lane & 3) * 4) == 2u)) break;
        CBAR(); SB();
      }
      if (hh == 14) {
#pragma unroll
        for (int c = 0; c < 4; ++c) { wa[c] += 65536; wc[c] += 65536; PIN(wa[c]); PIN(wc[c]); }
        SB();
      }
      if (hh == 31) __builtin_amdgcn_s_waitcnt(0xc07f); else __builtin_amdgcn_s_waitcnt(0xc87f);
      SB();
      if (hh + 2 < 32) {
#pragma unroll
        for (int c = 0; c < 4; ++c) wf[(hh + 2) % 3][c] = tr_frag(LP(wa[c]), LP(wc[c]), ((hh + 2) & 15) * 4096, ((hh + 2) & 15) * 4096);
      }
#pragma unroll
      for (int tt = 0; tt < 2; ++tt) {
#pragma unroll
        for (int c = 0; c < 4; ++c) oc[tt][c] = mfma(wf[hh % 3][c], as_frag(af[hh % OWIN][tt]), oc[tt][c]);
        if (hh + OWIN < 32) af[hh % OWIN][tt] = ld_sc1_128(rsWS, lane * 16 + tt * 1024, abase + ((hh + OWIN + hrot) & 31) * 65536);
      }
#pragma unroll
      for (int k = 0; k < 2; ++k) {
        __builtin_amdgcn_sched_group_barrier(0x008, 1, 0); __builtin_amdgcn_sched_group_barrier(0x100, 2, 0);
        __builtin_amdgcn_sched_group_barrier(0x008, 1, 0); __builtin_amdgcn_sched_group_barrier(0x100, 1, 0);
        __builtin_amdgcn_sched_group_barrier(0x008, 1, 0); __builtin_amdgcn_sched_group_barrier(0x100, 1, 0);
        __builtin_amdgcn_sched_group_barrier(0x008, 1, 0); __builtin_amdgcn_sched_group_barrier(0x020, 1, 0);
      }
      SB();
    }
    STAMP(6);
    {
      const u32 stl = (sk & 0x7ffu) - (u32)(pan2 * 128 + par2 + 32 * t2);
      u64 hit = __ballot(stl < 64u && (stl & 1u) == 0u) & 0xffull;
      if (hit) {
        lds_wait_set(L, L_UPDF + (lane & 1) * 4);
        while (hit) {
          const int c = __builtin_ctzll(hit);
          hit &= hit - 1;
          const u32 tl = (u32)__builtin_amdgcn_readlane((int)stl, c), ob = (u32)__builtin_amdgcn_readlane((int)sk, c) >> 16;
          const float sg = rbf(sigm(bf_lo((ob & 0x8000u) ? (ob & 0x7fffu) : (~ob & 0xffffu))));
          const float f0 = ((tl >> 1) == (u32)i) ? sg : 0.f, f1 = ((tl >> 1) == (u32)i + 16u) ? sg : 0.f;
#pragma unroll
          for (int cc = 0; cc < 4; ++cc) {
            const f32x4 u = *(LAS f32x4*)(L + L_UPD + (c * 64 + 16 * cc + 4 * g) * 4);
            oc[0][cc] += u * f0; oc[1][cc] += u * f1;
          }
        }
      }
    }
    const rsrc_t rsO = mk_rsrc(P.out, S_ * D_ * 2);
    const u32 xo = ((pan2 * 128 + par2 + 32 * t2 + 2 * i) * D_ + cg * 64 + (g & 1) * 16 + (g >> 1) * 8) * 2;
#pragma unroll
    for (int tt = 0; tt < 2; ++tt)
#pragma unroll
      for (int cp = 0; cp < 2; ++cp) {
        const u32 a0 = pack2(oc[tt][2 * cp][0], oc[tt][2 * cp][1]), a1 = pack2(oc[tt][2 * cp][2], oc[tt][2 * cp][3]);
        const u32 b0 = pack2(oc[tt][2 * cp + 1][0], oc[tt][2 * cp + 1][1]), b1 = pack2(oc[tt][2 * cp + 1][2], oc[tt][2 * cp + 1][3]);
        const auto e0 = __builtin_amdgcn_permlane16_swap(a0, b0, false, false);
        const auto e1 = __builtin_amdgcn_permlane16_swap(a1, b1, false, false);
        st_sc1_128(u32x4{e0[0], e1[0], e0[1], e1[1]}, rsO, xo + tt * 65536 + cp * 64);
      }
    STAMP(7);
  }
}

static u64 k_order(int odd, int pl, int hl) {
  const int np = 7 - odd;
  int slot[16], used[16];
  for (int k = 0; k < 16; ++k) { slot[k] = -1; used[k] = 0; }
  for (int k = 2; k < 16; ++k) {
    const int h0 = k & 3, p0 = (k * (odd ? 5 : 3)) % np;
    int want = -1;
    if (hl == h0 && pl % np == p0) want = k - 2; else if (hl == h0 || pl % np == p0) want = k - 1;
    if (want >= 0) {
      if (want < 1) want = 1;
      while (want > 1 && used[want]) --want;
      if (!used[want]) { slot[k] = want; used[want] = 1; }
    }
  }
  int s = 0;
  for (int k = 0; k < 16; ++k) if (slot[k] < 0) { while (used[s]) ++s; slot[k] = s; used[s] = 1; }
  u64 r = 0;
  for (int k = 0; k < 16; ++k) r |= (u64)k << (4 * slot[k]);
  return r;
}

extern "C" void kernel_launch(void* const* d_in, const int* in_sizes, int n_in, void* d_out, int out_size, void* d_ws, size_t ws_size,
                              hipStream_t stream) {
  Args A;
  Params& P = A.p;
  P.x = (const u16*)d_in[0]; P.wqkv = (const u16*)d_in[1]; P.wo = (const u16*)d_in[2]; P.wr = (const u16*)d_in[4];
  P.wg = (const u16*)d_in[5]; P.wu = (const u16*)d_in[6]; P.wd = (const u16*)d_in[7];
  P.out = (u16*)d_out; P.ws = (char*)d_ws;
  for (int e = 0; e < 64; ++e) A.kt[e] = k_order(e >> 5, (e >> 2) & 7, e & 3);
  (void)hipFuncSetAttribute((const void*)fused_kernel, hipFuncAttributeMaxDynamicSharedMemorySize, L_END + 64);
  hipLaunchKernelGGL(fused_kernel, dim3(256), dim3(512), L_END + 64, stream, A);
}
```

```cpp
#include <hip/hip_runtime.h>
#include <stdint.h>

typedef unsigned short u16;
typedef unsigned u32;
typedef unsigned long long u64;
typedef __attribute__((ext_vector_type(4))) short s16x4;
typedef __attribute__((ext_vector_type(8))) short s16x8;
typedef __attribute__((ext_vector_type(2))) float f32x2;
typedef __attribute__((ext_vector_type(4))) float f32x4;
typedef __attribute__((ext_vector_type(4))) unsigned u32x4;
typedef __attribute__((ext_vector_type(2))) unsigned u32x2;
typedef __attribute__((ext_vector_type(16))) unsigned u32x16;
typedef __attribute__((ext_vector_type(2))) __bf16 bf16x2;
typedef __amdgpu_buffer_rsrc_t rsrc_t;
#define LAS __attribute__((address_space(3)))
#define GAS __attribute__((address_space(1)))
#define DEV __device__ __forceinline__

#define STAMPS 0
constexpr int OWIN = 6;
constexpr int GWIN = 6, GPRE = 4;
constexpr int T_GATHER = 350, T_SEL = 580, T_H = 1300, T_O = 1520;
constexpr int WARM_AT = 10;
constexpr u64 HELPK = 0x765432ull, ROLLK = 0xfedcba98ull;
constexpr int HELPN = 6, RS0 = 1, RSN = 8;
constexpr int CODE_LINES = 338;

constexpr int S_ = 2048, D_ = 1024, F_ = 3584;
constexpr u32 WS_EPOCH = 0;
constexpr u32 WS_SEL = 4096;
constexpr u32 WS_RG = 16384;
constexpr u32 WS_SG = 32768;
constexpr u32 WS_OF = 65536;
constexpr u32 WS_H = WS_OF + 131072;
constexpr u32 WS_YS = WS_H + 131072;
constexpr u32 WS_O = WS_YS + 524288;
constexpr u32 WS_END = WS_O + S_ * D_ * 2;
constexpr u32 WS_STAMP = 16u << 20;
constexpr u32 WOFF = 22528, SLOT = 47104;
constexpr u32 L_HX = 131072;
constexpr u32 L_XT = 3 * SLOT;
constexpr u32 L_STQ = L_XT + 16384;
constexpr u32 L_STO = L_STQ + 1408;
constexpr u32 L_UPD = L_STO + 1024;
constexpr u32 L_ONE = L_UPD + 2048;
constexpr u32 L_CTL = L_ONE + 128;
constexpr u32 L_TAG = L_CTL, L_ARR = L_CTL + 4, L_UPDF = L_CTL + 8  , L_HS = L_CTL + 16  , L_SEL = L_CTL + 32  ;
constexpr u32 L_PW = L_CTL + 64, L_HR = L_CTL + 96;
constexpr u32 L_GO = L_CTL + 128;
constexpr u32 L_END = L_CTL + 192;
constexpr int SPIN_CAP = 1 << 22;
static_assert(L_END <= 163840 && L_ONE % 128 == 0, "LDS");

struct Params {
  const u16 *x, *wqkv, *wo, *wr, *wg, *wu, *wd;
  u16* out;
  char* ws;
};
struct Args {
  Params p;
  u64 kt[64];
};

DEV float bf_lo(u32 v) { return __uint_as_float(v << 16); }
DEV float bf_hi(u32 v) { return __uint_as_float(v & 0xffff0000u); }
DEV u32 pack2(float a, float b) { bf16x2 p = {(__bf16)a, (__bf16)b}; return __builtin_bit_cast(u32, p); }
DEV float rbf(float a) { return bf_lo(pack2(a, a)); }
DEV float dot2(u32 a, u32 b, float c) { return __builtin_amdgcn_fdot2_f32_bf16(__builtin_bit_cast(bf16x2, a), __builtin_bit_cast(bf16x2, b), c, false); }
DEV s16x8 as_frag(u32x4 v) { return __builtin_bit_cast(s16x8, v); }
DEV f32x4 mfma(s16x8 a, s16x8 b, f32x4 c) { return __builtin_amdgcn_mfma_f32_16x16x32_bf16(a, b, c, 0, 0, 0); }
DEV int rfl(int v) { return __builtin_amdgcn_readfirstlane(v); }
template <int CTRL, int RM, bool BC> DEV float dppf(float v) { return __int_as_float(__builtin_amdgcn_update_dpp(0, __float_as_int(v), CTRL, RM, 0xf, BC)); }
template <int CTRL, int RM, bool BC> DEV u32 dppu(u32 v) { return (u32)__builtin_amdgcn_update_dpp(0, (int)v, CTRL, RM, 0xf, BC); }
DEV float half_sum(float v) {
  v += dppf<0x111, 0xf, true>(v); v += dppf<0x112, 0xf, true>(v); v += dppf<0x114, 0xf, true>(v); v += dppf<0x118, 0xf, true>(v);
  v += dppf<0x142, 0xa, false>(v);
  return v;
}
DEV u32 wave_max_u32(u32 v) {
  v = max(v, dppu<0x111, 0xf, true>(v)); v = max(v, dppu<0x112, 0xf, true>(v)); v = max(v, dppu<0x114, 0xf, true>(v)); v = max(v, dppu<0x118, 0xf, true>(v));
  v = max(v, dppu<0x142, 0xa, false>(v)); v = max(v, dppu<0x143, 0xc, false>(v));
  return (u32)__builtin_amdgcn_readlane((int)v, 63);
}
DEV float vmax(float a, float b) { return __builtin_elementwise_maximum(a, b); }
DEV float vmax3(float a, float b, float c) { return vmax(vmax(a, b), c); }
DEV float max16(float v) { u32 a = __float_as_uint(v); auto r = __builtin_amdgcn_permlane16_swap(a, a, false, false); u32 x = r[0], y = r[1]; return vmax(__uint_as_float(x), __uint_as_float(y)); }
DEV float max32(float v) { u32 a = __float_as_uint(v); auto r = __builtin_amdgcn_permlane32_swap(a, a, false, false); u32 x = r[0], y = r[1]; return vmax(__uint_as_float(x), __uint_as_float(y)); }
DEV float sum32(float v) { u32 a = __float_as_uint(v); auto r = __builtin_amdgcn_permlane32_swap(a, a, false, false); u32 x = r[0], y = r[1]; return __uint_as_float(x) + __uint_as_float(y); }
DEV u32 ord16(u32 b) { return (b & 0x8000u) ? (~b & 0xffffu) : (b | 0x8000u); }
DEV float sigm(float v) { return __builtin_amdgcn_rcpf(1.f + __builtin_amdgcn_exp2f(-v * 1.4426950408889634f)); }

DEV rsrc_t mk_rsrc(const void* p, u32 bytes) { return __builtin_amdgcn_make_buffer_rsrc((void*)p, 0, (int)bytes, 0x00020000); }
DEV u32x4 ld_nt_128(rsrc_t rs, u32 voff, u32 soff) { return __builtin_amdgcn_raw_buffer_load_b128(rs, voff, soff, 2); }
DEV u32x4 ld_sc1_128(rsrc_t rs, u32 voff, u32 soff) { return __builtin_amdgcn_raw_buffer_load_b128(rs, voff, soff, 16); }
DEV u32x2 ld_sc1_64(rsrc_t rs, u32 voff, u32 soff) { return __builtin_amdgcn_raw_buffer_load_b64(rs, voff, soff, 16); }
DEV u32 ld_sc1_32(rsrc_t rs, u32 voff, u32 soff) { return __builtin_amdgcn_raw_buffer_load_b32(rs, voff, soff, 16); }
DEV void st_sc1_128(u32x4 v, rsrc_t rs, u32 voff) { __builtin_amdgcn_raw_buffer_store_b128(v, rs, voff, 0, 16); }
DEV void st_sc1_64(u32x2 v, rsrc_t rs, u32 voff) { __builtin_amdgcn_raw_buffer_store_b64(v, rs, voff, 0, 16); }
DEV void st_sc1_32(u32 v, rsrc_t rs, u32 voff) { __builtin_amdgcn_raw_buffer_store_b32(v, rs, voff, 0, 16); }
#define VMCNT(n) asm volatile("s_waitcnt vmcnt(" #n ")" ::: "memory")
#define LGKM0() asm volatile("s_waitcnt lgkmcnt(0)" ::: "memory")
#define CBAR() asm volatile("" ::: "memory")
#define BAR() do { CBAR(); __builtin_amdgcn_s_barrier(); CBAR(); } while (0)
#define SB() __builtin_amdgcn_sched_barrier(0)
#define LP(a) ((LAS char*)(uintptr_t)(a))
#define PIN(x) asm volatile("" : "+v"(x))

#define TICK() 0ull
#define RT() 0ull
#define STAMP(k) do {} while (0)
#define CSTAMP(k) do {} while (0)
#define LSTAMP(k) do {} while (0)

DEV u32 lds_rd(LAS char* L, u32 off) { return *(volatile LAS u32*)(L + off); }
DEV void lds_wr(LAS char* L, u32 off, u32 v) { *(volatile LAS u32*)(L + off) = v; }
DEV void lds_wait_set(LAS char* L, u32 off) {
  _Pragma("nounroll") for (int sp = 0; sp < SPIN_CAP; ++sp) {
    if (__all(lds_rd(L, off) != 0u)) break;
    __builtin_amdgcn_s_sleep(1);
  }
  CBAR();
}
DEV void wait_until(u64 t0, u32 ticks) {
  while (__builtin_amdgcn_s_memrealtime() - t0 < ticks) __builtin_amdgcn_s_sleep(4);
}
#define DMA_HEAD "s_mov_b32 m0, %0\n\ts_nop 0\n\t"
DEV void dma16(u32 lds, u32 voff, rsrc_t rs, u32 soff) {
  asm volatile(DMA_HEAD "buffer_load_dwordx4 %1, %2, %3 offen lds" ::"s"(lds), "v"(voff), "s"(rs), "s"(soff) : "memory");
}
DEV void dma16x4(u32 lds, u32 v0, u32 v1, u32 v2, u32 v3, rsrc_t rs, u32 soff) {
  asm volatile(DMA_HEAD "buffer_load_dwordx4 %1, %5, %6 offen lds\n\tbuffer_load_dwordx4 %2, %5, %6 offen offset:1024 lds\n\t"
               "buffer_load_dwordx4 %3, %5, %6 offen offset:2048 lds\n\tbuffer_load_dwordx4 %4, %5, %6 offen offset:3072 lds"
               ::"s"(lds), "v"(v0), "v"(v1), "v"(v2), "v"(v3), "s"(rs), "s"(soff) : "memory");
}
DEV void dma16x2(u32 lds, u32 v0, u32 v1, rsrc_t rs, u32 soff) {
  asm volatile(DMA_HEAD "buffer_load_dwordx4 %1, %3, %4 offen lds\n\tbuffer_load_dwordx4 %2, %3, %4 offen offset:1024 lds"
               ::"s"(lds), "v"(v0), "v"(v1), "s"(rs), "s"(soff) : "memory");
}

struct Frags { s16x8 x[6]; s16x8 w[6]; };

DEV s16x8 tr_frag(LAS char* p, LAS char* q, u32 o0, u32 o1) {
  s16x4 a = __builtin_amdgcn_ds_read_tr16_b64_v4i16((LAS s16x4*)(p + o0));
  s16x4 b = __builtin_amdgcn_ds_read_tr16_b64_v4i16((LAS s16x4*)(q + o1));
  return __builtin_shufflevector(a, b, 0, 1, 2, 3, 4, 5, 6, 7);
}
template <int KK> DEV void load_frags(Frags& f, LAS char* aH, LAS char* aT, LAS char* w0, LAS char* w1) {
#pragma unroll
  for (int t = 0; t < 5; ++t) f.x[1 + t] = *(LAS s16x8*)(aT + t * 2048);
  f.x[0] = *(LAS s16x8*)aH;
#pragma unroll
  for (int sec = 0; sec < 3; ++sec) {
    f.w[2 * sec] = tr_frag(w0, w0, sec * 8192 + KK * 4096, sec * 8192 + KK * 4096 + 512);
    f.w[2 * sec + 1] = tr_frag(w1, w1, sec * 8192 + KK * 4096, sec * 8192 + KK * 4096 + 512);
  }
}
template <int A, int B> DEV void mma_range(f32x4 (&acc)[6][6], const Frags& f, s16x8 e0) {
#pragma unroll
  for (int n = A; n < B; ++n) {
    const int p = n / 11, r = n % 11, w = 2 * p + (r >= 6), t = (r >= 6) ? 11 - r : 5 - r;
    acc[w][t] = (w < 4) ? mfma(f.w[w], (w == 0 && t == 0) ? e0 : f.x[t], acc[w][t]) : mfma(f.x[t], f.w[w], acc[w][t]);
  }
}
DEV void interleave() {
#pragma unroll
  for (int k = 0; k < 6; ++k) { __builtin_amdgcn_sched_group_barrier(0x008, 1, 0); __builtin_amdgcn_sched_group_barrier(0x100, 1, 0); }
#pragma unroll
  for (int k = 0; k < 6; ++k) {
    __builtin_amdgcn_sched_group_barrier(0x008, 1, 0); __builtin_amdgcn_sched_group_barrier(0x100, 1, 0);
    __builtin_amdgcn_sched_group_barrier(0x008, 1, 0); __builtin_amdgcn_sched_group_barrier(0x100, 1, 0);
    __builtin_amdgcn_sched_group_barrier(0x008, 1, 0);
  }
}

template <int E> DEV s16x8 col_frag(const u32x4* L) {
  constexpr u32 sel = (E & 1) ? 0x07060302u : 0x05040100u;
  u32x4 r;
  r.x = __builtin_amdgcn_perm(L[1][E >> 1], L[0][E >> 1], sel);
  r.y = __builtin_amdgcn_perm(L[3][E >> 1], L[2][E >> 1], sel);
  r.z = __builtin_amdgcn_perm(L[5][E >> 1], L[4][E >> 1], sel);
  r.w = __builtin_amdgcn_perm(L[7][E >> 1], L[6][E >> 1], sel);
  return as_frag(r);
}
DEV void mma_cols(f32x4* acc, const u32x4* L, s16x8 b) {
  acc[0] = mfma(col_frag<0>(L), b, acc[0]); acc[1] = mfma(col_frag<1>(L), b, acc[1]);
  acc[2] = mfma(col_frag<2>(L), b, acc[2]); acc[3] = mfma(col_frag<3>(L), b, acc[3]);
  acc[4] = mfma(col_frag<4>(L), b, acc[4]); acc[5] = mfma(col_frag<5>(L), b, acc[5]);
  acc[6] = mfma(col_frag<6>(L), b, acc[6]); acc[7] = mfma(col_frag<7>(L), b, acc[7]);
}

DEV void transpose_grp(u32x4* L) {
  const s16x8 f0 = col_frag<0>(L), f1 = col_frag<1>(L), f2 = col_frag<2>(L), f3 = col_frag<3>(L), f4 = col_frag<4>(L), f5 = col_frag<5>(L), f6 = col_frag<6>(L), f7 = col_frag<7>(L);
  L[0] = __builtin_bit_cast(u32x4, f0); L[1] = __builtin_bit_cast(u32x4, f1); L[2] = __builtin_bit_cast(u32x4, f2); L[3] = __builtin_bit_cast(u32x4, f3);
  L[4] = __builtin_bit_cast(u32x4, f4); L[5] = __builtin_bit_cast(u32x4, f5); L[6] = __builtin_bit_cast(u32x4, f6); L[7] = __builtin_bit_cast(u32x4, f7);
#pragma unroll
  for (int e = 0; e < 8; ++e) PIN(L[e]);
  SB();
}
DEV void mma_frags8(f32x4* acc, const u32x4* L, s16x8 b) {
#pragma unroll
  for (int e = 0; e < 8; ++e) acc[e] = mfma(as_frag(L[e]), b, acc[e]);
}
DEV void warm_line(u32 lds, const char* a) {
  asm volatile(DMA_HEAD "global_load_lds_dword %1, off" ::"s"(lds), "v"(a) : "memory");
}

__global__ void __launch_bounds__(512, 2) fused_kernel(Args Parg) {
  extern __shared__ __attribute__((aligned(128))) char smem[];
  u32x16 ka;
  u64 kb;
  asm volatile("s_load_dwordx16 %0, %2, 0x0\n\ts_load_dwordx2 %1, %2, 0x40" : "=&s"(ka), "=&s"(kb) : "s"(__builtin_amdgcn_kernarg_segment_ptr()));
  Params P;
  const char* pc0;
  asm volatile("s_getpc_b64 %0" : "=s"(pc0));
  LAS char* L = (LAS char*)smem;
  const u32 lbase = (u32)(uintptr_t)L;
  const int lane = threadIdx.x & 63;
  const int wave = rfl(threadIdx.x >> 6);
  const int bid = blockIdx.x, xg = bid & 7, loc = bid >> 3;
  const int np = 7 - (xg & 1);
  const bool gemm = loc < 4 * np;
  const int pl = loc >> 2, hl = loc & 3;
  const int pan = (xg & 1) * 7 + pl, hp = (xg >> 1) * 4 + hl, r0 = pan * 160;
  const int fi = loc - 4 * np;
  const bool dwn = (u32)(fi - 2 - 3 * (xg & 1)) <= 2u * (xg & 1);
  const int fu = (xg & 1) * 12 + (xg >> 1) * (3 + 2 * (xg & 1)) + min(fi, 2 + 2 * (xg & 1));
  const int du = (xg & 1) ? 4 + (xg >> 1) * 3 + fi - 5 : (xg >> 1);
  const int pan2 = (xg & 1) * 8 + (xg >> 1) * 2 + (loc >> 4);
  const int cg = (int)((((loc & 16) ? 0xfedc0a987653421bull : 0xfedc8a9b76534210ull) >> (4 * (loc & 15))) & 15), hrot = 2 * cg;
  const int g = lane >> 4, i = lane & 15;
  u64 kord;
  asm volatile("s_load_dwordx2 %0, %1, %2" : "=&s"(kord) : "s"(__builtin_amdgcn_kernarg_segment_ptr()), "s"(72 + (((xg & 1) * 32 + loc) & 63) * 8));
  rsrc_t rsWS;
  const u64 tstart = __builtin_amdgcn_s_memrealtime();
  u32 tag = 0;
  f32x4 oc[2][4];
  auto oc_init = [&]() {
    const int tl = (wave >> 1) + 64 * (wave & 1) + 2 * i;
    f32x2 st[2];
    u32x2 v[2][4];
#pragma unroll
    for (int tt = 0; tt < 2; ++tt) {
      st[tt] = *(LAS f32x2*)(L + L_STO + tl * 8 + tt * 256);
#pragma unroll
      for (int c = 0; c < 4; ++c) v[tt][c] = *(LAS u32x2*)(L + L_XT + tl * 128 + (((2 * c + (g >> 1)) ^ (i & 7)) << 4) + (g & 1) * 8 + tt * 4096);
    }
    SB();
#pragma unroll
    for (int tt = 0; tt < 2; ++tt) {
      const float k1 = 1.f + st[tt].y, k0 = -st[tt].x * st[tt].y;
#pragma unroll
      for (int c = 0; c < 4; ++c) oc[tt][c] = f32x4{bf_lo(v[tt][c].x), bf_hi(v[tt][c].x), bf_lo(v[tt][c].y), bf_hi(v[tt][c].y)} * k1 + k0;
    }
#pragma unroll
    for (int q = 0; q < 8; ++q) PIN(oc[q >> 2][q & 3]);
  };

  u32 tq[3], vO, vx;
  auto helper_offsets = [&]() {
    u32 lane = threadIdx.x & 63;
    PIN(lane);
    const int lr = lane >> 3, lc = lane & 7;
#pragma unroll
    for (int j = 0; j < 3; ++j) {
      const int rho = min(64 * j + (int)lane, 175), pr = rho >= 96;
      const int tk = r0 + ((rho < 16) ? rho - 16 : pr + 2 * (rho - 16 - 80 * pr));
      const u32 a = (u32)(pan2 * 128 + (rho & 127)) * 8, b = (((u32)tk >= (u32)S_) | (rho == 0)) ? 1u : (u32)tk * 8;
      tq[j] = (wave == 7) ? a : b;
      PIN(tq[j]);
    }
    vO = lr * 2048 + ((u32)(lc ^ (((lr >> 1) & 3) << 1)) << 4) + cg * 128;
    vx = lr * 2048 + ((u32)(lc ^ (lr >> 1)) << 4) + cg * 128;
    PIN(vO); PIN(vx);
  };
  auto wo_grp = [&](int q) { dma16x4(lbase + q * 4096, vO, vO + 15 * 1024, vO + 30 * 1024, vO + 45 * 1024, mk_rsrc(P.wo, D_ * D_ * 2), ((q + hrot) & 31) * 65536); };
  auto helper_fill = [&]() {
    const int lid = wave - 4;
    tag = (u32)rfl((int)lds_rd(L, L_TAG));
    if (gemm) { VMCNT(11); BAR(); }
    if (lid >= 2) {
      u32x2 sv[3];
#pragma unroll
      for (int q = 2 * lid - 4; q < 2 * lid - 2; ++q) dma16x4(lbase + L_XT + q * 4096, vx, (vx ^ 64) + 15 * 1024, vx + 30 * 1024, (vx ^ 64) + 45 * 1024, mk_rsrc(P.x, S_ * D_ * 2), (pan2 * 128 + 32 * q) * 2048);
#pragma unroll
      for (int j = 0; j < 3; ++j) sv[j] = ld_sc1_64(rsWS, tq[j] & ~7u, WS_SG);
      SB();
      if (gemm) { VMCNT(11); BAR(); }
      _Pragma("nounroll") for (int sp = 0; sp < SPIN_CAP; ++sp) {
        if (__all(sv[0].y == tag && sv[1].y == tag && sv[2].y == tag)) break;
        __builtin_amdgcn_s_sleep(2);
#pragma unroll
        for (int j = 0; j < 3; ++j) sv[j] = ld_sc1_64(rsWS, tq[j] & ~7u, WS_SG);
      }
      f32x2 st[3];
      u32 ta = lbase + ((lid == 3) ? L_STO + lane * 8 : L_STQ + lane * 4), tb2 = ta + ((lid == 3) ? 4 : 704);
      u32 tc = (lid == 2 && lane < 48) ? ta + 512 : lbase + L_UPD + lane * 4, td = (lid == 2 && lane < 48) ? tb2 + 512 : tc;
      const u32 ts = (lid == 3) ? 512 : 256;
#pragma unroll
      for (int j = 0; j < 3; ++j) {
        st[j] = f32x2{__uint_as_float((sv[j].x << 16) ^ ((lid == 3) ? 0u : 0x80000000u)), __builtin_amdgcn_rcpf(bf_hi(sv[j].x))};
        if (tq[j] & 1u) st[j] = f32x2{0.f, 0.f};
        PIN(st[j]);
      }
      u32 te = ta + ts, tf = tb2 + ts;
      PIN(ta); PIN(tb2); PIN(tc); PIN(td); PIN(te); PIN(tf);
      VMCNT(0);
      if (gemm) BAR();
      *(LAS float*)LP(ta) = st[0].x; *(LAS float*)LP(tb2) = st[0].y;
      *(LAS float*)LP(te) = st[1].x; *(LAS float*)LP(tf) = st[1].y;
      *(LAS float*)LP(tc) = st[2].x; *(LAS float*)LP(td) = st[2].y;
      LGKM0();
    } else {
      if (gemm) { VMCNT(0); BAR(); }
      wo_grp(12 + lid); wo_grp(14 + lid);
#pragma unroll
      for (int q = 0; q < 4; ++q) wo_grp(16 + 2 * q + lid);
      if (gemm) BAR();
    }
    if (gemm) BAR();
    int l2 = lid;
    asm volatile("" : "+s"(l2));
    wo_grp(l2); wo_grp(4 + l2); wo_grp(8 + l2);
    if (!dwn) { wo_grp(24 + l2); wo_grp(28 + l2); }
    if (lid == 2) {
      u32x2 sk;
      _Pragma("nounroll") for (int sp = 0; sp < SPIN_CAP; ++sp) {
        sk = ld_sc1_64(rsWS, (lane & 7) * 8, WS_SEL);
        if (__all(sk.y == tag)) break;
        __builtin_amdgcn_s_sleep(8);
      }
      if (lane < 8) lds_wr(L, L_SEL + 4 * lane, sk.x);
      LGKM0();
    }
  };
  {
    u32 ln = threadIdx.x & 63;
    PIN(ln);
    if (ln < 2) warm_line(lbase + L_UPD, pc0 + min((loc * 8 + wave) * 2 + (int)ln, CODE_LINES - 1) * 128);
  }
  const int lid = wave - 4;
  const bool isx = lid < 2;
  u32 v0 = 0, v1 = 0, v2 = 0, v3 = 0, vh = 0, g0 = 0, g1 = 0, g2 = 0, kstep = 0, d0 = 0;
  {
    int wave_a = wave;
    asm volatile("" : "+s"(wave_a));
    if (gemm && wave_a >= 4) {
      u32 ln = threadIdx.x & 63;
      PIN(ln);
      const int lr = ln >> 3, lc = ln & 7;
      const u32 sw = (u32)(lc ^ (isx ? lr >> 1 : lr & 2)) << 4;
      const u32 vb = lr * (isx ? 4096 : 6144) + sw + (isx ? (r0 + lid) * 2048 : hp * 128);
      const u32 pp = (isx ? 32768 : 49152) - 1024;
      v0 = vb; v1 = (vb ^ 64) + pp; v2 = vb + 2 * pp; v3 = (vb ^ 64) + 3 * pp;
      g0 = (lid == 3) ? 198656 : 0; g1 = isx ? 131072 : (lid == 3) ? 4096 : 196608; g2 = isx ? 262144 : (lid == 3) ? 200704 : 2048;
      vh = max(r0 - 16 + 8 * lid + lr, 0) * 2048 + (sw ^ (lid << 6));
      kstep = isx ? 128u : 64u * 6144u; d0 = lbase + (isx ? 2 + 10 * lid : 22 + 12 * (lid - 2)) * 1024;
      PIN(v0); PIN(v1); PIN(v2); PIN(v3); PIN(vh);
      asm volatile("" : "+s"(g0), "+s"(g1), "+s"(g2), "+s"(kstep), "+s"(d0));
    }
  }
  u32 ev;
  asm volatile("s_waitcnt lgkmcnt(0)" : "+s"(ka), "+s"(kb), "+s"(kord));
  {
    auto ptr = [&](int k) { return (char*)(GAS char*)(((u64)ka[2 * k + 1] << 32) | ka[2 * k]); };
    P.x = (const u16*)ptr(0); P.wqkv = (const u16*)ptr(1); P.wo = (const u16*)ptr(2); P.wr = (const u16*)ptr(3);
    P.wg = (const u16*)ptr(4); P.wu = (const u16*)ptr(5); P.wd = (const u16*)ptr(6); P.out = (u16*)ptr(7);
    P.ws = (char*)(GAS char*)kb;
  }
  rsWS = mk_rsrc(P.ws, WS_END);
  STAMP(0);
  if (wave < 4) {
    asm volatile("s_load_dword %0, %1, 0x0" : "=s"(ev) : "s"(P.ws + WS_EPOCH) : "memory");
    u32 ln = threadIdx.x & 63;
    PIN(ln);
    const u32 d = lbase + 2 * SLOT + wave * 6144, v = ln * 16;
    dma16x4(d, v, v, v, v, mk_rsrc(P.x, S_ * D_ * 2), (pan2 * 128 + cg * 8 + wave * 2) * 2048);
    dma16x2(d + 4096, v, v, mk_rsrc(P.wr, D_ * 2), 0);
  }
  SB();
  int wave_b = wave;
  asm volatile("" : "+s"(wave_b));
  if (gemm && wave_b >= 4) {
    const rsrc_t rs = mk_rsrc(isx ? P.x : P.wqkv, isx ? S_ * D_ * 2 : D_ * 3 * D_ * 2);
    auto issue_k = [&](u32 ko, u32 so) {
      dma16x4(d0 + so, v0, v1, v2, v3, rs, ko + g0);
      dma16x4(d0 + so + 4096, v0, v1, v2, v3, rs, ko + g1);
      if (isx) { dma16x2(d0 + so + 8192, v0, v1, rs, ko + g2); dma16(lbase + so + lid * 1024, vh, rs, ko); }
      else dma16x4(d0 + so + 8192, v0, v1, v2, v3, rs, ko + g2);
    };
    auto issue = [&](int s, u32 so) { issue_k(((u32)(kord >> (4 * s)) & 15u) * kstep, so); };
    LSTAMP(20);
    issue(0, 0);
    LSTAMP(21);
    helper_offsets();
    issue(1, SLOT);
    LSTAMP(22);
    if (isx) VMCNT(11); else VMCNT(12);
    LSTAMP(23);
    BAR();
    const bool helps = !isx && !(xg & 2) && hl != 3;
    if (helps) {
      constexpr int HL = (HELPN * 192 + 35) / 36;
      const int n = min(((pl * 3 + hl) * 2 + lid - 2) * HL + min(lane, HL - 1), HELPN * 192 - 1), row = n / 3;
      warm_line(lbase + L_UPD, (const char*)P.wqkv + (size_t)((u32)((HELPK >> (4 * (row >> 6))) & 15) * 64 + (row & 63)) * 6144 + (n % 3) * 2048 + (hp | 3) * 128);
    }
    issue(2, 2 * SLOT);
    u32 so = 0, sdum = 0;
    const int sn = min((pl * 4 + hl) * 4 + lid, 95) * 2;
    const u32 so0 = (sn / 3) * 6144 + (sn % 3) * 2048, so1 = ((sn + 1) / 3) * 6144 + ((sn + 1) % 3) * 2048;
    const char* sbase = (const char*)P.wqkv + (hp | 3) * 128;
    _Pragma("nounroll") for (int s = 0; s < 13; ++s) {
      if (helps && s == 0) VMCNT(13); else if (!isx || s == WARM_AT + 1 || s == WARM_AT + 2) VMCNT(12); else VMCNT(11);
      if (s == 0) LSTAMP(24);
      if (s == 1) LSTAMP(25);
      if (s == 2) LSTAMP(26);
      BAR();
      issue(s + 3, so);
      if (!(xg & 2) && (u32)(s - RS0) < RSN) {
        const u32 ko = (u32)((ROLLK >> (4 * (s - RS0))) & 15) * (64u * 6144u), a0 = ko + so0, a1 = ko + so1;
        asm volatile("s_load_dword %0, %1, %2\n\ts_load_dword %0, %1, %2 offset:64\n\ts_load_dword %0, %1, %3\n\ts_load_dword %0, %1, %3 offset:64"
                     : "+s"(sdum) : "s"(sbase), "s"(a0), "s"(a1) : "memory");
      }
      if (s == WARM_AT && isx) warm_line(lbase + L_UPD, (const char*)P.wo + (size_t)min((pan * 16 + hp) * 80 + lid * 40 + min(lane, 39), 16383) * 128);
      so = (so == 2 * SLOT) ? 0 : so + SLOT;
    }
    asm volatile("s_waitcnt lgkmcnt(0)" : "+s"(sdum));
    STAMP(2);
    helper_fill();
  }
  if (wave < 4) {
    __builtin_amdgcn_s_setprio(1);
    if (wave == 1) lds_wr(L, L_ONE + lane * 4, lane < 32 ? 0x3f803f80u : 0u);
    if (wave == 2) lds_wr(L, L_GO, 0u);
    f32x4 acc[6][6];
#pragma unroll
    for (int w = 0; w < 6; ++w)
#pragma unroll
      for (int t = (w & 1); t < 6; ++t) { acc[w][t] = f32x4{0.f, 0.f, 0.f, 0.f}; PIN(acc[w][t]); }
    f32x4 bprev, bhalo;
    u32 mc[2];
    u64 cur[4];
#pragma unroll
    for (int r = 0; r < 4; ++r) {
      const int kap = 4 * g + r, d = 16 + (wave & 1) + 2 * i - kap;
      bprev[r] = (kap - i >= 9) ? 0.f : -1e30f;
      bhalo[r] = (((d & 1) == 0) & (d <= 14) & (pan > 0)) ? 0.f : -1e30f;
      cur[r] = __ballot((u32)(i - kap) <= 7u);
      asm volatile("" : "+s"(cur[r]));
    }
    mc[0] = (((u32)(i - 4 * g) <= 7u) ? 0xffffu : 0u) | (((u32)(i - 4 * g - 1) <= 7u) ? 0xffff0000u : 0u);
    mc[1] = (((u32)(i - 4 * g - 2) <= 7u) ? 0xffffu : 0u) | (((u32)(i - 4 * g - 3) <= 7u) ? 0xffff0000u : 0u);
    u32 hxl = lbase + L_HX + (wave & 2) * 2304 + lane * 16;
    u32 obase = WS_O + (((wave & 1) * 32 + hp * 2 + (wave >> 1)) * 64 + pan * 5) * 1024 + lane * 16;
    PIN(bprev); PIN(bhalo); PIN(mc[0]); PIN(mc[1]); PIN(hxl); PIN(obase);
    SB();
    {
      const int tok = pan2 * 128 + cg * 8 + wave * 2 + (lane >> 5);
      u32x4 xv[4], wv[4];
      VMCNT(0);
      {
        LAS char* sp = L + 2 * SLOT + wave * 6144 + (lane & 31) * 16;
#pragma unroll
        for (int j = 0; j < 4; ++j) { xv[j] = *(LAS u32x4*)(sp + (lane >> 5) * 2048 + j * 512); wv[j] = *(LAS u32x4*)(sp + 4096 + j * 512); }
      }
      asm volatile("s_waitcnt lgkmcnt(0)" : "+s"(ev) :: "memory");
      const u32 e = ev;
      tag = e * 0x9E3779B1u + 0x7F4A7C15u; tag ^= tag >> 15; tag = (u32)rfl((int)(tag ^ (u32)(tag == e)));
      if (wave == 1) lds_wr(L, L_TAG, tag);
      float sx = 0.f, sxx = 0.f, sxw = 0.f, sw = 0.f;
#pragma unroll
      for (int j = 0; j < 4; ++j)
#pragma unroll
        for (int e2 = 0; e2 < 4; ++e2) {
          const u32 a = xv[j][e2], b = wv[j][e2];
          sx = dot2(a, 0x3f803f80u, sx); sxx = dot2(a, a, sxx); sxw = dot2(a, b, sxw); sw = dot2(b, 0x3f803f80u, sw);
        }
      const float mean = half_sum(sx) * (1.f / D_);
      const float var = fmaxf(half_sum(sxx) * (1.f / D_) - mean * mean, 0.f);
      const float mb = rbf(mean), sb = rbf(__builtin_amdgcn_sqrtf(rbf(rbf(var) + 1e-5f)));
      const u32 ro = ord16(pack2((half_sum(sxw) - mb * half_sum(sw)) * __builtin_amdgcn_rcpf(sb), 0.f) & 0xffffu);
      const u32 rlo = (u32)__builtin_amdgcn_readlane((int)ro, 31);
      if ((lane & 31) == 31) st_sc1_64(u32x2{pack2(mb, sb), tag}, rsWS, WS_SG + tok * 8);
      if (lane == 63) st_sc1_64(u32x2{rlo | (ro << 16), tag}, rsWS, WS_RG + (tok >> 1) * 8);
      if (!gemm) { LGKM0(); BAR(); }
    }
    STAMP(1);
    if (gemm) {
      const int h = wave >> 1, par = wave & 1;
        const int q4 = i >> 2, p4 = i & 3;
        u32 a0 = lbase + i * 128 + ((g ^ (i >> 1)) << 4);
        const u32 wsw = ((q4 >> 1) & 1) | ((g & 1) << 1);
        u32 w0 = lbase + WOFF + (8 * g + q4) * 128 + (((2 * h + par) ^ wsw) << 5) + p4 * 8;
        const u32 tb = (1 + 5 * par) * 2048;
        const u32 one = lbase + L_ONE;
        u32 pH = (i == 0) ? one : a0, pT = a0 + tb, pW0 = w0, pW1 = w0 ^ 32;
        PIN(a0); PIN(w0); PIN(pH); PIN(pT); PIN(pW0); PIN(pW1);
        Frags f0, f1;
        const u32 e1 = (i == 0) ? 0x3f803f80u : 0u;
        s16x8 e0 = as_frag(u32x4{e1, e1, e1, e1});
        PIN(e0);
        __builtin_amdgcn_s_waitcnt(0xc07f);
        BAR();
        STAMP(2); CSTAMP(14);
        load_frags<0>(f0, LP(pH), LP(pT), LP(pW0), LP(pW1));
        pH ^= 64; pT ^= 64;
        __builtin_amdgcn_s_waitcnt(0xc07f);
        u32 so = 0, trc = 0;
        auto half1 = [&]() {
          SB();
          load_frags<1>(f1, LP(pH), LP(pT), LP(pW0), LP(pW1));
          mma_range<0, 24>(acc, f0, e0);
          interleave();
          SB();
          so = (so == 2 * SLOT) ? 0 : so + SLOT;
          mma_range<24, 25>(acc, f0, e0);
          SB();
          pT = so + a0; pW0 = so + w0; PIN(pT); PIN(pW0);
          mma_range<25, 26>(acc, f0, e0);
          SB();
          pH = (i == 0) ? one : pT; pW1 = pW0 ^ 32; PIN(pH); PIN(pW1);
          mma_range<26, 27>(acc, f0, e0);
          SB();
          pT += tb; PIN(pT);
          mma_range<27, 28>(acc, f0, e0);
          SB();
        };
        auto half1b = [&](int s) {
          const u64 tnow = TICK(), rnow = RT();
          mma_range<28, 30>(acc, f0, e0);
          SB();
          __builtin_amdgcn_s_waitcnt(0xc07f);
          if (STAMPS) { trc = (lane == s) ? (u32)tnow : trc; trc = (lane == s + 16) ? (u32)rnow : trc; }
          mma_range<30, 31>(acc, f0, e0);
          SB();
          BAR();
          SB();
          mma_range<31, 33>(acc, f0, e0);
          SB();
        };
        half1();
        for (int s = 0; s < 15; ++s) {
          half1b(s);
          load_frags<0>(f0, LP(pH), LP(pT), LP(pW0), LP(pW1));
          mma_range<0, 24>(acc, f1, e0);
          interleave();
          SB();
          pH ^= 64; pT ^= 64; PIN(pH); PIN(pT);
          mma_range<24, 31>(acc, f1, e0);
          SB();
          __builtin_amdgcn_s_waitcnt(0xc07f);
          mma_range<31, 33>(acc, f1, e0);
          half1();
        }
        half1b(15);
        if (STAMPS && lane < 32) ((GAS u32*)(P.ws + WS_STAMP + (1 << 20)))[(bid * 4 + wave) * 32 + lane] = trc;
      mma_range<5, 6>(acc, f1, e0); mma_range<16, 17>(acc, f1, e0); mma_range<27, 28>(acc, f1, e0);
      SB();
      mma_range<11, 16>(acc, f1, e0);
      u32 tz = threadIdx.x;
      PIN(tz);
      const u32 i = tz & 15, g = (tz >> 4) & 3;
      u32 sqi = lbase + L_STQ + i * 4, sqg = lbase + L_STQ + g * 16;
      PIN(sqi); PIN(sqg);
      const u32 hx = hxl + par * 2304, hy = hxl + (par ^ 1) * 2304;
      u32 hxg = hx - i * 16, hyg = hy - i * 16, hxi = hx - g * 256, hyi = hy - g * 256, sqi2 = sqi + par * 320, sqg2 = sqg + par * 320;
      const u32 hqa = hx + 2048 - g * 240, hqb = hxl + (L_UPD - L_HX) - (wave & 2) * 2304;
      u32 hq = __builtin_amdgcn_inverse_ballot_w64(0x0001000100010001ull) ? hqa : hqb;
      PIN(hxg); PIN(hyg); PIN(hxi); PIN(hyi); PIN(sqi2); PIN(sqg2); PIN(hq);
#pragma unroll
      for (int k = 0; k < 5; ++k) { __builtin_amdgcn_sched_group_barrier(0x008, 1, 0); __builtin_amdgcn_sched_group_barrier(0x002, 2, 0); }
      SB();
      *(LAS f32x4*)LP(hx) = acc[2][0];
      *(LAS f32x4*)LP(hx + 1024) = acc[4][0];
      *(LAS f32x4*)LP(hq) = acc[0][0];
      mma_range<17, 22>(acc, f1, e0);
#pragma unroll
      for (int k = 0; k < 3; ++k) { __builtin_amdgcn_sched_group_barrier(0x008, 1, 0); __builtin_amdgcn_sched_group_barrier(0x200, 1, 0); }
      SB();
      mma_range<0, 3>(acc, f1, e0);
      SB();
      __builtin_amdgcn_s_waitcnt(0xc07f);
      BAR();
      SB();
      f32x4 cs[4];
      cs[2] = *(LAS f32x4*)LP(hxg); cs[3] = *(LAS f32x4*)LP(hyg);
      cs[0] = *(LAS f32x4*)LP(hxg + 2048 - g * 240); cs[1] = *(LAS f32x4*)LP(hyg + 2048 - g * 240);
      float m[6], rs[6];
#pragma unroll
      for (int t = 0; t < 6; ++t) { m[t] = *(LAS float*)LP((t ? sqi2 : sqi) + t * 64); rs[t] = *(LAS float*)LP((t ? sqi2 : sqi) + t * 64 + 704); }
      acc[3][0] = *(LAS f32x4*)LP(hy);
      const float cv[2] = {*(LAS float*)LP(hxi + 1024), *(LAS float*)LP(hyi + 1024)};
      acc[5][0] = *(LAS f32x4*)LP(hy + 1024);
      mma_range<3, 5>(acc, f1, e0); mma_range<6, 11>(acc, f1, e0);
#pragma unroll
      for (int k = 0; k < 7; ++k) { __builtin_amdgcn_sched_group_barrier(0x008, 1, 0); __builtin_amdgcn_sched_group_barrier(0x100, 2, 0); }
      SB();
      STAMP(3); CSTAMP(15);
      const float QS = 0.17677669529663687f * 1.4426950408889634f;
      const f32x4 z4 = {0.f, 0.f, 0.f, 0.f};
      f32x4 s0[5], s1[5], m4[6], r4[6];
      {
        s16x8 kf[6];
#pragma unroll
        for (int t = 5; t >= 0; --t) {
          const f32x4 k0 = (acc[2][t] + m[t] * cs[2]) * rs[t], k1 = (acc[3][t] + m[t] * cs[3]) * rs[t];
          kf[t] = as_frag(u32x4{pack2(k0[0], k0[1]), pack2(k0[2], k0[3]), pack2(k1[0], k1[1]), pack2(k1[2], k1[3])});
        }
        mma_range<22, 27>(acc, f1, e0); mma_range<28, 33>(acc, f1, e0);
#pragma unroll
        for (int k = 0; k < 10; ++k) { __builtin_amdgcn_sched_group_barrier(0x008, 1, 0); __builtin_amdgcn_sched_group_barrier(0x002, 3, 0); }
        SB();
#pragma unroll
        for (int t = 0; t < 6; ++t) { m4[t] = *(LAS f32x4*)LP((t ? sqg2 : sqg) + t * 64); r4[t] = *(LAS f32x4*)LP((t ? sqg2 : sqg) + t * 64 + 704); }
#pragma unroll
        for (int t = 0; t < 5; ++t) {
          const f32x4 q0 = acc[0][t + 1] + m[t + 1] * cs[0], q1 = acc[1][t + 1] + m[t + 1] * cs[1];
          const s16x8 qf = as_frag(u32x4{pack2(q0[0], q0[1]), pack2(q0[2], q0[3]), pack2(q1[0], q1[1]), pack2(q1[2], q1[3])});
          s0[t] = mfma(kf[t], qf, t == 0 ? bhalo : bprev); s1[t] = mfma(kf[t + 1], qf, z4);
        }
      }
      SB();
      u32x16 vp[2];
#pragma unroll
      for (int t = 0; t < 6; ++t) {
#pragma unroll
        for (int c = 0; c < 2; ++c) {
          const f32x4 v = (acc[4 + c][t] + m4[t] * cv[c]) * r4[t];
          vp[c][2 * t] = pack2(v[0], v[1]); vp[c][2 * t + 1] = pack2(v[2], v[3]);
        }
      }
      PIN(vp[0]); PIN(vp[1]);
      float l[5][4], mx[5], rq[5];
#pragma unroll
      for (int t = 0; t < 5; ++t) {
#pragma unroll
        for (int r = 0; r < 4; ++r) l[t][r] = __builtin_amdgcn_inverse_ballot_w64(cur[r]) ? s1[t][r] : s0[t][r];
        mx[t] = vmax3(vmax(l[t][0], l[t][1]), l[t][2], l[t][3]);
        rq[t] = rs[t + 1] * QS;
      }
#pragma unroll
      for (int t = 0; t < 4; t += 2) {
        const auto r = __builtin_amdgcn_permlane16_swap(__float_as_uint(mx[t]), __float_as_uint(mx[t + 1]), false, false);
        const u32 r0 = r[0], r1 = r[1];
        const float z = max32(vmax(__uint_as_float(r0), __uint_as_float(r1)));
        const auto b = __builtin_amdgcn_permlane16_swap(__float_as_uint(z), __float_as_uint(z), false, false);
        const u32 b0 = b[0], b1 = b[1];
        mx[t] = -__uint_as_float(b0) * rq[t]; mx[t + 1] = -__uint_as_float(b1) * rq[t + 1];
      }
      mx[4] = -max32(max16(mx[4])) * rq[4];
      const s16x8 ones = as_frag(u32x4{0x3f803f80u, 0x3f803f80u, 0x3f803f80u, 0x3f803f80u});
      f32x4 sm[5], o0[5], o1[5];
#pragma unroll
      for (int t = 0; t < 5; ++t) {
#pragma unroll
        for (int r = 0; r < 4; ++r) l[t][r] = __builtin_amdgcn_exp2f(__builtin_fmaf(l[t][r], rq[t], mx[t]));
        const u32 p01 = pack2(l[t][0], l[t][1]), p23 = pack2(l[t][2], l[t][3]);
        const s16x8 pp = as_frag(u32x4{p01 & ~mc[0], p23 & ~mc[1], p01 & mc[0], p23 & mc[1]});
        sm[t] = mfma(ones, pp, z4);
        o0[t] = mfma(as_frag(u32x4{vp[0][2 * t], vp[0][2 * t + 1], vp[0][2 * t + 2], vp[0][2 * t + 3]}), pp, z4);
        o1[t] = mfma(as_frag(u32x4{vp[1][2 * t], vp[1][2 * t + 1], vp[1][2 * t + 2], vp[1][2 * t + 3]}), pp, z4);
      }
#pragma unroll
      for (int t = 0; t < 5; ++t) {
        const float inv = __builtin_amdgcn_rcpf(sm[t][0]);
        o0[t] *= inv; o1[t] *= inv;
        st_sc1_128(u32x4{pack2(o0[t][0], o0[t][1]), pack2(o0[t][2], o0[t][3]), pack2(o1[t][0], o1[t][1]), pack2(o1[t][2], o1[t][3])}, rsWS,
                   (t == 4) ? obase + ((pan == 12) ? 0x40000000u : 4096u) : obase + t * 1024);
      }
      STAMP(8);
    }
  }
  if (!gemm) {
  if (dwn) {
    const rsrc_t rsD = mk_rsrc(P.wd, F_ * D_ * 2);
    const int k0 = (du >> 1) * 14 + (wave >> 2) * 7;
    const u32 vd = (g * D_ + (du & 1) * 512 + (wave & 3) * 128 + 8 * i) * 2;
    auto drow = [&](int q, int j) -> u32 { return (u32)(64 * ((k0 + q) >> 1) + 4 * ((k0 + q) & 1) + 8 * j) * (D_ * 2); };
    constexpr u32 D_W = 24 * 4096, D_H = D_W + 8 * 4352;
    u32x4 wb[56];
#pragma unroll
    for (int j = 0; j < 8; ++j) wb[j] = ld_nt_128(rsD, vd, drow(0, j));
    SB();
    if (wave >= 4) { BAR(); tag = (u32)rfl((int)lds_rd(L, L_TAG)); }
#pragma unroll
    for (int j = 8; j < 48; ++j) wb[j] = ld_nt_128(rsD, vd, drow(j >> 3, j & 7));
    SB();
    transpose_grp(wb);
#pragma unroll
    for (int e = 0; e < 4; ++e) *(LAS u32x4*)(L + D_W + wave * 4352 + e * 1024 + lane * 16) = wb[e];
    SB();
#pragma unroll
    for (int q = 1; q < 6; ++q) transpose_grp(wb + 8 * q);
#pragma unroll
    for (int j = 48; j < 56; ++j) wb[j] = ld_nt_128(rsD, vd, drow(6, j & 7));
    SB();
    __builtin_amdgcn_s_setprio(0);
    if (wave >= 4) { helper_offsets(); helper_fill(); }
    transpose_grp(wb + 48);
    VMCNT(0);
    STAMP(12);
    BAR();
    STAMP(11);
    {
      wait_until(tstart, T_H);
      const int qa = 2 * (wave & 3), qb = min(qa + 1, 6);
      u32x4 va, vb;
      _Pragma("nounroll") for (int sp = 0; sp < SPIN_CAP; ++sp) {
        va = ld_sc1_128(rsWS, lane * 16, WS_H + (k0 + qa) * 1024);
        vb = ld_sc1_128(rsWS, lane * 16, WS_H + (k0 + qb) * 1024);
        if (__all(va.y == tag && va.w == tag && vb.y == tag && vb.w == tag)) break;
        __builtin_amdgcn_s_sleep(1);
      }
      *(LAS u32x2*)(L + D_H + ((wave >> 2) * 7 + qa) * 512 + (lane & 31) * 16 + (lane >> 5) * 8) = u32x2{va.x, va.z};
      *(LAS u32x2*)(L + D_H + ((wave >> 2) * 7 + qb) * 512 + (lane & 31) * 16 + (lane >> 5) * 8) = u32x2{vb.x, vb.z};
      LGKM0();
      lds_wr(L, L_HR + wave * 4, 1);
      lds_wait_set(L, L_HR + (wave & 4) * 4 + (lane & 3) * 4);
    }
    STAMP(2);
    f32x4 ga[8];
#pragma unroll
    for (int e = 0; e < 8; ++e) ga[e] = f32x4{0.f, 0.f, 0.f, 0.f};
    {
      s16x8 hf = *(LAS s16x8*)(L + D_H + ((wave >> 2) * 7 + 6) * 512 + (g * 8 + (i & 7)) * 16);
#pragma unroll
      for (int q = 6; q >= 0; --q) {
        s16x8 hn = hf;
        if (q > 0) hn = *(LAS s16x8*)(L + D_H + ((wave >> 2) * 7 + q - 1) * 512 + (g * 8 + (i & 7)) * 16);
        mma_frags8(ga, wb + q * 8, hf);
        if (q == 5) {
#pragma unroll
          for (int e = 0; e < 4; ++e) wb[e] = *(LAS u32x4*)(L + D_W + wave * 4352 + e * 1024 + lane * 16);
        }
        SB();
        hf = hn;
      }
    }
    STAMP(4);
    {
      u32 ln = threadIdx.x & 63;
      PIN(ln);
      if ((ln & 15) < 8) {
        const u32 o = D_W + wave * 4352 + (ln & 7) * 64 + (((ln >> 4) ^ ((ln >> 1) & 3)) << 4);
#pragma unroll
        for (int e = 0; e < 8; ++e) *(LAS f32x4*)(L + o + e * 512) = ga[e];
      }
    }
    LGKM0();
    lds_wr(L, L_PW + 4 * wave, 1);
    if (wave >= 4) {
      {
        u32 ln = threadIdx.x & 63;
        PIN(ln);
        const u32 gq = 2 * (ln >> 5) + ((ln >> 1) & 1), o = lbase + D_W + (wave - 4) * 4352 + ((ln >> 2) & 7) * 512 + (ln & 1) * 8;
        u32 a[4], oy = WS_YS + (du >> 1) * 65536 + (du & 1) * 4096 + (wave - 4) * 1024 + ln * 16;
#pragma unroll
        for (int k = 0; k < 4; ++k) { a[k] = o + k * 128 + ((gq ^ k) << 4); PIN(a[k]); }
        PIN(oy);
        lds_wait_set(L, L_PW + (wave - 4) * 4);
        f32x2 p[8], q[8];
#pragma unroll
        for (int tk = 0; tk < 8; ++tk) { p[tk] = *(LAS f32x2*)(LP(a[tk >> 1]) + (tk & 1) * 64); q[tk] = *(LAS f32x2*)(LP(a[tk >> 1]) + (tk & 1) * 64 + 4 * 4352); }
        SB();
#pragma unroll
        for (int tk = 0; tk < 8; ++tk) {
          const f32x2 y = p[tk] + q[tk];
          st_sc1_128(u32x4{__float_as_uint(y.x), tag, __float_as_uint(y.y), tag}, rsWS, oy + tk * 8192);
        }
      }
      LGKM0();
      lds_wr(L, L_HS + 4 * (wave - 4), 1);
      lds_wait_set(L, L_HS + (lane & 3) * 4);
      u32 ln = threadIdx.x & 63;
      PIN(ln);
      vO = (ln >> 3) * 2048 + (((ln & 7) ^ (((ln >> 4) & 3) << 1)) << 4) + cg * 128;
      wo_grp(20 + wave); wo_grp(24 + wave);
      VMCNT(0);
      lds_wr(L, L_HS + 4 * (wave - 4), 2);
    }
    STAMP(8);
  } else {
    const int lr = lane >> 3, lc = lane & 7;
    const int fv = fu - (fu > 2);
    const bool two = fu != 2 && fv < 24;
    const int la = (fu == 2) ? 50 : (fv >= 24) ? (fv - 24) * 8 + 3 : (fv >= 21) ? (fv - 21) * 16 + 2 : (fv / 3) * 8 + ((fv % 3) ? 2 + 2 * (fv % 3) : 0);
    const int lb = la + ((fv >= 21) ? 8 : 1);
    const rsrc_t rsG = mk_rsrc(P.wg, D_ * F_ * 2), rsU = mk_rsrc(P.wu, D_ * F_ * 2), rsX = mk_rsrc(P.x, S_ * D_ * 2);
    const u32 vg = (i < 8 || two) ? ((wave * 128 + 8 * g) * F_ + ((i < 8) ? la : lb) * 64 + 8 * (i & 7)) * 2 : 0x7f000000u;
    u32x4 wb[8 * GWIN];
    auto ldgrp = [&](int q) {
#pragma unroll
      for (int j = 0; j < 8; ++j) wb[(q % GWIN) * 8 + j] = ld_nt_128((q & 1) ? rsU : rsG, vg, ((q >> 1) * 32 + j) * (F_ * 2));
      SB();
    };
    __builtin_amdgcn_s_setprio(0);
    ldgrp(0);
    ldgrp(1);
    if (wave >= 4) { BAR(); tag = (u32)rfl((int)lds_rd(L, L_TAG)); }
    if (fu == 2) {
      *(LAS u32x4*)(L + 32 + threadIdx.x * 16) = u32x4{0u, 0u, 0u, 0u};
      if (threadIdx.x < 4) *(LAS u32x4*)(L + 32 + 8192 + threadIdx.x * 16) = u32x4{0u, 0u, 0u, 0u};
      wait_until(tstart, T_GATHER);
      u32x4 v;
      _Pragma("nounroll") for (int sp = 0; sp < SPIN_CAP; ++sp) {
        v = ld_sc1_128(rsWS, lane * 16, WS_RG + wave * 1024);
        if (__all(v.y == tag && v.w == tag)) break;
        __builtin_amdgcn_s_sleep(2);
      }
      const u32 ib = 2047 - 256 * wave - 4 * lane;
      const u32 key[4] = {(v.x << 16) | ib, (v.x & 0xffff0000u) | (ib - 1), (v.z << 16) | (ib - 2), (v.z & 0xffff0000u) | (ib - 3)};
      lds_wr(L, wave * 4, wave_max_u32(max(max(key[0], key[1]), max(key[2], key[3]))));
      LGKM0();
      BAR();
      STAMP(9);
      const u32x4 m0 = *(LAS u32x4*)L, m1 = *(LAS u32x4*)(L + 16);
      const u32 thr = min(min(min(m0.x, m0.y), min(m0.z, m0.w)), min(min(m1.x, m1.y), min(m1.z, m1.w)));
      const u32 c0 = key[0] >= thr, c1 = key[1] >= thr, c2 = key[2] >= thr, c3 = key[3] >= thr;
      if (c0 | c1 | c2 | c3) {
        const u32 p = 64 + 4 * __hip_atomic_fetch_add((LAS u32*)(L + 32), c0 + c1 + c2 + c3, __ATOMIC_RELAXED, __HIP_MEMORY_SCOPE_WORKGROUP);
        lds_wr(L, c0 ? p : 36, key[0]); lds_wr(L, c1 ? p + 4 * c0 : 36, key[1]);
        lds_wr(L, c2 ? p + 4 * (c0 + c1) : 36, key[2]); lds_wr(L, c3 ? p + 4 * (c0 + c1 + c2) : 36, key[3]);
      }
      LGKM0();
      BAR();
      const int n = rfl((int)lds_rd(L, 32));
      _Pragma("nounroll") for (int b = threadIdx.x; b < n; b += 512) {
        const u32 my = lds_rd(L, 64 + 4 * b);
        int rank = 0;
        _Pragma("nounroll") for (int t = 0; t < n; t += 8) {
          const u32x4 o = *(LAS u32x4*)(L + 64 + 4 * t), q = *(LAS u32x4*)(L + 80 + 4 * t);
          rank += (o.x > my) + (o.y > my) + (o.z > my) + (o.w > my) + (q.x > my) + (q.y > my) + (q.z > my) + (q.w > my);
        }
        if (rank < 8) st_sc1_64(u32x2{my ^ 0x7ffu, tag}, rsWS, WS_SEL + rank * 8);
      }
      if (threadIdx.x == 0) st_sc1_32(tag, rsWS, WS_EPOCH);
      STAMP(10);
      LGKM0();
      BAR();
    }
    constexpr u32 F_XS = 81920;
    ldgrp(2);
    if (wave == 0) {
      wait_until(tstart, T_SEL);
      u32x2 sk;
      _Pragma("nounroll") for (int sp = 0; sp < SPIN_CAP; ++sp) {
        sk = ld_sc1_64(rsWS, lr * 8, WS_SEL);
        if (__all(sk.y == tag)) break;
        __builtin_amdgcn_s_sleep(4);
      }
      STAMP(2);
      u32x4 xr[16];
#pragma unroll
      for (int j = 0; j < 16; ++j) xr[j] = __builtin_amdgcn_raw_buffer_load_b128(rsX, (sk.x & 0x7ffu) * 2048 + lc * 16, j * 128, 0);
      u32x2 ms;
      _Pragma("nounroll") for (int sp = 0; sp < SPIN_CAP; ++sp) {
        ms = ld_sc1_64(rsWS, (sk.x & 0x7ffu) * 8, WS_SG);
        if (__all(ms.y == tag)) break;
      }
#pragma unroll
      for (int j = 0; j < 16; ++j) *(LAS u32x4*)(L + F_XS + lr * 2064 + j * 128 + lc * 16) = xr[j];
      *(LAS u32*)(L + F_XS + 8 * 2064 + lr * 4) = ms.x;
      LGKM0();
      lds_wr(L, L_HR, 1);
    }
#pragma unroll
    for (int q = 3; q < GWIN; ++q) ldgrp(q);
#pragma unroll
    for (int q = 0; q < GPRE; ++q) transpose_grp(wb + 8 * q);
    lds_wait_set(L, L_HR);
    f32x4 ga[16];
#pragma unroll
    for (int e = 0; e < 16; ++e) ga[e] = f32x4{0.f, 0.f, 0.f, 0.f};
    const u32 ms = *(LAS u32*)(L + F_XS + 8 * 2064 + (i & 7) * 4);
    const float rsn = __builtin_amdgcn_rcpf(bf_hi(ms)), mrs = -bf_lo(ms) * rsn;
    s16x8 xf;
#pragma unroll
    for (int q = 0; q < 8; ++q) {
      if ((q & 1) == 0) {
        const u32x4 xv = *(LAS u32x4*)(L + F_XS + (i & 7) * 2064 + (wave * 128 + (q >> 1) * 32 + 8 * g) * 2);
        u32x4 xn;
#pragma unroll
        for (int e = 0; e < 4; ++e) xn[e] = pack2(__builtin_fmaf(bf_lo(xv[e]), rsn, mrs), __builtin_fmaf(bf_hi(xv[e]), rsn, mrs));
        xf = as_frag(xn);
      }
      if (q < GPRE) mma_frags8(ga + 8 * (q & 1), wb + q * 8, xf); else mma_cols(ga + 8 * (q & 1), wb + (q % GWIN) * 8, xf);
      SB();
      if (q == 1 || q == 2) ldgrp(q + 5);
    }
    STAMP(4);
    {
      u32 ln = threadIdx.x & 63;
      PIN(ln);
      if ((ln & 15) < 8) {
        const u32 o = wave * 10240 + (ln & 7) * 80 + (ln >> 4) * 16;
#pragma unroll
        for (int e = 0; e < 16; ++e) *(LAS f32x4*)(L + o + e * 640) = ga[e];
      }
    }
    {
      u32 ln = threadIdx.x & 63;
      PIN(ln);
      const u32 e = 4 * ((wave >> 1) & 1) + (ln >> 4), tk = (ln >> 1) & 7, m = 8 * (wave >> 2) + 4 * (wave & 1) + 2 * (ln & 1);
      u32 o0 = lbase + (e * 8 + tk) * 80 + m * 4, o1 = o0 + 40960, oh = WS_H + ((wave < 4) ? la : lb) * 2048 + (wave & 3) * 512 + ln * 8;
      PIN(o0); PIN(o1); PIN(oh);
      LGKM0();
      BAR();
      f32x2 pg[8], pu[8];
#pragma unroll
      for (int w = 0; w < 8; ++w) { pg[w] = *(LAS f32x2*)(LP((w < 4) ? o0 : o1) + (w & 3) * 10240); pu[w] = *(LAS f32x2*)(LP((w < 4) ? o0 : o1) + (w & 3) * 10240 + 5120); }
      SB();
      const f32x2 gs = ((pg[0] + pg[1]) + (pg[2] + pg[3])) + ((pg[4] + pg[5]) + (pg[6] + pg[7])), us = ((pu[0] + pu[1]) + (pu[2] + pu[3])) + ((pu[4] + pu[5]) + (pu[6] + pu[7]));
      const float g0 = rbf(gs.x), g1 = rbf(gs.y);
      const u32 hv = pack2(rbf(g0 * sigm(g0)) * rbf(us.x), rbf(g1 * sigm(g1)) * rbf(us.y));
      if (wave < 4 || two) st_sc1_64(u32x2{hv, tag}, rsWS, oh);
    }
    LGKM0();
    BAR();
    STAMP(8);
    if (wave >= 4) { helper_offsets(); helper_fill(); }
  }
  }

  if (wave >= 4) {
    const int lid = wave - 4;
    if (!dwn) {
      VMCNT(0);
      STAMP(3);
      BAR();
    }
    if (lid >= 2) {
      const u32 sk = lds_rd(L, L_SEL + 4 * (lane & 7));
      _Pragma("nounroll") for (int c = lid - 2; c < 8; c += 2) {
        const u32 tk = (u32)__builtin_amdgcn_readlane((int)sk, c) & 0x7ffu;
        if ((int)(tk >> 7) != pan2) continue;
        float y0, y1;
        _Pragma("nounroll") for (int sp = 0; sp < SPIN_CAP; ++sp) {
          u32 bad = 0;
          y0 = 0.f; y1 = 0.f;
#pragma unroll
          for (int j = 0; j < 4; ++j) {
            const u32x4 v = ld_sc1_128(rsWS, (lane >> 5) * 65536 + (lane & 31) * 16, WS_YS + j * 131072 + c * 8192 + cg * 512);
            bad |= (v.y ^ tag) | (v.w ^ tag);
            y0 += __uint_as_float(v.x); y1 += __uint_as_float(v.z);
          }
          if (__all(bad == 0)) break;
          __builtin_amdgcn_s_sleep(8);
        }
        y0 = sum32(y0); y1 = sum32(y1);
        if (lane < 32) {
          const u32 o = L_UPD + (c * 64 + 16 * (lane & 3) + (lane >> 2)) * 4;
          *(LAS float*)(L + o) = y0; *(LAS float*)(L + o + 32) = y1;
        }
      }
      LGKM0();
      lds_wr(L, L_UPDF + 4 * (lid - 2), 1);
      STAMP(7);
    }
    return;
  }

  {
    const bool pol = wave == 0;
    const u32 fo = WS_OF + (((pan2 * 4 + 3 * (lane >> 5)) / 5) * 16 + (lane & 15)) * 128;
    u32 fv = ~tag;
    if (dwn && pol) fv = ld_sc1_32(rsWS, fo, 0);
    SB();
    if (!gemm && !dwn) BAR();
    const int par2 = wave >> 1, t2 = (wave & 1) * 2;
    u32 wa[4], wc[4];
    s16x8 wf[3][4];
    auto wo_addr = [&]() {
      u32 ln = threadIdx.x & 63;
      PIN(ln);
      const u32 g = ln >> 4, i = ln & 15, q4 = i >> 2, p4 = i & 3;
      const u32 wb = lbase + (4 * g + q4) * 128 + p4 * 8 + par2 * 2048, wsw = (2 * g + (q4 >> 1)) & 3;
#pragma unroll
      for (int c = 0; c < 4; ++c) { wa[c] = wb + ((c ^ wsw) << 5); wc[c] = wa[c] ^ 2048; PIN(wa[c]); PIN(wc[c]); }
    };
    auto wf_first = [&]() {
#pragma unroll
      for (int k = 0; k < 2; ++k)
#pragma unroll
        for (int c = 0; c < 4; ++c) wf[k][c] = tr_frag(LP(wa[c]), LP(wc[c]), k * 4096, k * 4096);
    };
    if (gemm) oc_init();
    SB();
    if (gemm) {
      VMCNT(0);
      if (lane == 0 && __hip_atomic_fetch_add((LAS u32*)(L + L_ARR), 1u, __ATOMIC_RELAXED, __HIP_MEMORY_SCOPE_WORKGROUP) == 3)
        st_sc1_32(tag, rsWS, WS_OF + (pan * 16 + hp) * 128);
      STAMP(4);
      SB();
      wo_addr();
      BAR();
      wf_first();
    }
    const int T0 = pan2 * 4 + t2;
    const u32 sk = lds_rd(L, L_SEL + 4 * (lane & 7));
    __builtin_amdgcn_s_waitcnt(0xc07f);
    SB();
    if (!gemm) wait_until(tstart, T_O);
    if (pol) {
      _Pragma("nounroll") for (int sp = 0; sp < SPIN_CAP; ++sp) {
        if (__all(fv == tag)) break;
        if (sp) __builtin_amdgcn_s_sleep(2);
        fv = ld_sc1_32(rsWS, fo, 0);
      }
      lds_wr(L, L_GO, 1);
    } else {
      _Pragma("nounroll") for (int sp = 0; sp < SPIN_CAP; ++sp) if (rfl((int)lds_rd(L, L_GO))) break;
    }
    CBAR();
    STAMP(5);
    const u32 abase = WS_O + (par2 * 32 * 64 + T0) * 1024;
    u32x4 af[OWIN][2];
#pragma unroll
    for (int hh = 0; hh < OWIN; ++hh) {
#pragma unroll
      for (int tt = 0; tt < 2; ++tt) af[hh][tt] = ld_sc1_128(rsWS, lane * 16 + tt * 1024, abase + ((hh + hrot) & 31) * 65536);
      SB();
    }
    if (!gemm) {
      wo_addr(); wf_first();
      SB();
      oc_init();
      __builtin_amdgcn_s_waitcnt(0xc07f);
      SB();
    }
#pragma unroll
    for (int hh = 0; hh < 32; ++hh) {
      if (hh == 21 && dwn) {
        _Pragma("nounroll") for (int sp = 0; sp < SPIN_CAP; ++sp) if (__all(lds_rd(L, L_HS + (lane & 3) * 4) == 2u)) break;
        CBAR(); SB();
      }
      if (hh == 14) {
#pragma unroll
        for (int c = 0; c < 4; ++c) { wa[c] += 65536; wc[c] += 65536; PIN(wa[c]); PIN(wc[c]); }
        SB();
      }
      if (hh == 31) __builtin_amdgcn_s_waitcnt(0xc07f); else __builtin_amdgcn_s_waitcnt(0xc87f);
      SB();
      if (hh + 2 < 32) {
#pragma unroll
        for (int c = 0; c < 4; ++c) wf[(hh + 2) % 3][c] = tr_frag(LP(wa[c]), LP(wc[c]), ((hh + 2) & 15) * 4096, ((hh + 2) & 15) * 4096);
      }
#pragma unroll
      for (int tt = 0; tt < 2; ++tt) {
#pragma unroll
        for (int c = 0; c < 4; ++c) oc[tt][c] = mfma(wf[hh % 3][c], as_frag(af[hh % OWIN][tt]), oc[tt][c]);
        if (hh + OWIN < 32) af[hh % OWIN][tt] = ld_sc1_128(rsWS, lane * 16 + tt * 1024, abase + ((hh + OWIN + hrot) & 31) * 65536);
      }
#pragma unroll
      for (int k = 0; k < 2; ++k) {
        __builtin_amdgcn_sched_group_barrier(0x008, 1, 0); __builtin_amdgcn_sched_group_barrier(0x100, 2, 0);
        __builtin_amdgcn_sched_group_barrier(0x008, 1, 0); __builtin_amdgcn_sched_group_barrier(0x100, 1, 0);
        __builtin_amdgcn_sched_group_barrier(0x008, 1, 0); __builtin_amdgcn_sched_group_barrier(0x100, 1, 0);
        __builtin_amdgcn_sched_group_barrier(0x008, 1, 0); __builtin_amdgcn_sched_group_barrier(0x020, 1, 0);
      }
      SB();
    }
    STAMP(6);
    {
      const u32 stl = (sk & 0x7ffu) - (u32)(pan2 * 128 + par2 + 32 * t2);
      u64 hit = __ballot(stl < 64u && (stl & 1u) == 0u) & 0xffull;
      if (hit) {
        lds_wait_set(L, L_UPDF + (lane & 1) * 4);
        while (hit) {
          const int c = __builtin_ctzll(hit);
          hit &= hit - 1;
          const u32 tl = (u32)__builtin_amdgcn_readlane((int)stl, c), ob = (u32)__builtin_amdgcn_readlane((int)sk, c) >> 16;
          const float sg = rbf(sigm(bf_lo((ob & 0x8000u) ? (ob & 0x7fffu) : (~ob & 0xffffu))));
          const float f0 = ((tl >> 1) == (u32)i) ? sg : 0.f, f1 = ((tl >> 1) == (u32)i + 16u) ? sg : 0.f;
#pragma unroll
          for (int cc = 0; cc < 4; ++cc) {
            const f32x4 u = *(LAS f32x4*)(L + L_UPD + (c * 64 + 16 * cc + 4 * g) * 4);
            oc[0][cc] += u * f0; oc[1][cc] += u * f1;
          }
        }
      }
    }
    const rsrc_t rsO = mk_rsrc(P.out, S_ * D_ * 2);
    const u32 xo = ((pan2 * 128 + par2 + 32 * t2 + 2 * i) * D_ + cg * 64 + (g & 1) * 16 + (g >> 1) * 8) * 2;
#pragma unroll
    for (int tt = 0; tt < 2; ++tt)
#pragma unroll
      for (int cp = 0; cp < 2; ++cp) {
        const u32 a0 = pack2(oc[tt][2 * cp][0], oc[tt][2 * cp][1]), a1 = pack2(oc[tt][2 * cp][2], oc[tt][2 * cp][3]);
        const u32 b0 = pack2(oc[tt][2 * cp + 1][0], oc[tt][2 * cp + 1][1]), b1 = pack2(oc[tt][2 * cp + 1][2], oc[tt][2 * cp + 1][3]);
        const auto e0 = __builtin_amdgcn_permlane16_swap(a0, b0, false, false);
        const auto e1 = __builtin_amdgcn_permlane16_swap(a1, b1, false, false);
        st_sc1_128(u32x4{e0[0], e1[0], e0[1], e1[1]}, rsO, xo + tt * 65536 + cp * 64);
      }
    STAMP(7);
  }
}

static u64 k_order(int odd, int pl, int hl) {
  const int np = 7 - odd;
  int slot[16], used[16];
  for (int k = 0; k < 16; ++k) { slot[k] = -1; used[k] = 0; }
  for (int k = 2; k < 16; ++k) {
    const int h0 = k & 3, p0 = (k * (odd ? 5 : 3)) % np;
    int want = -1;
    if (hl == h0 && pl % np == p0) want = k - 2; else if (hl == h0 || pl % np == p0) want = k - 1;
    if (want >= 0) {
      if (want < 1) want = 1;
      while (want > 1 && used[want]) --want;
      if (!used[want]) { slot[k] = want; used[want] = 1; }
    }
  }
  int s = 0;
  for (int k = 0; k < 16; ++k) if (slot[k] < 0) { while (used[s]) ++s; slot[k] = s; used[s] = 1; }
  u64 r = 0;
  for (int k = 0; k < 16; ++k) r |= (u64)k << (4 * slot[k]);
  return r;
}

extern "C" void kernel_launch(void* const* d_in, const int* in_sizes, int n_in, void* d_out, int out_size, void* d_ws, size_t ws_size,
                              hipStream_t stream) {
  Args A;
  Params& P = A.p;
  P.x = (const u16*)d_in[0]; P.wqkv = (const u16*)d_in[1]; P.wo = (const u16*)d_in[2]; P.wr = (const u16*)d_in[4];
  P.wg = (const u16*)d_in[5]; P.wu = (const u16*)d_in[6]; P.wd = (const u16*)d_in[7];
  P.out = (u16*)d_out; P.ws = (char*)d_ws;
  for (int e = 0; e < 64; ++e) A.kt[e] = k_order(e >> 5, (e >> 2) & 7, e & 3);
  (void)hipFuncSetAttribute((const void*)fused_kernel, hipFuncAttributeMaxDynamicSharedMemorySize, L_END + 64);
  hipLaunchKernelGGL(fused_kernel, dim3(256), dim3(512), L_END + 64, stream, A);
}
```

```cpp
#include <hip/hip_runtime.h>
#include <stdint.h>

typedef unsigned short u16;
typedef unsigned u32;
typedef unsigned long long u64;
typedef __attribute__((ext_vector_type(4))) short s16x4;
typedef __attribute__((ext_vector_type(8))) short s16x8;
typedef __attribute__((ext_vector_type(2))) float f32x2;
typedef __attribute__((ext_vector_type(4))) float f32x4;
typedef __attribute__((ext_vector_type(4))) unsigned u32x4;
typedef __attribute__((ext_vector_type(2))) unsigned u32x2;
typedef __attribute__((ext_vector_type(16))) unsigned u32x16;
typedef __attribute__((ext_vector_type(2))) __bf16 bf16x2;
typedef __amdgpu_buffer_rsrc_t rsrc_t;
#define LAS __attribute__((address_space(3)))
#define GAS __attribute__((address_space(1)))
#define DEV __device__ __forceinline__

#define STAMPS 0
constexpr int OWIN = 6;
constexpr int GWIN = 6, GPRE = 4;
constexpr int T_GATHER = 350, T_SEL = 580, T_H = 1300, T_O = 1520;
constexpr int T_Y = 100;
constexpr int WARM_AT = 10;
constexpr u64 HELPK = 0x765432ull, ROLLK = 0xfedcba98ull;
constexpr int HELPN = 6, RS0 = 1, RSN = 8;
constexpr int CODE_LINES = 339;

constexpr int S_ = 2048, D_ = 1024, F_ = 3584;
constexpr u32 WS_EPOCH = 0;
constexpr u32 WS_SEL = 4096;
constexpr u32 WS_RG = 16384;
constexpr u32 WS_SG = 32768;
constexpr u32 WS_OF = 65536;
constexpr u32 WS_H = WS_OF + 131072;
constexpr u32 WS_YS = WS_H + 131072;
constexpr u32 WS_O = WS_YS + 524288;
constexpr u32 WS_END = WS_O + S_ * D_ * 2;
constexpr u32 WS_STAMP = 16u << 20;
constexpr u32 WOFF = 22528, SLOT = 47104;
constexpr u32 L_HX = 131072;
constexpr u32 L_XT = 3 * SLOT;
constexpr u32 L_STQ = L_XT + 16384;
constexpr u32 L_STO = L_STQ + 1408;
constexpr u32 L_UPD = L_STO + 1024;
constexpr u32 L_ONE = L_UPD + 2048;
constexpr u32 L_CTL = L_ONE + 128;
constexpr u32 L_TAG = L_CTL, L_ARR = L_CTL + 4, L_UPDF = L_CTL + 8  , L_HS = L_CTL + 16  , L_SEL = L_CTL + 32  ;
constexpr u32 L_PW = L_CTL + 64, L_HR = L_CTL + 96;
constexpr u32 L_GO = L_CTL + 128;
constexpr u32 L_END = L_CTL + 192;
constexpr int SPIN_CAP = 1 << 22;
static_assert(L_END <= 163840 && L_ONE % 128 == 0, "LDS");

struct Params {
  const u16 *x, *wqkv, *wo, *wr, *wg, *wu, *wd;
  u16* out;
  char* ws;
};
struct Args {
  Params p;
  u64 kt[64];
};

DEV float bf_lo(u32 v) { return __uint_as_float(v << 16); }
DEV float bf_hi(u32 v) { return __uint_as_float(v & 0xffff0000u); }
DEV u32 pack2(float a, float b) { bf16x2 p = {(__bf16)a, (__bf16)b}; return __builtin_bit_cast(u32, p); }
DEV float rbf(float a) { return bf_lo(pack2(a, a)); }
DEV float dot2(u32 a, u32 b, float c) { return __builtin_amdgcn_fdot2_f32_bf16(__builtin_bit_cast(bf16x2, a), __builtin_bit_cast(bf16x2, b), c, false); }
DEV s16x8 as_frag(u32x4 v) { return __builtin_bit_cast(s16x8, v); }
DEV f32x4 mfma(s16x8 a, s16x8 b, f32x4 c) { return __builtin_amdgcn_mfma_f32_16x16x32_bf16(a, b, c, 0, 0, 0); }
DEV int rfl(int v) { return __builtin_amdgcn_readfirstlane(v); }
template <int CTRL, int RM, bool BC> DEV float dppf(float v) { return __int_as_float(__builtin_amdgcn_update_dpp(0, __float_as_int(v), CTRL, RM, 0xf, BC)); }
template <int CTRL, int RM, bool BC> DEV u32 dppu(u32 v) { return (u32)__builtin_amdgcn_update_dpp(0, (int)v, CTRL, RM, 0xf, BC); }
DEV float half_sum(float v) {
  v += dppf<0x111, 0xf, true>(v); v += dppf<0x112, 0xf, true>(v); v += dppf<0x114, 0xf, true>(v); v += dppf<0x118, 0xf, true>(v);
  v += dppf<0x142, 0xa, false>(v);
  return v;
}
DEV u32 wave_max_u32(u32 v) {
  v = max(v, dppu<0x111, 0xf, true>(v)); v = max(v, dppu<0x112, 0xf, true>(v)); v = max(v, dppu<0x114, 0xf, true>(v)); v = max(v, dppu<0x118, 0xf, true>(v));
  v = max(v, dppu<0x142, 0xa, false>(v)); v = max(v, dppu<0x143, 0xc, false>(v));
  return (u32)__builtin_amdgcn_readlane((int)v, 63);
}
DEV float vmax(float a, float b) { return __builtin_elementwise_maximum(a, b); }
DEV float vmax3(float a, float b, float c) { return vmax(vmax(a, b), c); }
DEV float max16(float v) { u32 a = __float_as_uint(v); auto r = __builtin_amdgcn_permlane16_swap(a, a, false, false); u32 x = r[0], y = r[1]; return vmax(__uint_as_float(x), __uint_as_float(y)); }
DEV float max32(float v) { u32 a = __float_as_uint(v); auto r = __builtin_amdgcn_permlane32_swap(a, a, false, false); u32 x = r[0], y = r[1]; return vmax(__uint_as_float(x), __uint_as_float(y)); }
DEV float sum32(float v) { u32 a = __float_as_uint(v); auto r = __builtin_amdgcn_permlane32_swap(a, a, false, false); u32 x = r[0], y = r[1]; return __uint_as_float(x) + __uint_as_float(y); }
DEV u32 ord16(u32 b) { return (b & 0x8000u) ? (~b & 0xffffu) : (b | 0x8000u); }
DEV float sigm(float v) { return __builtin_amdgcn_rcpf(1.f + __builtin_amdgcn_exp2f(-v * 1.4426950408889634f)); }

DEV rsrc_t mk_rsrc(const void* p, u32 bytes) { return __builtin_amdgcn_make_buffer_rsrc((void*)p, 0, (int)bytes, 0x00020000); }
DEV u32x4 ld_nt_128(rsrc_t rs, u32 voff, u32 soff) { return __builtin_amdgcn_raw_buffer_load_b128(rs, voff, soff, 2); }
DEV u32x4 ld_sc1_128(rsrc_t rs, u32 voff, u32 soff) { return __builtin_amdgcn_raw_buffer_load_b128(rs, voff, soff, 16); }
DEV u32x2 ld_sc1_64(rsrc_t rs, u32 voff, u32 soff) { return __builtin_amdgcn_raw_buffer_load_b64(rs, voff, soff, 16); }
DEV u32 ld_sc1_32(rsrc_t rs, u32 voff, u32 soff) { return __builtin_amdgcn_raw_buffer_load_b32(rs, voff, soff, 16); }
DEV void st_sc1_128(u32x4 v, rsrc_t rs, u32 voff) { __builtin_amdgcn_raw_buffer_store_b128(v, rs, voff, 0, 16); }
DEV void st_sc1_64(u32x2 v, rsrc_t rs, u32 voff) { __builtin_amdgcn_raw_buffer_store_b64(v, rs, voff, 0, 16); }
DEV void st_sc1_32(u32 v, rsrc_t rs, u32 voff) { __builtin_amdgcn_raw_buffer_store_b32(v, rs, voff, 0, 16); }
#define VMCNT(n) asm volatile("s_waitcnt vmcnt(" #n ")" ::: "memory")
#define LGKM0() asm volatile("s_waitcnt lgkmcnt(0)" ::: "memory")
#define CBAR() asm volatile("" ::: "memory")
#define BAR() do { CBAR(); __builtin_amdgcn_s_barrier(); CBAR(); } while (0)
#define SB() __builtin_amdgcn_sched_barrier(0)
#define LP(a) ((LAS char*)(uintptr_t)(a))
#define PIN(x) asm volatile("" : "+v"(x))

#define TICK() 0ull
#define RT() 0ull
#define STAMP(k) do {} while (0)
#define CSTAMP(k) do {} while (0)
#define LSTAMP(k) do {} while (0)

DEV u32 lds_rd(LAS char* L, u32 off) { return *(volatile LAS u32*)(L + off); }
DEV void lds_wr(LAS char* L, u32 off, u32 v) { *(volatile LAS u32*)(L + off) = v; }
DEV void lds_wait_set(LAS char* L, u32 off) {
  _Pragma("nounroll") for (int sp = 0; sp < SPIN_CAP; ++sp) {
    if (__all(lds_rd(L, off) != 0u)) break;
    __builtin_amdgcn_s_sleep(1);
  }
  CBAR();
}
DEV void wait_until(u64 t0, u32 ticks) {
  while (__builtin_amdgcn_s_memrealtime() - t0 < ticks) __builtin_amdgcn_s_sleep(4);
}
#define DMA_HEAD "s_mov_b32 m0, %0\n\ts_nop 0\n\t"
DEV void dma16(u32 lds, u32 voff, rsrc_t rs, u32 soff) {
  asm volatile(DMA_HEAD "buffer_load_dwordx4 %1, %2, %3 offen lds" ::"s"(lds), "v"(voff), "s"(rs), "s"(soff) : "memory");
}
DEV void dma16x4(u32 lds, u32 v0, u32 v1, u32 v2, u32 v3, rsrc_t rs, u32 soff) {
  asm volatile(DMA_HEAD "buffer_load_dwordx4 %1, %5, %6 offen lds\n\tbuffer_load_dwordx4 %2, %5, %6 offen offset:1024 lds\n\t"
               "buffer_load_dwordx4 %3, %5, %6 offen offset:2048 lds\n\tbuffer_load_dwordx4 %4, %5, %6 offen offset:3072 lds"
               ::"s"(lds), "v"(v0), "v"(v1), "v"(v2), "v"(v3), "s"(rs), "s"(soff) : "memory");
}
DEV void dma16x2(u32 lds, u32 v0, u32 v1, rsrc_t rs, u32 soff) {
  asm volatile(DMA_HEAD "buffer_load_dwordx4 %1, %3, %4 offen lds\n\tbuffer_load_dwordx4 %2, %3, %4 offen offset:1024 lds"
               ::"s"(lds), "v"(v0), "v"(v1), "s"(rs), "s"(soff) : "memory");
}

struct Frags { s16x8 x[6]; s16x8 w[6]; };

DEV s16x8 tr_frag(LAS char* p, LAS char* q, u32 o0, u32 o1) {
  s16x4 a = __builtin_amdgcn_ds_read_tr16_b64_v4i16((LAS s16x4*)(p + o0));
  s16x4 b = __builtin_amdgcn_ds_read_tr16_b64_v4i16((LAS s16x4*)(q + o1));
  return __builtin_shufflevector(a, b, 0, 1, 2, 3, 4, 5, 6, 7);
}
template <int KK> DEV void load_frags(Frags& f, LAS char* aH, LAS char* aT, LAS char* w0, LAS char* w1) {
#pragma unroll
  for (int t = 0; t < 5; ++t) f.x[1 + t] = *(LAS s16x8*)(aT + t * 2048);
  f.x[0] = *(LAS s16x8*)aH;
#pragma unroll
  for (int sec = 0; sec < 3; ++sec) {
    f.w[2 * sec] = tr_frag(w0, w0, sec * 8192 + KK * 4096, sec * 8192 + KK * 4096 + 512);
    f.w[2 * sec + 1] = tr_frag(w1, w1, sec * 8192 + KK * 4096, sec * 8192 + KK * 4096 + 512);
  }
}
template <int A, int B> DEV void mma_range(f32x4 (&acc)[6][6], const Frags& f, s16x8 e0) {
#pragma unroll
  for (int n = A; n < B; ++n) {
    const int p = n / 11, r = n % 11, w = 2 * p + (r >= 6), t = (r >= 6) ? 11 - r : 5 - r;
    acc[w][t] = (w < 4) ? mfma(f.w[w], (w == 0 && t == 0) ? e0 : f.x[t], acc[w][t]) : mfma(f.x[t], f.w[w], acc[w][t]);
  }
}
DEV void interleave() {
#pragma unroll
  for (int k = 0; k < 6; ++k) { __builtin_amdgcn_sched_group_barrier(0x008, 1, 0); __builtin_amdgcn_sched_group_barrier(0x100, 1, 0); }
#pragma unroll
  for (int k = 0; k < 6; ++k) {
    __builtin_amdgcn_sched_group_barrier(0x008, 1, 0); __builtin_amdgcn_sched_group_barrier(0x100, 1, 0);
    __builtin_amdgcn_sched_group_barrier(0x008, 1, 0); __builtin_amdgcn_sched_group_barrier(0x100, 1, 0);
    __builtin_amdgcn_sched_group_barrier(0x008, 1, 0);
  }
}

template <int E> DEV s16x8 col_frag(const u32x4* L) {
  constexpr u32 sel = (E & 1) ? 0x07060302u : 0x05040100u;
  u32x4 r;
  r.x = __builtin_amdgcn_perm(L[1][E >> 1], L[0][E >> 1], sel);
  r.y = __builtin_amdgcn_perm(L[3][E >> 1], L[2][E >> 1], sel);
  r.z = __builtin_amdgcn_perm(L[5][E >> 1], L[4][E >> 1], sel);
  r.w = __builtin_amdgcn_perm(L[7][E >> 1], L[6][E >> 1], sel);
  return as_frag(r);
}
DEV void mma_cols(f32x4* acc, const u32x4* L, s16x8 b) {
  acc[0] = mfma(col_frag<0>(L), b, acc[0]); acc[1] = mfma(col_frag<1>(L), b, acc[1]);
  acc[2] = mfma(col_frag<2>(L), b, acc[2]); acc[3] = mfma(col_frag<3>(L), b, acc[3]);
  acc[4] = mfma(col_frag<4>(L), b, acc[4]); acc[5] = mfma(col_frag<5>(L), b, acc[5]);
  acc[6] = mfma(col_frag<6>(L), b, acc[6]); acc[7] = mfma(col_frag<7>(L), b, acc[7]);
}

DEV void transpose_grp(u32x4* L) {
  const s16x8 f0 = col_frag<0>(L), f1 = col_frag<1>(L), f2 = col_frag<2>(L), f3 = col_frag<3>(L), f4 = col_frag<4>(L), f5 = col_frag<5>(L), f6 = col_frag<6>(L), f7 = col_frag<7>(L);
  L[0] = __builtin_bit_cast(u32x4, f0); L[1] = __builtin_bit_cast(u32x4, f1); L[2] = __builtin_bit_cast(u32x4, f2); L[3] = __builtin_bit_cast(u32x4, f3);
  L[4] = __builtin_bit_cast(u32x4, f4); L[5] = __builtin_bit_cast(u32x4, f5); L[6] = __builtin_bit_cast(u32x4, f6); L[7] = __builtin_bit_cast(u32x4, f7);
#pragma unroll
  for (int e = 0; e < 8; ++e) PIN(L[e]);
  SB();
}
DEV void mma_frags8(f32x4* acc, const u32x4* L, s16x8 b) {
#pragma unroll
  for (int e = 0; e < 8; ++e) acc[e] = mfma(as_frag(L[e]), b, acc[e]);
}
DEV void warm_line(u32 lds, const char* a) {
  asm volatile(DMA_HEAD "global_load_lds_dword %1, off" ::"s"(lds), "v"(a) : "memory");
}

__global__ void __launch_bounds__(512, 2) fused_kernel(Args Parg) {
  extern __shared__ __attribute__((aligned(128))) char smem[];
  u32x16 ka;
  u64 kb;
  asm volatile("s_load_dwordx16 %0, %2, 0x0\n\ts_load_dwordx2 %1, %2, 0x40" : "=&s"(ka), "=&s"(kb) : "s"(__builtin_amdgcn_kernarg_segment_ptr()));
  Params P;
  const char* pc0;
  asm volatile("s_getpc_b64 %0" : "=s"(pc0));
  LAS char* L = (LAS char*)smem;
  const u32 lbase = (u32)(uintptr_t)L;
  const int lane = threadIdx.x & 63;
  const int wave = rfl(threadIdx.x >> 6);
  const int bid = blockIdx.x, xg = bid & 7, loc = bid >> 3;
  const int np = 7 - (xg & 1);
  const bool gemm = loc < 4 * np;
  const int pl = loc >> 2, hl = loc & 3;
  const int pan = (xg & 1) * 7 + pl, hp = (xg >> 1) * 4 + hl, r0 = pan * 160;
  const int fi = loc - 4 * np;
  const bool dwn = (u32)(fi - 2 - 3 * (xg & 1)) <= 2u * (xg & 1);
  const int fu = (xg & 1) * 12 + (xg >> 1) * (3 + 2 * (xg & 1)) + min(fi, 2 + 2 * (xg & 1));
  const int du = (xg & 1) ? 4 + (xg >> 1) * 3 + fi - 5 : (xg >> 1);
  const int pan2 = (xg & 1) * 8 + (xg >> 1) * 2 + (loc >> 4);
  const int cg = (int)((((loc & 16) ? 0xfedc0a987653421bull : 0xfedc8a9b76534210ull) >> (4 * (loc & 15))) & 15), hrot = 2 * cg;
  const int g = lane >> 4, i = lane & 15;
  u64 kord;
  asm volatile("s_load_dwordx2 %0, %1, %2" : "=&s"(kord) : "s"(__builtin_amdgcn_kernarg_segment_ptr()), "s"(72 + (((xg & 1) * 32 + loc) & 63) * 8));
  rsrc_t rsWS;
  const u64 tstart = __builtin_amdgcn_s_memrealtime();
  u32 tag = 0;
  f32x4 oc[2][4];
  auto oc_init = [&]() {
    const int tl = (wave >> 1) + 64 * (wave & 1) + 2 * i;
    f32x2 st[2];
    u32x2 v[2][4];
#pragma unroll
    for (int tt = 0; tt < 2; ++tt) {
      st[tt] = *(LAS f32x2*)(L + L_STO + tl * 8 + tt * 256);
#pragma unroll
      for (int c = 0; c < 4; ++c) v[tt][c] = *(LAS u32x2*)(L + L_XT + tl * 128 + (((2 * c + (g >> 1)) ^ (i & 7)) << 4) + (g & 1) * 8 + tt * 4096);
    }
    SB();
#pragma unroll
    for (int tt = 0; tt < 2; ++tt) {
      const float k1 = 1.f + st[tt].y, k0 = -st[tt].x * st[tt].y;
#pragma unroll
      for (int c = 0; c < 4; ++c) oc[tt][c] = f32x4{bf_lo(v[tt][c].x), bf_hi(v[tt][c].x), bf_lo(v[tt][c].y), bf_hi(v[tt][c].y)} * k1 + k0;
    }
#pragma unroll
    for (int q = 0; q < 8; ++q) PIN(oc[q >> 2][q & 3]);
  };

  u32 tq[3], vO, vx;
  auto helper_offsets = [&]() {
    u32 lane = threadIdx.x & 63;
    PIN(lane);
    const int lr = lane >> 3, lc = lane & 7;
#pragma unroll
    for (int j = 0; j < 3; ++j) {
      const int rho = min(64 * j + (int)lane, 175), pr = rho >= 96;
      const int tk = r0 + ((rho < 16) ? rho - 16 : pr + 2 * (rho - 16 - 80 * pr));
      const u32 a = (u32)(pan2 * 128 + (rho & 127)) * 8, b = (((u32)tk >= (u32)S_) | (rho == 0)) ? 1u : (u32)tk * 8;
      tq[j] = (wave == 7) ? a : b;
      PIN(tq[j]);
    }
    vO = lr * 2048 + ((u32)(lc ^ (((lr >> 1) & 3) << 1)) << 4) + cg * 128;
    vx = lr * 2048 + ((u32)(lc ^ (lr >> 1)) << 4) + cg * 128;
    PIN(vO); PIN(vx);
  };
  auto wo_grp = [&](int q) { dma16x4(lbase + q * 4096, vO, vO + 15 * 1024, vO + 30 * 1024, vO + 45 * 1024, mk_rsrc(P.wo, D_ * D_ * 2), ((q + hrot) & 31) * 65536); };
  auto helper_fill = [&]() {
    const int lid = wave - 4;
    tag = (u32)rfl((int)lds_rd(L, L_TAG));
    if (gemm) { VMCNT(11); BAR(); }
    if (lid >= 2) {
      u32x2 sv[3];
#pragma unroll
      for (int q = 2 * lid - 4; q < 2 * lid - 2; ++q) dma16x4(lbase + L_XT + q * 4096, vx, (vx ^ 64) + 15 * 1024, vx + 30 * 1024, (vx ^ 64) + 45 * 1024, mk_rsrc(P.x, S_ * D_ * 2), (pan2 * 128 + 32 * q) * 2048);
#pragma unroll
      for (int j = 0; j < 3; ++j) sv[j] = ld_sc1_64(rsWS, tq[j] & ~7u, WS_SG);
      SB();
      if (gemm) { VMCNT(11); BAR(); }
      _Pragma("nounroll") for (int sp = 0; sp < SPIN_CAP; ++sp) {
        if (__all(sv[0].y == tag && sv[1].y == tag && sv[2].y == tag)) break;
        __builtin_amdgcn_s_sleep(2);
#pragma unroll
        for (int j = 0; j < 3; ++j) sv[j] = ld_sc1_64(rsWS, tq[j] & ~7u, WS_SG);
      }
      f32x2 st[3];
      u32 ta = lbase + ((lid == 3) ? L_STO + lane * 8 : L_STQ + lane * 4), tb2 = ta + ((lid == 3) ? 4 : 704);
      u32 tc = (lid == 2 && lane < 48) ? ta + 512 : lbase + L_UPD + lane * 4, td = (lid == 2 && lane < 48) ? tb2 + 512 : tc;
      const u32 ts = (lid == 3) ? 512 : 256;
#pragma unroll
      for (int j = 0; j < 3; ++j) {
        st[j] = f32x2{__uint_as_float((sv[j].x << 16) ^ ((lid == 3) ? 0u : 0x80000000u)), __builtin_amdgcn_rcpf(bf_hi(sv[j].x))};
        if (tq[j] & 1u) st[j] = f32x2{0.f, 0.f};
        PIN(st[j]);
      }
      u32 te = ta + ts, tf = tb2 + ts;
      PIN(ta); PIN(tb2); PIN(tc); PIN(td); PIN(te); PIN(tf);
      VMCNT(0);
      if (gemm) BAR();
      *(LAS float*)LP(ta) = st[0].x; *(LAS float*)LP(tb2) = st[0].y;
      *(LAS float*)LP(te) = st[1].x; *(LAS float*)LP(tf) = st[1].y;
      *(LAS float*)LP(tc) = st[2].x; *(LAS float*)LP(td) = st[2].y;
      LGKM0();
    } else {
      if (gemm) { VMCNT(0); BAR(); }
      wo_grp(12 + lid); wo_grp(14 + lid);
#pragma unroll
      for (int q = 0; q < 4; ++q) wo_grp(16 + 2 * q + lid);
      if (gemm) BAR();
    }
    if (gemm) BAR();
    int l2 = lid;
    asm volatile("" : "+s"(l2));
    wo_grp(l2); wo_grp(4 + l2); wo_grp(8 + l2);
    if (!dwn) { wo_grp(24 + l2); wo_grp(28 + l2); }
    if (lid == 2) {
      u32x2 sk;
      _Pragma("nounroll") for (int sp = 0; sp < SPIN_CAP; ++sp) {
        sk = ld_sc1_64(rsWS, (lane & 7) * 8, WS_SEL);
        if (__all(sk.y == tag)) break;
        __builtin_amdgcn_s_sleep(8);
      }
      if (lane < 8) lds_wr(L, L_SEL + 4 * lane, sk.x);
      LGKM0();
    }
  };
  {
    u32 ln = threadIdx.x & 63;
    PIN(ln);
    if (ln < 2) warm_line(lbase + L_UPD, pc0 + min((loc * 8 + wave) * 2 + (int)ln, CODE_LINES - 1) * 128);
  }
  const int lid = wave - 4;
  const bool isx = lid < 2;
  u32 v0 = 0, v1 = 0, v2 = 0, v3 = 0, vh = 0, g0 = 0, g1 = 0, g2 = 0, kstep = 0, d0 = 0;
  {
    int wave_a = wave;
    asm volatile("" : "+s"(wave_a));
    if (gemm && wave_a >= 4) {
      u32 ln = threadIdx.x & 63;
      PIN(ln);
      const int lr = ln >> 3, lc = ln & 7;
      const u32 sw = (u32)(lc ^ (isx ? lr >> 1 : lr & 2)) << 4;
      const u32 vb = lr * (isx ? 4096 : 6144) + sw + (isx ? (r0 + lid) * 2048 : hp * 128);
      const u32 pp = (isx ? 32768 : 49152) - 1024;
      v0 = vb; v1 = (vb ^ 64) + pp; v2 = vb + 2 * pp; v3 = (vb ^ 64) + 3 * pp;
      g0 = (lid == 3) ? 198656 : 0; g1 = isx ? 131072 : (lid == 3) ? 4096 : 196608; g2 = isx ? 262144 : (lid == 3) ? 200704 : 2048;
      vh = max(r0 - 16 + 8 * lid + lr, 0) * 2048 + (sw ^ (lid << 6));
      kstep = isx ? 128u : 64u * 6144u; d0 = lbase + (isx ? 2 + 10 * lid : 22 + 12 * (lid - 2)) * 1024;
      PIN(v0); PIN(v1); PIN(v2); PIN(v3); PIN(vh);
      asm volatile("" : "+s"(g0), "+s"(g1), "+s"(g2), "+s"(kstep), "+s"(d0));
    }
  }
  u32 ev;
  asm volatile("s_waitcnt lgkmcnt(0)" : "+s"(ka), "+s"(kb), "+s"(kord));
  {
    auto ptr = [&](int k) { return (char*)(GAS char*)(((u64)ka[2 * k + 1] << 32) | ka[2 * k]); };
    P.x = (const u16*)ptr(0); P.wqkv = (const u16*)ptr(1); P.wo = (const u16*)ptr(2); P.wr = (const u16*)ptr(3);
    P.wg = (const u16*)ptr(4); P.wu = (const u16*)ptr(5); P.wd = (const u16*)ptr(6); P.out = (u16*)ptr(7);
    P.ws = (char*)(GAS char*)kb;
  }
  rsWS = mk_rsrc(P.ws, WS_END);
  STAMP(0);
  if (wave < 4) {
    asm volatile("s_load_dword %0, %1, 0x0" : "=s"(ev) : "s"(P.ws + WS_EPOCH) : "memory");
    u32 ln = threadIdx.x & 63;
    PIN(ln);
    const u32 d = lbase + 2 * SLOT + wave * 6144, v = ln * 16;
    dma16x4(d, v, v, v, v, mk_rsrc(P.x, S_ * D_ * 2), (pan2 * 128 + cg * 8 + wave * 2) * 2048);
    dma16x2(d + 4096, v, v, mk_rsrc(P.wr, D_ * 2), 0);
  }
  SB();
  int wave_b = wave;
  asm volatile("" : "+s"(wave_b));
  if (gemm && wave_b >= 4) {
    const rsrc_t rs = mk_rsrc(isx ? P.x : P.wqkv, isx ? S_ * D_ * 2 : D_ * 3 * D_ * 2);
    auto issue_k = [&](u32 ko, u32 so) {
      dma16x4(d0 + so, v0, v1, v2, v3, rs, ko + g0);
      dma16x4(d0 + so + 4096, v0, v1, v2, v3, rs, ko + g1);
      if (isx) { dma16x2(d0 + so + 8192, v0, v1, rs, ko + g2); dma16(lbase + so + lid * 1024, vh, rs, ko); }
      else dma16x4(d0 + so + 8192, v0, v1, v2, v3, rs, ko + g2);
    };
    auto issue = [&](int s, u32 so) { issue_k(((u32)(kord >> (4 * s)) & 15u) * kstep, so); };
    LSTAMP(20);
    issue(0, 0);
    LSTAMP(21);
    helper_offsets();
    issue(1, SLOT);
    LSTAMP(22);
    if (isx) VMCNT(11); else VMCNT(12);
    LSTAMP(23);
    BAR();
    const bool helps = !isx && !(xg & 2) && hl != 3;
    if (helps) {
      constexpr int HL = (HELPN * 192 + 35) / 36;
      const int n = min(((pl * 3 + hl) * 2 + lid - 2) * HL + min(lane, HL - 1), HELPN * 192 - 1), row = n / 3;
      warm_line(lbase + L_UPD, (const char*)P.wqkv + (size_t)((u32)((HELPK >> (4 * (row >> 6))) & 15) * 64 + (row & 63)) * 6144 + (n % 3) * 2048 + (hp | 3) * 128);
    }
    issue(2, 2 * SLOT);
    u32 so = 0, sdum = 0;
    const int sn = min((pl * 4 + hl) * 4 + lid, 95) * 2;
    const u32 so0 = (sn / 3) * 6144 + (sn % 3) * 2048, so1 = ((sn + 1) / 3) * 6144 + ((sn + 1) % 3) * 2048;
    const char* sbase = (const char*)P.wqkv + (hp | 3) * 128;
    _Pragma("nounroll") for (int s = 0; s < 13; ++s) {
      if (helps && s == 0) VMCNT(13); else if (!isx || s == WARM_AT + 1 || s == WARM_AT + 2) VMCNT(12); else VMCNT(11);
      if (s == 0) LSTAMP(24);
      if (s == 1) LSTAMP(25);
      if (s == 2) LSTAMP(26);
      BAR();
      issue(s + 3, so);
      if (!(xg & 2) && (u32)(s - RS0) < RSN) {
        const u32 ko = (u32)((ROLLK >> (4 * (s - RS0))) & 15) * (64u * 6144u), a0 = ko + so0, a1 = ko + so1;
        asm volatile("s_load_dword %0, %1, %2\n\ts_load_dword %0, %1, %2 offset:64\n\ts_load_dword %0, %1, %3\n\ts_load_dword %0, %1, %3 offset:64"
                     : "+s"(sdum) : "s"(sbase), "s"(a0), "s"(a1) : "memory");
      }
      if (s == WARM_AT && isx) warm_line(lbase + L_UPD, (const char*)P.wo + (size_t)min((pan * 16 + hp) * 80 + lid * 40 + min(lane, 39), 16383) * 128);
      so = (so == 2 * SLOT) ? 0 : so + SLOT;
    }
    asm volatile("s_waitcnt lgkmcnt(0)" : "+s"(sdum));
    STAMP(2);
    helper_fill();
  }
  if (wave < 4) {
    __builtin_amdgcn_s_setprio(1);
    if (wave == 1) lds_wr(L, L_ONE + lane * 4, lane < 32 ? 0x3f803f80u : 0u);
    if (wave == 2) lds_wr(L, L_GO, 0u);
    f32x4 acc[6][6];
#pragma unroll
    for (int w = 0; w < 6; ++w)
#pragma unroll
      for (int t = (w & 1); t < 6; ++t) { acc[w][t] = f32x4{0.f, 0.f, 0.f, 0.f}; PIN(acc[w][t]); }
    f32x4 bprev, bhalo;
    u32 mc[2];
    u64 cur[4];
#pragma unroll
    for (int r = 0; r < 4; ++r) {
      const int kap = 4 * g + r, d = 16 + (wave & 1) + 2 * i - kap;
      bprev[r] = (kap - i >= 9) ? 0.f : -1e30f;
      bhalo[r] = (((d & 1) == 0) & (d <= 14) & (pan > 0)) ? 0.f : -1e30f;
      cur[r] = __ballot((u32)(i - kap) <= 7u);
      asm volatile("" : "+s"(cur[r]));
    }
    mc[0] = (((u32)(i - 4 * g) <= 7u) ? 0xffffu : 0u) | (((u32)(i - 4 * g - 1) <= 7u) ? 0xffff0000u : 0u);
    mc[1] = (((u32)(i - 4 * g - 2) <= 7u) ? 0xffffu : 0u) | (((u32)(i - 4 * g - 3) <= 7u) ? 0xffff0000u : 0u);
    u32 hxl = lbase + L_HX + (wave & 2) * 2304 + lane * 16;
    u32 obase = WS_O + (((wave & 1) * 32 + hp * 2 + (wave >> 1)) * 64 + pan * 5) * 1024 + lane * 16;
    PIN(bprev); PIN(bhalo); PIN(mc[0]); PIN(mc[1]); PIN(hxl); PIN(obase);
    SB();
    {
      const int tok = pan2 * 128 + cg * 8 + wave * 2 + (lane >> 5);
      u32x4 xv[4], wv[4];
      VMCNT(0);
      {
        LAS char* sp = L + 2 * SLOT + wave * 6144 + (lane & 31) * 16;
#pragma unroll
        for (int j = 0; j < 4; ++j) { xv[j] = *(LAS u32x4*)(sp + (lane >> 5) * 2048 + j * 512); wv[j] = *(LAS u32x4*)(sp + 4096 + j * 512); }
      }
      asm volatile("s_waitcnt lgkmcnt(0)" : "+s"(ev) :: "memory");
      const u32 e = ev;
      tag = e * 0x9E3779B1u + 0x7F4A7C15u; tag ^= tag >> 15; tag = (u32)rfl((int)(tag ^ (u32)(tag == e)));
      if (wave == 1) lds_wr(L, L_TAG, tag);
      float sx = 0.f, sxx = 0.f, sxw = 0.f, sw = 0.f;
#pragma unroll
      for (int j = 0; j < 4; ++j)
#pragma unroll
        for (int e2 = 0; e2 < 4; ++e2) {
          const u32 a = xv[j][e2], b = wv[j][e2];
          sx = dot2(a, 0x3f803f80u, sx); sxx = dot2(a, a, sxx); sxw = dot2(a, b, sxw); sw = dot2(b, 0x3f803f80u, sw);
        }
      const float mean = half_sum(sx) * (1.f / D_);
      const float var = fmaxf(half_sum(sxx) * (1.f / D_) - mean * mean, 0.f);
      const float mb = rbf(mean), sb = rbf(__builtin_amdgcn_sqrtf(rbf(rbf(var) + 1e-5f)));
      const u32 ro = ord16(pack2((half_sum(sxw) - mb * half_sum(sw)) * __builtin_amdgcn_rcpf(sb), 0.f) & 0xffffu);
      const u32 rlo = (u32)__builtin_amdgcn_readlane((int)ro, 31);
      if ((lane & 31) == 31) st_sc1_64(u32x2{pack2(mb, sb), tag}, rsWS, WS_SG + tok * 8);
      if (lane == 63) st_sc1_64(u32x2{rlo | (ro << 16), tag}, rsWS, WS_RG + (tok >> 1) * 8);
      if (!gemm) { LGKM0(); BAR(); }
    }
    STAMP(1);
    if (gemm) {
      const int h = wave >> 1, par = wave & 1;
        const int q4 = i >> 2, p4 = i & 3;
        u32 a0 = lbase + i * 128 + ((g ^ (i >> 1)) << 4);
        const u32 wsw = ((q4 >> 1) & 1) | ((g & 1) << 1);
        u32 w0 = lbase + WOFF + (8 * g + q4) * 128 + (((2 * h + par) ^ wsw) << 5) + p4 * 8;
        const u32 tb = (1 + 5 * par) * 2048;
        const u32 one = lbase + L_ONE;
        u32 pH = (i == 0) ? one : a0, pT = a0 + tb, pW0 = w0, pW1 = w0 ^ 32;
        PIN(a0); PIN(w0); PIN(pH); PIN(pT); PIN(pW0); PIN(pW1);
        Frags f0, f1;
        const u32 e1 = (i == 0) ? 0x3f803f80u : 0u;
        s16x8 e0 = as_frag(u32x4{e1, e1, e1, e1});
        PIN(e0);
        __builtin_amdgcn_s_waitcnt(0xc07f);
        BAR();
        STAMP(2); CSTAMP(14);
        load_frags<0>(f0, LP(pH), LP(pT), LP(pW0), LP(pW1));
        pH ^= 64; pT ^= 64;
        __builtin_amdgcn_s_waitcnt(0xc07f);
        u32 so = 0, trc = 0;
        auto half1 = [&]() {
          SB();
          load_frags<1>(f1, LP(pH), LP(pT), LP(pW0), LP(pW1));
          mma_range<0, 24>(acc, f0, e0);
          interleave();
          SB();
          so = (so == 2 * SLOT) ? 0 : so + SLOT;
          mma_range<24, 25>(acc, f0, e0);
          SB();
          pT = so + a0; pW0 = so + w0; PIN(pT); PIN(pW0);
          mma_range<25, 26>(acc, f0, e0);
          SB();
          pH = (i == 0) ? one : pT; pW1 = pW0 ^ 32; PIN(pH); PIN(pW1);
          mma_range<26, 27>(acc, f0, e0);
          SB();
          pT += tb; PIN(pT);
          mma_range<27, 28>(acc, f0, e0);
          SB();
        };
        auto half1b = [&](int s) {
          const u64 tnow = TICK(), rnow = RT();
          mma_range<28, 30>(acc, f0, e0);
          SB();
          __builtin_amdgcn_s_waitcnt(0xc07f);
          if (STAMPS) { trc = (lane == s) ? (u32)tnow : trc; trc = (lane == s + 16) ? (u32)rnow : trc; }
          mma_range<30, 31>(acc, f0, e0);
          SB();
          BAR();
          SB();
          mma_range<31, 33>(acc, f0, e0);
          SB();
        };
        half1();
        for (int s = 0; s < 15; ++s) {
          half1b(s);
          load_frags<0>(f0, LP(pH), LP(pT), LP(pW0), LP(pW1));
          mma_range<0, 24>(acc, f1, e0);
          interleave();
          SB();
          pH ^= 64; pT ^= 64; PIN(pH); PIN(pT);
          mma_range<24, 31>(acc, f1, e0);
          SB();
          __builtin_amdgcn_s_waitcnt(0xc07f);
          mma_range<31, 33>(acc, f1, e0);
          half1();
        }
        half1b(15);
        if (STAMPS && lane < 32) ((GAS u32*)(P.ws + WS_STAMP + (1 << 20)))[(bid * 4 + wave) * 32 + lane] = trc;
      mma_range<5, 6>(acc, f1, e0); mma_range<16, 17>(acc, f1, e0); mma_range<27, 28>(acc, f1, e0);
      SB();
      mma_range<11, 16>(acc, f1, e0);
      u32 tz = threadIdx.x;
      PIN(tz);
      const u32 i = tz & 15, g = (tz >> 4) & 3;
      u32 sqi = lbase + L_STQ + i * 4, sqg = lbase + L_STQ + g * 16;
      PIN(sqi); PIN(sqg);
      const u32 hx = hxl + par * 2304, hy = hxl + (par ^ 1) * 2304;
      u32 hxg = hx - i * 16, hyg = hy - i * 16, hxi = hx - g * 256, hyi = hy - g * 256, sqi2 = sqi + par * 320, sqg2 = sqg + par * 320;
      const u32 hqa = hx + 2048 - g * 240, hqb = hxl + (L_UPD - L_HX) - (wave & 2) * 2304;
      u32 hq = __builtin_amdgcn_inverse_ballot_w64(0x0001000100010001ull) ? hqa : hqb;
      PIN(hxg); PIN(hyg); PIN(hxi); PIN(hyi); PIN(sqi2); PIN(sqg2); PIN(hq);
#pragma unroll
      for (int k = 0; k < 5; ++k) { __builtin_amdgcn_sched_group_barrier(0x008, 1, 0); __builtin_amdgcn_sched_group_barrier(0x002, 2, 0); }
      SB();
      *(LAS f32x4*)LP(hx) = acc[2][0];
      *(LAS f32x4*)LP(hx + 1024) = acc[4][0];
      *(LAS f32x4*)LP(hq) = acc[0][0];
      mma_range<17, 22>(acc, f1, e0);
#pragma unroll
      for (int k = 0; k < 3; ++k) { __builtin_amdgcn_sched_group_barrier(0x008, 1, 0); __builtin_amdgcn_sched_group_barrier(0x200, 1, 0); }
      SB();
      mma_range<0, 3>(acc, f1, e0);
      SB();
      __builtin_amdgcn_s_waitcnt(0xc07f);
      BAR();
      SB();
      f32x4 cs[4];
      cs[2] = *(LAS f32x4*)LP(hxg); cs[3] = *(LAS f32x4*)LP(hyg);
      cs[0] = *(LAS f32x4*)LP(hxg + 2048 - g * 240); cs[1] = *(LAS f32x4*)LP(hyg + 2048 - g * 240);
      float m[6], rs[6];
#pragma unroll
      for (int t = 0; t < 6; ++t) { m[t] = *(LAS float*)LP((t ? sqi2 : sqi) + t * 64); rs[t] = *(LAS float*)LP((t ? sqi2 : sqi) + t * 64 + 704); }
      acc[3][0] = *(LAS f32x4*)LP(hy);
      const float cv[2] = {*(LAS float*)LP(hxi + 1024), *(LAS float*)LP(hyi + 1024)};
      acc[5][0] = *(LAS f32x4*)LP(hy + 1024);
      mma_range<3, 5>(acc, f1, e0); mma_range<6, 11>(acc, f1, e0);
#pragma unroll
      for (int k = 0; k < 7; ++k) { __builtin_amdgcn_sched_group_barrier(0x008, 1, 0); __builtin_amdgcn_sched_group_barrier(0x100, 2, 0); }
      SB();
      STAMP(3); CSTAMP(15);
      const float QS = 0.17677669529663687f * 1.4426950408889634f;
      const f32x4 z4 = {0.f, 0.f, 0.f, 0.f};
      f32x4 s0[5], s1[5], m4[6], r4[6];
      {
        s16x8 kf[6];
#pragma unroll
        for (int t = 5; t >= 0; --t) {
          const f32x4 k0 = (acc[2][t] + m[t] * cs[2]) * rs[t], k1 = (acc[3][t] + m[t] * cs[3]) * rs[t];
          kf[t] = as_frag(u32x4{pack2(k0[0], k0[1]), pack2(k0[2], k0[3]), pack2(k1[0], k1[1]), pack2(k1[2], k1[3])});
        }
        mma_range<22, 27>(acc, f1, e0); mma_range<28, 33>(acc, f1, e0);
#pragma unroll
        for (int k = 0; k < 10; ++k) { __builtin_amdgcn_sched_group_barrier(0x008, 1, 0); __builtin_amdgcn_sched_group_barrier(0x002, 3, 0); }
        SB();
#pragma unroll
        for (int t = 0; t < 6; ++t) { m4[t] = *(LAS f32x4*)LP((t ? sqg2 : sqg) + t * 64); r4[t] = *(LAS f32x4*)LP((t ? sqg2 : sqg) + t * 64 + 704); }
#pragma unroll
        for (int t = 0; t < 5; ++t) {
          const f32x4 q0 = acc[0][t + 1] + m[t + 1] * cs[0], q1 = acc[1][t + 1] + m[t + 1] * cs[1];
          const s16x8 qf = as_frag(u32x4{pack2(q0[0], q0[1]), pack2(q0[2], q0[3]), pack2(q1[0], q1[1]), pack2(q1[2], q1[3])});
          s0[t] = mfma(kf[t], qf, t == 0 ? bhalo : bprev); s1[t] = mfma(kf[t + 1], qf, z4);
        }
      }
      SB();
      u32x16 vp[2];
#pragma unroll
      for (int t = 0; t < 6; ++t) {
#pragma unroll
        for (int c = 0; c < 2; ++c) {
          const f32x4 v = (acc[4 + c][t] + m4[t] * cv[c]) * r4[t];
          vp[c][2 * t] = pack2(v[0], v[1]); vp[c][2 * t + 1] = pack2(v[2], v[3]);
        }
      }
      PIN(vp[0]); PIN(vp[1]);
      float l[5][4], mx[5], rq[5];
#pragma unroll
      for (int t = 0; t < 5; ++t) {
#pragma unroll
        for (int r = 0; r < 4; ++r) l[t][r] = __builtin_amdgcn_inverse_ballot_w64(cur[r]) ? s1[t][r] : s0[t][r];
        mx[t] = vmax3(vmax(l[t][0], l[t][1]), l[t][2], l[t][3]);
        rq[t] = rs[t + 1] * QS;
      }
#pragma unroll
      for (int t = 0; t < 4; t += 2) {
        const auto r = __builtin_amdgcn_permlane16_swap(__float_as_uint(mx[t]), __float_as_uint(mx[t + 1]), false, false);
        const u32 r0 = r[0], r1 = r[1];
        const float z = max32(vmax(__uint_as_float(r0), __uint_as_float(r1)));
        const auto b = __builtin_amdgcn_permlane16_swap(__float_as_uint(z), __float_as_uint(z), false, false);
        const u32 b0 = b[0], b1 = b[1];
        mx[t] = -__uint_as_float(b0) * rq[t]; mx[t + 1] = -__uint_as_float(b1) * rq[t + 1];
      }
      mx[4] = -max32(max16(mx[4])) * rq[4];
      const s16x8 ones = as_frag(u32x4{0x3f803f80u, 0x3f803f80u, 0x3f803f80u, 0x3f803f80u});
      f32x4 sm[5], o0[5], o1[5];
#pragma unroll
      for (int t = 0; t < 5; ++t) {
#pragma unroll
        for (int r = 0; r < 4; ++r) l[t][r] = __builtin_amdgcn_exp2f(__builtin_fmaf(l[t][r], rq[t], mx[t]));
        const u32 p01 = pack2(l[t][0], l[t][1]), p23 = pack2(l[t][2], l[t][3]);
        const s16x8 pp = as_frag(u32x4{p01 & ~mc[0], p23 & ~mc[1], p01 & mc[0], p23 & mc[1]});
        sm[t] = mfma(ones, pp, z4);
        o0[t] = mfma(as_frag(u32x4{vp[0][2 * t], vp[0][2 * t + 1], vp[0][2 * t + 2], vp[0][2 * t + 3]}), pp, z4);
        o1[t] = mfma(as_frag(u32x4{vp[1][2 * t], vp[1][2 * t + 1], vp[1][2 * t + 2], vp[1][2 * t + 3]}), pp, z4);
      }
#pragma unroll
      for (int t = 0; t < 5; ++t) {
        const float inv = __builtin_amdgcn_rcpf(sm[t][0]);
        o0[t] *= inv; o1[t] *= inv;
        st_sc1_128(u32x4{pack2(o0[t][0], o0[t][1]), pack2(o0[t][2], o0[t][3]), pack2(o1[t][0], o1[t][1]), pack2(o1[t][2], o1[t][3])}, rsWS,
                   (t == 4) ? obase + ((pan == 12) ? 0x40000000u : 4096u) : obase + t * 1024);
      }
      STAMP(8);
    }
  }
  if (!gemm) {
  if (dwn) {
    const rsrc_t rsD = mk_rsrc(P.wd, F_ * D_ * 2);
    const int k0 = (du >> 1) * 14 + (wave >> 2) * 7;
    const u32 vd = (g * D_ + (du & 1) * 512 + (wave & 3) * 128 + 8 * i) * 2;
    auto drow = [&](int q, int j) -> u32 { return (u32)(64 * ((k0 + q) >> 1) + 4 * ((k0 + q) & 1) + 8 * j) * (D_ * 2); };
    constexpr u32 D_W = 24 * 4096, D_H = D_W + 8 * 4352;
    u32x4 wb[56];
#pragma unroll
    for (int j = 0; j < 8; ++j) wb[j] = ld_nt_128(rsD, vd, drow(0, j));
    SB();
    if (wave >= 4) { BAR(); tag = (u32)rfl((int)lds_rd(L, L_TAG)); }
#pragma unroll
    for (int j = 8; j < 48; ++j) wb[j] = ld_nt_128(rsD, vd, drow(j >> 3, j & 7));
    SB();
    transpose_grp(wb);
#pragma unroll
    for (int e = 0; e < 4; ++e) *(LAS u32x4*)(L + D_W + wave * 4352 + e * 1024 + lane * 16) = wb[e];
    SB();
#pragma unroll
    for (int q = 1; q < 6; ++q) transpose_grp(wb + 8 * q);
#pragma unroll
    for (int j = 48; j < 56; ++j) wb[j] = ld_nt_128(rsD, vd, drow(6, j & 7));
    SB();
    __builtin_amdgcn_s_setprio(0);
    if (wave >= 4) { helper_offsets(); helper_fill(); }
    transpose_grp(wb + 48);
    VMCNT(0);
    STAMP(12);
    BAR();
    STAMP(11);
    {
      wait_until(tstart, T_H);
      const int qa = 2 * (wave & 3), qb = min(qa + 1, 6);
      u32x4 va, vb;
      _Pragma("nounroll") for (int sp = 0; sp < SPIN_CAP; ++sp) {
        va = ld_sc1_128(rsWS, lane * 16, WS_H + (k0 + qa) * 1024);
        vb = ld_sc1_128(rsWS, lane * 16, WS_H + (k0 + qb) * 1024);
        if (__all(va.y == tag && va.w == tag && vb.y == tag && vb.w == tag)) break;
        __builtin_amdgcn_s_sleep(1);
      }
      *(LAS u32x2*)(L + D_H + ((wave >> 2) * 7 + qa) * 512 + (lane & 31) * 16 + (lane >> 5) * 8) = u32x2{va.x, va.z};
      *(LAS u32x2*)(L + D_H + ((wave >> 2) * 7 + qb) * 512 + (lane & 31) * 16 + (lane >> 5) * 8) = u32x2{vb.x, vb.z};
      LGKM0();
      lds_wr(L, L_HR + wave * 4, 1);
      lds_wait_set(L, L_HR + (wave & 4) * 4 + (lane & 3) * 4);
    }
    STAMP(2);
    f32x4 ga[8];
#pragma unroll
    for (int e = 0; e < 8; ++e) ga[e] = f32x4{0.f, 0.f, 0.f, 0.f};
    {
      s16x8 hf = *(LAS s16x8*)(L + D_H + ((wave >> 2) * 7 + 6) * 512 + (g * 8 + (i & 7)) * 16);
#pragma unroll
      for (int q = 6; q >= 0; --q) {
        s16x8 hn = hf;
        if (q > 0) hn = *(LAS s16x8*)(L + D_H + ((wave >> 2) * 7 + q - 1) * 512 + (g * 8 + (i & 7)) * 16);
        mma_frags8(ga, wb + q * 8, hf);
        if (q == 5) {
#pragma unroll
          for (int e = 0; e < 4; ++e) wb[e] = *(LAS u32x4*)(L + D_W + wave * 4352 + e * 1024 + lane * 16);
        }
        SB();
        hf = hn;
      }
    }
    STAMP(4);
    {
      u32 ln = threadIdx.x & 63;
      PIN(ln);
      if ((ln & 15) < 8) {
        const u32 o = D_W + wave * 4352 + (ln & 7) * 64 + (((ln >> 4) ^ ((ln >> 1) & 3)) << 4);
#pragma unroll
        for (int e = 0; e < 8; ++e) *(LAS f32x4*)(L + o + e * 512) = ga[e];
      }
    }
    LGKM0();
    lds_wr(L, L_PW + 4 * wave, 1);
    if (wave >= 4) {
      {
        u32 ln = threadIdx.x & 63;
        PIN(ln);
        const u32 gq = 2 * (ln >> 5) + ((ln >> 1) & 1), o = lbase + D_W + (wave - 4) * 4352 + ((ln >> 2) & 7) * 512 + (ln & 1) * 8;
        u32 a[4], oy = WS_YS + (du >> 1) * 65536 + (du & 1) * 4096 + (wave - 4) * 1024 + ln * 16;
#pragma unroll
        for (int k = 0; k < 4; ++k) { a[k] = o + k * 128 + ((gq ^ k) << 4); PIN(a[k]); }
        PIN(oy);
        lds_wait_set(L, L_PW + (wave - 4) * 4);
        f32x2 p[8], q[8];
#pragma unroll
        for (int tk = 0; tk < 8; ++tk) { p[tk] = *(LAS f32x2*)(LP(a[tk >> 1]) + (tk & 1) * 64); q[tk] = *(LAS f32x2*)(LP(a[tk >> 1]) + (tk & 1) * 64 + 4 * 4352); }
        SB();
#pragma unroll
        for (int tk = 0; tk < 8; ++tk) {
          const f32x2 y = p[tk] + q[tk];
          st_sc1_128(u32x4{__float_as_uint(y.x), tag, __float_as_uint(y.y), tag}, rsWS, oy + tk * 8192);
        }
      }
      LGKM0();
      lds_wr(L, L_HS + 4 * (wave - 4), 1);
      lds_wait_set(L, L_HS + (lane & 3) * 4);
      u32 ln = threadIdx.x & 63;
      PIN(ln);
      vO = (ln >> 3) * 2048 + (((ln & 7) ^ (((ln >> 4) & 3) << 1)) << 4) + cg * 128;
      wo_grp(20 + wave); wo_grp(24 + wave);
      VMCNT(0);
      lds_wr(L, L_HS + 4 * (wave - 4), 2);
    }
    STAMP(8);
  } else {
    const int lr = lane >> 3, lc = lane & 7;
    const int fv = fu - (fu > 2);
    const bool two = fu != 2 && fv < 24;
    const int la = (fu == 2) ? 50 : (fv >= 24) ? (fv - 24) * 8 + 3 : (fv >= 21) ? (fv - 21) * 16 + 2 : (fv / 3) * 8 + ((fv % 3) ? 2 + 2 * (fv % 3) : 0);
    const int lb = la + ((fv >= 21) ? 8 : 1);
    const rsrc_t rsG = mk_rsrc(P.wg, D_ * F_ * 2), rsU = mk_rsrc(P.wu, D_ * F_ * 2), rsX = mk_rsrc(P.x, S_ * D_ * 2);
    const u32 vg = (i < 8 || two) ? ((wave * 128 + 8 * g) * F_ + ((i < 8) ? la : lb) * 64 + 8 * (i & 7)) * 2 : 0x7f000000u;
    u32x4 wb[8 * GWIN];
    auto ldgrp = [&](int q) {
#pragma unroll
      for (int j = 0; j < 8; ++j) wb[(q % GWIN) * 8 + j] = ld_nt_128((q & 1) ? rsU : rsG, vg, ((q >> 1) * 32 + j) * (F_ * 2));
      SB();
    };
    __builtin_amdgcn_s_setprio(0);
    ldgrp(0);
    ldgrp(1);
    if (wave >= 4) { BAR(); tag = (u32)rfl((int)lds_rd(L, L_TAG)); }
    if (fu == 2) {
      *(LAS u32x4*)(L + 32 + threadIdx.x * 16) = u32x4{0u, 0u, 0u, 0u};
      if (threadIdx.x < 4) *(LAS u32x4*)(L + 32 + 8192 + threadIdx.x * 16) = u32x4{0u, 0u, 0u, 0u};
      wait_until(tstart, T_GATHER);
      u32x4 v;
      _Pragma("nounroll") for (int sp = 0; sp < SPIN_CAP; ++sp) {
        v = ld_sc1_128(rsWS, lane * 16, WS_RG + wave * 1024);
        if (__all(v.y == tag && v.w == tag)) break;
        __builtin_amdgcn_s_sleep(2);
      }
      const u32 ib = 2047 - 256 * wave - 4 * lane;
      const u32 key[4] = {(v.x << 16) | ib, (v.x & 0xffff0000u) | (ib - 1), (v.z << 16) | (ib - 2), (v.z & 0xffff0000u) | (ib - 3)};
      lds_wr(L, wave * 4, wave_max_u32(max(max(key[0], key[1]), max(key[2], key[3]))));
      LGKM0();
      BAR();
      STAMP(9);
      const u32x4 m0 = *(LAS u32x4*)L, m1 = *(LAS u32x4*)(L + 16);
      const u32 thr = min(min(min(m0.x, m0.y), min(m0.z, m0.w)), min(min(m1.x, m1.y), min(m1.z, m1.w)));
      const u32 c0 = key[0] >= thr, c1 = key[1] >= thr, c2 = key[2] >= thr, c3 = key[3] >= thr;
      if (c0 | c1 | c2 | c3) {
        const u32 p = 64 + 4 * __hip_atomic_fetch_add((LAS u32*)(L + 32), c0 + c1 + c2 + c3, __ATOMIC_RELAXED, __HIP_MEMORY_SCOPE_WORKGROUP);
        lds_wr(L, c0 ? p : 36, key[0]); lds_wr(L, c1 ? p + 4 * c0 : 36, key[1]);
        lds_wr(L, c2 ? p + 4 * (c0 + c1) : 36, key[2]); lds_wr(L, c3 ? p + 4 * (c0 + c1 + c2) : 36, key[3]);
      }
      LGKM0();
      BAR();
      const int n = rfl((int)lds_rd(L, 32));
      _Pragma("nounroll") for (int b = threadIdx.x; b < n; b += 512) {
        const u32 my = lds_rd(L, 64 + 4 * b);
        int rank = 0;
        _Pragma("nounroll") for (int t = 0; t < n; t += 8) {
          const u32x4 o = *(LAS u32x4*)(L + 64 + 4 * t), q = *(LAS u32x4*)(L + 80 + 4 * t);
          rank += (o.x > my) + (o.y > my) + (o.z > my) + (o.w > my) + (q.x > my) + (q.y > my) + (q.z > my) + (q.w > my);
        }
        if (rank < 8) st_sc1_64(u32x2{my ^ 0x7ffu, tag}, rsWS, WS_SEL + rank * 8);
      }
      if (threadIdx.x == 0) st_sc1_32(tag, rsWS, WS_EPOCH);
      STAMP(10);
      LGKM0();
      BAR();
    }
    constexpr u32 F_XS = 81920;
    ldgrp(2);
    if (wave == 0) {
      wait_until(tstart, T_SEL);
      u32x2 sk;
      _Pragma("nounroll") for (int sp = 0; sp < SPIN_CAP; ++sp) {
        sk = ld_sc1_64(rsWS, lr * 8, WS_SEL);
        if (__all(sk.y == tag)) break;
        __builtin_amdgcn_s_sleep(4);
      }
      STAMP(2);
      u32x4 xr[16];
#pragma unroll
      for (int j = 0; j < 16; ++j) xr[j] = __builtin_amdgcn_raw_buffer_load_b128(rsX, (sk.x & 0x7ffu) * 2048 + lc * 16, j * 128, 0);
      u32x2 ms;
      _Pragma("nounroll") for (int sp = 0; sp < SPIN_CAP; ++sp) {
        ms = ld_sc1_64(rsWS, (sk.x & 0x7ffu) * 8, WS_SG);
        if (__all(ms.y == tag)) break;
      }
#pragma unroll
      for (int j = 0; j < 16; ++j) *(LAS u32x4*)(L + F_XS + lr * 2064 + j * 128 + lc * 16) = xr[j];
      *(LAS u32*)(L + F_XS + 8 * 2064 + lr * 4) = ms.x;
      LGKM0();
      lds_wr(L, L_HR, 1);
    }
#pragma unroll
    for (int q = 3; q < GWIN; ++q) ldgrp(q);
#pragma unroll
    for (int q = 0; q < GPRE; ++q) transpose_grp(wb + 8 * q);
    lds_wait_set(L, L_HR);
    f32x4 ga[16];
#pragma unroll
    for (int e = 0; e < 16; ++e) ga[e] = f32x4{0.f, 0.f, 0.f, 0.f};
    const u32 ms = *(LAS u32*)(L + F_XS + 8 * 2064 + (i & 7) * 4);
    const float rsn = __builtin_amdgcn_rcpf(bf_hi(ms)), mrs = -bf_lo(ms) * rsn;
    s16x8 xf;
#pragma unroll
    for (int q = 0; q < 8; ++q) {
      if ((q & 1) == 0) {
        const u32x4 xv = *(LAS u32x4*)(L + F_XS + (i & 7) * 2064 + (wave * 128 + (q >> 1) * 32 + 8 * g) * 2);
        u32x4 xn;
#pragma unroll
        for (int e = 0; e < 4; ++e) xn[e] = pack2(__builtin_fmaf(bf_lo(xv[e]), rsn, mrs), __builtin_fmaf(bf_hi(xv[e]), rsn, mrs));
        xf = as_frag(xn);
      }
      if (q < GPRE) mma_frags8(ga + 8 * (q & 1), wb + q * 8, xf); else mma_cols(ga + 8 * (q & 1), wb + (q % GWIN) * 8, xf);
      SB();
      if (q == 1 || q == 2) ldgrp(q + 5);
    }
    STAMP(4);
    {
      u32 ln = threadIdx.x & 63;
      PIN(ln);
      if ((ln & 15) < 8) {
        const u32 o = wave * 10240 + (ln & 7) * 80 + (ln >> 4) * 16;
#pragma unroll
        for (int e = 0; e < 16; ++e) *(LAS f32x4*)(L + o + e * 640) = ga[e];
      }
    }
    {
      u32 ln = threadIdx.x & 63;
      PIN(ln);
      const u32 e = 4 * ((wave >> 1) & 1) + (ln >> 4), tk = (ln >> 1) & 7, m = 8 * (wave >> 2) + 4 * (wave & 1) + 2 * (ln & 1);
      u32 o0 = lbase + (e * 8 + tk) * 80 + m * 4, o1 = o0 + 40960, oh = WS_H + ((wave < 4) ? la : lb) * 2048 + (wave & 3) * 512 + ln * 8;
      PIN(o0); PIN(o1); PIN(oh);
      LGKM0();
      BAR();
      f32x2 pg[8], pu[8];
#pragma unroll
      for (int w = 0; w < 8; ++w) { pg[w] = *(LAS f32x2*)(LP((w < 4) ? o0 : o1) + (w & 3) * 10240); pu[w] = *(LAS f32x2*)(LP((w < 4) ? o0 : o1) + (w & 3) * 10240 + 5120); }
      SB();
      const f32x2 gs = ((pg[0] + pg[1]) + (pg[2] + pg[3])) + ((pg[4] + pg[5]) + (pg[6] + pg[7])), us = ((pu[0] + pu[1]) + (pu[2] + pu[3])) + ((pu[4] + pu[5]) + (pu[6] + pu[7]));
      const float g0 = rbf(gs.x), g1 = rbf(gs.y);
      const u32 hv = pack2(rbf(g0 * sigm(g0)) * rbf(us.x), rbf(g1 * sigm(g1)) * rbf(us.y));
      if (wave < 4 || two) st_sc1_64(u32x2{hv, tag}, rsWS, oh);
    }
    LGKM0();
    BAR();
    STAMP(8);
    if (wave >= 4) { helper_offsets(); helper_fill(); }
  }
  }

  if (wave >= 4) {
    const int lid = wave - 4;
    if (!dwn) {
      VMCNT(0);
      STAMP(3);
      BAR();
    }
    if (lid >= 2) {
      const u32 sk = lds_rd(L, L_SEL + 4 * (lane & 7));
      bool late = false;
      _Pragma("nounroll") for (int c = lid - 2; c < 8; c += 2) {
        const u32 tk = (u32)__builtin_amdgcn_readlane((int)sk, c) & 0x7ffu;
        if ((int)(tk >> 7) != pan2) continue;
        if (!late) {
          _Pragma("nounroll") for (int sp = 0; sp < SPIN_CAP; ++sp) { if (rfl((int)lds_rd(L, L_GO))) break; __builtin_amdgcn_s_sleep(4); }
          wait_until(__builtin_amdgcn_s_memrealtime(), T_Y);
          late = true;
        }
        float y0, y1;
        _Pragma("nounroll") for (int sp = 0; sp < SPIN_CAP; ++sp) {
          u32 bad = 0;
          y0 = 0.f; y1 = 0.f;
#pragma unroll
          for (int j = 0; j < 4; ++j) {
            const u32x4 v = ld_sc1_128(rsWS, (lane >> 5) * 65536 + (lane & 31) * 16, WS_YS + j * 131072 + c * 8192 + cg * 512);
            bad |= (v.y ^ tag) | (v.w ^ tag);
            y0 += __uint_as_float(v.x); y1 += __uint_as_float(v.z);
          }
          if (__all(bad == 0)) break;
          __builtin_amdgcn_s_sleep(8);
        }
        y0 = sum32(y0); y1 = sum32(y1);
        if (lane < 32) {
          const u32 o = L_UPD + (c * 64 + 16 * (lane & 3) + (lane >> 2)) * 4;
          *(LAS float*)(L + o) = y0; *(LAS float*)(L + o + 32) = y1;
        }
      }
      LGKM0();
      lds_wr(L, L_UPDF + 4 * (lid - 2), 1);
      STAMP(7);
    }
    return;
  }

  {
    const bool pol = wave == 0;
    const u32 fo = WS_OF + (((pan2 * 4 + 3 * (lane >> 5)) / 5) * 16 + (lane & 15)) * 128;
    u32 fv = ~tag;
    if (dwn && pol) fv = ld_sc1_32(rsWS, fo, 0);
    SB();
    if (!gemm && !dwn) BAR();
    const int par2 = wave >> 1, t2 = (wave & 1) * 2;
    u32 wa[4], wc[4];
    s16x8 wf[3][4];
    auto wo_addr = [&]() {
      u32 ln = threadIdx.x & 63;
      PIN(ln);
      const u32 g = ln >> 4, i = ln & 15, q4 = i >> 2, p4 = i & 3;
      const u32 wb = lbase + (4 * g + q4) * 128 + p4 * 8 + par2 * 2048, wsw = (2 * g + (q4 >> 1)) & 3;
#pragma unroll
      for (int c = 0; c < 4; ++c) { wa[c] = wb + ((c ^ wsw) << 5); wc[c] = wa[c] ^ 2048; PIN(wa[c]); PIN(wc[c]); }
    };
    auto wf_first = [&]() {
#pragma unroll
      for (int k = 0; k < 2; ++k)
#pragma unroll
        for (int c = 0; c < 4; ++c) wf[k][c] = tr_frag(LP(wa[c]), LP(wc[c]), k * 4096, k * 4096);
    };
    if (gemm) oc_init();
    SB();
    if (gemm) {
      VMCNT(0);
      if (lane == 0 && __hip_atomic_fetch_add((LAS u32*)(L + L_ARR), 1u, __ATOMIC_RELAXED, __HIP_MEMORY_SCOPE_WORKGROUP) == 3)
        st_sc1_32(tag, rsWS, WS_OF + (pan * 16 + hp) * 128);
      STAMP(4);
      SB();
      wo_addr();
      BAR();
      wf_first();
    }
    const int T0 = pan2 * 4 + t2;
    const u32 sk = lds_rd(L, L_SEL + 4 * (lane & 7));
    __builtin_amdgcn_s_waitcnt(0xc07f);
    SB();
    if (!gemm) wait_until(tstart, T_O);
    if (pol) {
      _Pragma("nounroll") for (int sp = 0; sp < SPIN_CAP; ++sp) {
        if (__all(fv == tag)) break;
        if (sp) __builtin_amdgcn_s_sleep(2);
        fv = ld_sc1_32(rsWS, fo, 0);
      }
      lds_wr(L, L_GO, 1);
    } else {
      _Pragma("nounroll") for (int sp = 0; sp < SPIN_CAP; ++sp) if (rfl((int)lds_rd(L, L_GO))) break;
    }
    CBAR();
    STAMP(5);
    const u32 abase = WS_O + (par2 * 32 * 64 + T0) * 1024;
    u32x4 af[OWIN][2];
#pragma unroll
    for (int hh = 0; hh < OWIN; ++hh) {
#pragma unroll
      for (int tt = 0; tt < 2; ++tt) af[hh][tt] = ld_sc1_128(rsWS, lane * 16 + tt * 1024, abase + ((hh + hrot) & 31) * 65536);
      SB();
    }
    if (!gemm) {
      wo_addr(); wf_first();
      SB();
      oc_init();
      __builtin_amdgcn_s_waitcnt(0xc07f);
      SB();
    }
#pragma unroll
    for (int hh = 0; hh < 32; ++hh) {
      if (hh == 21 && dwn) {
        _Pragma("nounroll") for (int sp = 0; sp < SPIN_CAP; ++sp) if (__all(lds_rd(L, L_HS + (lane & 3) * 4) == 2u)) break;
        CBAR(); SB();
      }
      if (hh == 14) {
#pragma unroll
        for (int c = 0; c < 4; ++c) { wa[c] += 65536; wc[c] += 65536; PIN(wa[c]); PIN(wc[c]); }
        SB();
      }
      if (hh == 31) __builtin_amdgcn_s_waitcnt(0xc07f); else __builtin_amdgcn_s_waitcnt(0xc87f);
      SB();
      if (hh + 2 < 32) {
#pragma unroll
        for (int c = 0; c < 4; ++c) wf[(hh + 2) % 3][c] = tr_frag(LP(wa[c]), LP(wc[c]), ((hh + 2) & 15) * 4096, ((hh + 2) & 15) * 4096);
      }
#pragma unroll
      for (int tt = 0; tt < 2; ++tt) {
#pragma unroll
        for (int c = 0; c < 4; ++c) oc[tt][c] = mfma(wf[hh % 3][c], as_frag(af[hh % OWIN][tt]), oc[tt][c]);
        if (hh + OWIN < 32) af[hh % OWIN][tt] = ld_sc1_128(rsWS, lane * 16 + tt * 1024, abase + ((hh + OWIN + hrot) & 31) * 65536);
      }
#pragma unroll
      for (int k = 0; k < 2; ++k) {
        __builtin_amdgcn_sched_group_barrier(0x008, 1, 0); __builtin_amdgcn_sched_group_barrier(0x100, 2, 0);
        __builtin_amdgcn_sched_group_barrier(0x008, 1, 0); __builtin_amdgcn_sched_group_barrier(0x100, 1, 0);
        __builtin_amdgcn_sched_group_barrier(0x008, 1, 0); __builtin_amdgcn_sched_group_barrier(0x100, 1, 0);
        __builtin_amdgcn_sched_group_barrier(0x008, 1, 0); __builtin_amdgcn_sched_group_barrier(0x020, 1, 0);
      }
      SB();
    }
    STAMP(6);
    {
      const u32 stl = (sk & 0x7ffu) - (u32)(pan2 * 128 + par2 + 32 * t2);
      u64 hit = __ballot(stl < 64u && (stl & 1u) == 0u) & 0xffull;
      if (hit) {
        lds_wait_set(L, L_UPDF + (lane & 1) * 4);
        while (hit) {
          const int c = __builtin_ctzll(hit);
          hit &= hit - 1;
          const u32 tl = (u32)__builtin_amdgcn_readlane((int)stl, c), ob = (u32)__builtin_amdgcn_readlane((int)sk, c) >> 16;
          const float sg = rbf(sigm(bf_lo((ob & 0x8000u) ? (ob & 0x7fffu) : (~ob & 0xffffu))));
          const float f0 = ((tl >> 1) == (u32)i) ? sg : 0.f, f1 = ((tl >> 1) == (u32)i + 16u) ? sg : 0.f;
#pragma unroll
          for (int cc = 0; cc < 4; ++cc) {
            const f32x4 u = *(LAS f32x4*)(L + L_UPD + (c * 64 + 16 * cc + 4 * g) * 4);
            oc[0][cc] += u * f0; oc[1][cc] += u * f1;
          }
        }
      }
    }
    const rsrc_t rsO = mk_rsrc(P.out, S_ * D_ * 2);
    const u32 xo = ((pan2 * 128 + par2 + 32 * t2 + 2 * i) * D_ + cg * 64 + (g & 1) * 16 + (g >> 1) * 8) * 2;
#pragma unroll
    for (int tt = 0; tt < 2; ++tt)
#pragma unroll
      for (int cp = 0; cp < 2; ++cp) {
        const u32 a0 = pack2(oc[tt][2 * cp][0], oc[tt][2 * cp][1]), a1 = pack2(oc[tt][2 * cp][2], oc[tt][2 * cp][3]);
        const u32 b0 = pack2(oc[tt][2 * cp + 1][0], oc[tt][2 * cp + 1][1]), b1 = pack2(oc[tt][2 * cp + 1][2], oc[tt][2 * cp + 1][3]);
        const auto e0 = __builtin_amdgcn_permlane16_swap(a0, b0, false, false);
        const auto e1 = __builtin_amdgcn_permlane16_swap(a1, b1, false, false);
        st_sc1_128(u32x4{e0[0], e1[0], e0[1], e1[1]}, rsO, xo + tt * 65536 + cp * 64);
      }
    STAMP(7);
  }
}

static u64 k_order(int odd, int pl, int hl) {
  const int np = 7 - odd;
  int slot[16], used[16];
  for (int k = 0; k < 16; ++k) { slot[k] = -1; used[k] = 0; }
  for (int k = 2; k < 16; ++k) {
    const int h0 = k & 3, p0 = (k * (odd ? 5 : 3)) % np;
    int want = -1;
    if (hl == h0 && pl % np == p0) want = k - 2; else if (hl == h0 || pl % np == p0) want = k - 1;
    if (want >= 0) {
      if (want < 1) want = 1;
      while (want > 1 && used[want]) --want;
      if (!used[want]) { slot[k] = want; used[want] = 1; }
    }
  }
  int s = 0;
  for (int k = 0; k < 16; ++k) if (slot[k] < 0) { while (used[s]) ++s; slot[k] = s; used[s] = 1; }
  u64 r = 0;
  for (int k = 0; k < 16; ++k) r |= (u64)k << (4 * slot[k]);
  return r;
}

extern "C" void kernel_launch(void* const* d_in, const int* in_sizes, int n_in, void* d_out, int out_size, void* d_ws, size_t ws_size,
                              hipStream_t stream) {
  Args A;
  Params& P = A.p;
  P.x = (const u16*)d_in[0]; P.wqkv = (const u16*)d_in[1]; P.wo = (const u16*)d_in[2]; P.wr = (const u16*)d_in[4];
  P.wg = (const u16*)d_in[5]; P.wu = (const u16*)d_in[6]; P.wd = (const u16*)d_in[7];
  P.out = (u16*)d_out; P.ws = (char*)d_ws;
  for (int e = 0; e < 64; ++e) A.kt[e] = k_order(e >> 5, (e >> 2) & 7, e & 3);
  (void)hipFuncSetAttribute((const void*)fused_kernel, hipFuncAttributeMaxDynamicSharedMemorySize, L_END + 64);
  hipLaunchKernelGGL(fused_kernel, dim3(256), dim3(512), L_END + 64, stream, A);
}
```

```cpp
#include <hip/hip_runtime.h>
#include <stdint.h>

typedef unsigned short u16;
typedef unsigned u32;
typedef unsigned long long u64;
typedef __attribute__((ext_vector_type(4))) short s16x4;
typedef __attribute__((ext_vector_type(8))) short s16x8;
typedef __attribute__((ext_vector_type(2))) float f32x2;
typedef __attribute__((ext_vector_type(4))) float f32x4;
typedef __attribute__((ext_vector_type(4))) unsigned u32x4;
typedef __attribute__((ext_vector_type(2))) unsigned u32x2;
typedef __attribute__((ext_vector_type(16))) unsigned u32x16;
typedef __attribute__((ext_vector_type(2))) __bf16 bf16x2;
typedef __amdgpu_buffer_rsrc_t rsrc_t;
#define LAS __attribute__((address_space(3)))
#define GAS __attribute__((address_space(1)))
#define DEV __device__ __forceinline__

#define STAMPS 0
constexpr int OWIN = 6;
constexpr int GWIN = 6, GPRE = 4;
constexpr int T_GATHER = 350, T_SEL = 580, T_H = 1200, T_O = 1520;
constexpr int T_Y = 150;
constexpr int WARM_AT = 10;
constexpr u64 HELPK = 0x765432ull, ROLLK = 0xfedcba98ull;
constexpr int HELPN = 6, RS0 = 1, RSN = 8;
constexpr int CODE_LINES = 339;

constexpr int S_ = 2048, D_ = 1024, F_ = 3584;
constexpr u32 WS_EPOCH = 0;
constexpr u32 WS_SEL = 4096;
constexpr u32 WS_RG = 16384;
constexpr u32 WS_SG = 32768;
constexpr u32 WS_OF = 65536;
constexpr u32 WS_H = WS_OF + 131072;
constexpr u32 WS_YS = WS_H + 131072;
constexpr u32 WS_O = WS_YS + 524288;
constexpr u32 WS_END = WS_O + S_ * D_ * 2;
constexpr u32 WS_STAMP = 16u << 20;
constexpr u32 WOFF = 22528, SLOT = 47104;
constexpr u32 L_HX = 131072;
constexpr u32 L_XT = 3 * SLOT;
constexpr u32 L_STQ = L_XT + 16384;
constexpr u32 L_STO = L_STQ + 1408;
constexpr u32 L_UPD = L_STO + 1024;
constexpr u32 L_ONE = L_UPD + 2048;
constexpr u32 L_CTL = L_ONE + 128;
constexpr u32 L_TAG = L_CTL, L_ARR = L_CTL + 4, L_UPDF = L_CTL + 8  , L_HS = L_CTL + 16  , L_SEL = L_CTL + 32  ;
constexpr u32 L_PW = L_CTL + 64, L_HR = L_CTL + 96;
constexpr u32 L_GO = L_CTL + 128;
constexpr u32 L_END = L_CTL + 192;
constexpr int SPIN_CAP = 1 << 22;
static_assert(L_END <= 163840 && L_ONE % 128 == 0, "LDS");

struct Params {
  const u16 *x, *wqkv, *wo, *wr, *wg, *wu, *wd;
  u16* out;
  char* ws;
};
struct Args {
  Params p;
  u64 kt[64];
};

DEV float bf_lo(u32 v) { return __uint_as_float(v << 16); }
DEV float bf_hi(u32 v) { return __uint_as_float(v & 0xffff0000u); }
DEV u32 pack2(float a, float b) { bf16x2 p = {(__bf16)a, (__bf16)b}; return __builtin_bit_cast(u32, p); }
DEV float rbf(float a) { return bf_lo(pack2(a, a)); }
DEV float dot2(u32 a, u32 b, float c) { return __builtin_amdgcn_fdot2_f32_bf16(__builtin_bit_cast(bf16x2, a), __builtin_bit_cast(bf16x2, b), c, false); }
DEV s16x8 as_frag(u32x4 v) { return __builtin_bit_cast(s16x8, v); }
DEV f32x4 mfma(s16x8 a, s16x8 b, f32x4 c) { return __builtin_amdgcn_mfma_f32_16x16x32_bf16(a, b, c, 0, 0, 0); }
DEV int rfl(int v) { return __builtin_amdgcn_readfirstlane(v); }
template <int CTRL, int RM, bool BC> DEV float dppf(float v) { return __int_as_float(__builtin_amdgcn_update_dpp(0, __float_as_int(v), CTRL, RM, 0xf, BC)); }
template <int CTRL, int RM, bool BC> DEV u32 dppu(u32 v) { return (u32)__builtin_amdgcn_update_dpp(0, (int)v, CTRL, RM, 0xf, BC); }
DEV float half_sum(float v) {
  v += dppf<0x111, 0xf, true>(v); v += dppf<0x112, 0xf, true>(v); v += dppf<0x114, 0xf, true>(v); v += dppf<0x118, 0xf, true>(v);
  v += dppf<0x142, 0xa, false>(v);
  return v;
}
DEV u32 wave_max_u32(u32 v) {
  v = max(v, dppu<0x111, 0xf, true>(v)); v = max(v, dppu<0x112, 0xf, true>(v)); v = max(v, dppu<0x114, 0xf, true>(v)); v = max(v, dppu<0x118, 0xf, true>(v));
  v = max(v, dppu<0x142, 0xa, false>(v)); v = max(v, dppu<0x143, 0xc, false>(v));
  return (u32)__builtin_amdgcn_readlane((int)v, 63);
}
DEV float vmax(float a, float b) { return __builtin_elementwise_maximum(a, b); }
DEV float vmax3(float a, float b, float c) { return vmax(vmax(a, b), c); }
DEV float max16(float v) { u32 a = __float_as_uint(v); auto r = __builtin_amdgcn_permlane16_swap(a, a, false, false); u32 x = r[0], y = r[1]; return vmax(__uint_as_float(x), __uint_as_float(y)); }
DEV float max32(float v) { u32 a = __float_as_uint(v); auto r = __builtin_amdgcn_permlane32_swap(a, a, false, false); u32 x = r[0], y = r[1]; return vmax(__uint_as_float(x), __uint_as_float(y)); }
DEV float sum32(float v) { u32 a = __float_as_uint(v); auto r = __builtin_amdgcn_permlane32_swap(a, a, false, false); u32 x = r[0], y = r[1]; return __uint_as_float(x) + __uint_as_float(y); }
DEV u32 ord16(u32 b) { return (b & 0x8000u) ? (~b & 0xffffu) : (b | 0x8000u); }
DEV float sigm(float v) { return __builtin_amdgcn_rcpf(1.f + __builtin_amdgcn_exp2f(-v * 1.4426950408889634f)); }

DEV rsrc_t mk_rsrc(const void* p, u32 bytes) { return __builtin_amdgcn_make_buffer_rsrc((void*)p, 0, (int)bytes, 0x00020000); }
DEV u32x4 ld_nt_128(rsrc_t rs, u32 voff, u32 soff) { return __builtin_amdgcn_raw_buffer_load_b128(rs, voff, soff, 2); }
DEV u32x4 ld_sc1_128(rsrc_t rs, u32 voff, u32 soff) { return __builtin_amdgcn_raw_buffer_load_b128(rs, voff, soff, 16); }
DEV u32x2 ld_sc1_64(rsrc_t rs, u32 voff, u32 soff) { return __builtin_amdgcn_raw_buffer_load_b64(rs, voff, soff, 16); }
DEV u32 ld_sc1_32(rsrc_t rs, u32 voff, u32 soff) { return __builtin_amdgcn_raw_buffer_load_b32(rs, voff, soff, 16); }
DEV void st_sc1_128(u32x4 v, rsrc_t rs, u32 voff) { __builtin_amdgcn_raw_buffer_store_b128(v, rs, voff, 0, 16); }
DEV void st_sc1_64(u32x2 v, rsrc_t rs, u32 voff) { __builtin_amdgcn_raw_buffer_store_b64(v, rs, voff, 0, 16); }
DEV void st_sc1_32(u32 v, rsrc_t rs, u32 voff) { __builtin_amdgcn_raw_buffer_store_b32(v, rs, voff, 0, 16); }
#define VMCNT(n) asm volatile("s_waitcnt vmcnt(" #n ")" ::: "memory")
#define LGKM0() asm volatile("s_waitcnt lgkmcnt(0)" ::: "memory")
#define CBAR() asm volatile("" ::: "memory")
#define BAR() do { CBAR(); __builtin_amdgcn_s_barrier(); CBAR(); } while (0)
#define SB() __builtin_amdgcn_sched_barrier(0)
#define LP(a) ((LAS char*)(uintptr_t)(a))
#define PIN(x) asm volatile("" : "+v"(x))

#define TICK() 0ull
#define RT() 0ull
#define STAMP(k) do {} while (0)
#define CSTAMP(k) do {} while (0)
#define LSTAMP(k) do {} while (0)

DEV u32 lds_rd(LAS char* L, u32 off) { return *(volatile LAS u32*)(L + off); }
DEV void lds_wr(LAS char* L, u32 off, u32 v) { *(volatile LAS u32*)(L + off) = v; }
DEV void lds_wait_set(LAS char* L, u32 off) {
  _Pragma("nounroll") for (int sp = 0; sp < SPIN_CAP; ++sp) {
    if (__all(lds_rd(L, off) != 0u)) break;
    __builtin_amdgcn_s_sleep(1);
  }
  CBAR();
}
DEV void wait_until(u64 t0, u32 ticks) {
  while (__builtin_amdgcn_s_memrealtime() - t0 < ticks) __builtin_amdgcn_s_sleep(4);
}
#define DMA_HEAD "s_mov_b32 m0, %0\n\ts_nop 0\n\t"
DEV void dma16(u32 lds, u32 voff, rsrc_t rs, u32 soff) {
  asm volatile(DMA_HEAD "buffer_load_dwordx4 %1, %2, %3 offen lds" ::"s"(lds), "v"(voff), "s"(rs), "s"(soff) : "memory");
}
DEV void dma16x4(u32 lds, u32 v0, u32 v1, u32 v2, u32 v3, rsrc_t rs, u32 soff) {
  asm volatile(DMA_HEAD "buffer_load_dwordx4 %1, %5, %6 offen lds\n\tbuffer_load_dwordx4 %2, %5, %6 offen offset:1024 lds\n\t"
               "buffer_load_dwordx4 %3, %5, %6 offen offset:2048 lds\n\tbuffer_load_dwordx4 %4, %5, %6 offen offset:3072 lds"
               ::"s"(lds), "v"(v0), "v"(v1), "v"(v2), "v"(v3), "s"(rs), "s"(soff) : "memory");
}
DEV void dma16x2(u32 lds, u32 v0, u32 v1, rsrc_t rs, u32 soff) {
  asm volatile(DMA_HEAD "buffer_load_dwordx4 %1, %3, %4 offen lds\n\tbuffer_load_dwordx4 %2, %3, %4 offen offset:1024 lds"
               ::"s"(lds), "v"(v0), "v"(v1), "s"(rs), "s"(soff) : "memory");
}

struct Frags { s16x8 x[6]; s16x8 w[6]; };

DEV s16x8 tr_frag(LAS char* p, LAS char* q, u32 o0, u32 o1) {
  s16x4 a = __builtin_amdgcn_ds_read_tr16_b64_v4i16((LAS s16x4*)(p + o0));
  s16x4 b = __builtin_amdgcn_ds_read_tr16_b64_v4i16((LAS s16x4*)(q + o1));
  return __builtin_shufflevector(a, b, 0, 1, 2, 3, 4, 5, 6, 7);
}
template <int KK> DEV void load_frags(Frags& f, LAS char* aH, LAS char* aT, LAS char* w0, LAS char* w1) {
#pragma unroll
  for (int t = 0; t < 5; ++t) f.x[1 + t] = *(LAS s16x8*)(aT + t * 2048);
  f.x[0] = *(LAS s16x8*)aH;
#pragma unroll
  for (int sec = 0; sec < 3; ++sec) {
    f.w[2 * sec] = tr_frag(w0, w0, sec * 8192 + KK * 4096, sec * 8192 + KK * 4096 + 512);
    f.w[2 * sec + 1] = tr_frag(w1, w1, sec * 8192 + KK * 4096, sec * 8192 + KK * 4096 + 512);
  }
}
template <int A, int B> DEV void mma_range(f32x4 (&acc)[6][6], const Frags& f, s16x8 e0) {
#pragma unroll
  for (int n = A; n < B; ++n) {
    const int p = n / 11, r = n % 11, w = 2 * p + (r >= 6), t = (r >= 6) ? 11 - r : 5 - r;
    acc[w][t] = (w < 4) ? mfma(f.w[w], (w == 0 && t == 0) ? e0 : f.x[t], acc[w][t]) : mfma(f.x[t], f.w[w], acc[w][t]);
  }
}
DEV void interleave() {
#pragma unroll
  for (int k = 0; k < 6; ++k) { __builtin_amdgcn_sched_group_barrier(0x008, 1, 0); __builtin_amdgcn_sched_group_barrier(0x100, 1, 0); }
#pragma unroll
  for (int k = 0; k < 6; ++k) {
    __builtin_amdgcn_sched_group_barrier(0x008, 1, 0); __builtin_amdgcn_sched_group_barrier(0x100, 1, 0);
    __builtin_amdgcn_sched_group_barrier(0x008, 1, 0); __builtin_amdgcn_sched_group_barrier(0x100, 1, 0);
    __builtin_amdgcn_sched_group_barrier(0x008, 1, 0);
  }
}

template <int E> DEV s16x8 col_frag(const u32x4* L) {
  constexpr u32 sel = (E & 1) ? 0x07060302u : 0x05040100u;
  u32x4 r;
  r.x = __builtin_amdgcn_perm(L[1][E >> 1], L[0][E >> 1], sel);
  r.y = __builtin_amdgcn_perm(L[3][E >> 1], L[2][E >> 1], sel);
  r.z = __builtin_amdgcn_perm(L[5][E >> 1], L[4][E >> 1], sel);
  r.w = __builtin_amdgcn_perm(L[7][E >> 1], L[6][E >> 1], sel);
  return as_frag(r);
}
DEV void mma_cols(f32x4* acc, const u32x4* L, s16x8 b) {
  acc[0] = mfma(col_frag<0>(L), b, acc[0]); acc[1] = mfma(col_frag<1>(L), b, acc[1]);
  acc[2] = mfma(col_frag<2>(L), b, acc[2]); acc[3] = mfma(col_frag<3>(L), b, acc[3]);
  acc[4] = mfma(col_frag<4>(L), b, acc[4]); acc[5] = mfma(col_frag<5>(L), b, acc[5]);
  acc[6] = mfma(col_frag<6>(L), b, acc[6]); acc[7] = mfma(col_frag<7>(L), b, acc[7]);
}

DEV void transpose_grp(u32x4* L) {
  const s16x8 f0 = col_frag<0>(L), f1 = col_frag<1>(L), f2 = col_frag<2>(L), f3 = col_frag<3>(L), f4 = col_frag<4>(L), f5 = col_frag<5>(L), f6 = col_frag<6>(L), f7 = col_frag<7>(L);
  L[0] = __builtin_bit_cast(u32x4, f0); L[1] = __builtin_bit_cast(u32x4, f1); L[2] = __builtin_bit_cast(u32x4, f2); L[3] = __builtin_bit_cast(u32x4, f3);
  L[4] = __builtin_bit_cast(u32x4, f4); L[5] = __builtin_bit_cast(u32x4, f5); L[6] = __builtin_bit_cast(u32x4, f6); L[7] = __builtin_bit_cast(u32x4, f7);
#pragma unroll
  for (int e = 0; e < 8; ++e) PIN(L[e]);
  SB();
}
DEV void mma_frags8(f32x4* acc, const u32x4* L, s16x8 b) {
#pragma unroll
  for (int e = 0; e < 8; ++e) acc[e] = mfma(as_frag(L[e]), b, acc[e]);
}
DEV void warm_line(u32 lds, const char* a) {
  asm volatile(DMA_HEAD "global_load_lds_dword %1, off" ::"s"(lds), "v"(a) : "memory");
}

__global__ void __launch_bounds__(512, 2) fused_kernel(Args Parg) {
  extern __shared__ __attribute__((aligned(128))) char smem[];
  u32x16 ka;
  u64 kb;
  asm volatile("s_load_dwordx16 %0, %2, 0x0\n\ts_load_dwordx2 %1, %2, 0x40" : "=&s"(ka), "=&s"(kb) : "s"(__builtin_amdgcn_kernarg_segment_ptr()));
  Params P;
  const char* pc0;
  asm volatile("s_getpc_b64 %0" : "=s"(pc0));
  LAS char* L = (LAS char*)smem;
  const u32 lbase = (u32)(uintptr_t)L;
  const int lane = threadIdx.x & 63;
  const int wave = rfl(threadIdx.x >> 6);
  const int bid = blockIdx.x, xg = bid & 7, loc = bid >> 3;
  const int np = 7 - (xg & 1);
  const bool gemm = loc < 4 * np;
  const int pl = loc >> 2, hl = loc & 3;
  const int pan = (xg & 1) * 7 + pl, hp = (xg >> 1) * 4 + hl, r0 = pan * 160;
  const int fi = loc - 4 * np;
  const bool dwn = (u32)(fi - 2 - 3 * (xg & 1)) <= 2u * (xg & 1);
  const int fu = (xg & 1) * 12 + (xg >> 1) * (3 + 2 * (xg & 1)) + min(fi, 2 + 2 * (xg & 1));
  const int du = (xg & 1) ? 4 + (xg >> 1) * 3 + fi - 5 : (xg >> 1);
  const int pan2 = (xg & 1) * 8 + (xg >> 1) * 2 + (loc >> 4);
  const int cg = (int)((((loc & 16) ? 0xfedc0a987653421bull : 0xfedc8a9b76534210ull) >> (4 * (loc & 15))) & 15), hrot = 2 * cg;
  const int g = lane >> 4, i = lane & 15;
  u64 kord;
  asm volatile("s_load_dwordx2 %0, %1, %2" : "=&s"(kord) : "s"(__builtin_amdgcn_kernarg_segment_ptr()), "s"(72 + (((xg & 1) * 32 + loc) & 63) * 8));
  rsrc_t rsWS;
  const u64 tstart = __builtin_amdgcn_s_memrealtime();
  u32 tag = 0;
  f32x4 oc[2][4];
  auto oc_init = [&]() {
    const int tl = (wave >> 1) + 64 * (wave & 1) + 2 * i;
    f32x2 st[2];
    u32x2 v[2][4];
#pragma unroll
    for (int tt = 0; tt < 2; ++tt) {
      st[tt] = *(LAS f32x2*)(L + L_STO + tl * 8 + tt * 256);
#pragma unroll
      for (int c = 0; c < 4; ++c) v[tt][c] = *(LAS u32x2*)(L + L_XT + tl * 128 + (((2 * c + (g >> 1)) ^ (i & 7)) << 4) + (g & 1) * 8 + tt * 4096);
    }
    SB();
#pragma unroll
    for (int tt = 0; tt < 2; ++tt) {
      const float k1 = 1.f + st[tt].y, k0 = -st[tt].x * st[tt].y;
#pragma unroll
      for (int c = 0; c < 4; ++c) oc[tt][c] = f32x4{bf_lo(v[tt][c].x), bf_hi(v[tt][c].x), bf_lo(v[tt][c].y), bf_hi(v[tt][c].y)} * k1 + k0;
    }
#pragma unroll
    for (int q = 0; q < 8; ++q) PIN(oc[q >> 2][q & 3]);
  };

  u32 tq[3], vO, vx;
  auto helper_offsets = [&]() {
    u32 lane = threadIdx.x & 63;
    PIN(lane);
    const int lr = lane >> 3, lc = lane & 7;
#pragma unroll
    for (int j = 0; j < 3; ++j) {
      const int rho = min(64 * j + (int)lane, 175), pr = rho >= 96;
      const int tk = r0 + ((rho < 16) ? rho - 16 : pr + 2 * (rho - 16 - 80 * pr));
      const u32 a = (u32)(pan2 * 128 + (rho & 127)) * 8, b = (((u32)tk >= (u32)S_) | (rho == 0)) ? 1u : (u32)tk * 8;
      tq[j] = (wave == 7) ? a : b;
      PIN(tq[j]);
    }
    vO = lr * 2048 + ((u32)(lc ^ (((lr >> 1) & 3) << 1)) << 4) + cg * 128;
    vx = lr * 2048 + ((u32)(lc ^ (lr >> 1)) << 4) + cg * 128;
    PIN(vO); PIN(vx);
  };
  auto wo_grp = [&](int q) { dma16x4(lbase + q * 4096, vO, vO + 15 * 1024, vO + 30 * 1024, vO + 45 * 1024, mk_rsrc(P.wo, D_ * D_ * 2), ((q + hrot) & 31) * 65536); };
  auto helper_fill = [&]() {
    const int lid = wave - 4;
    tag = (u32)rfl((int)lds_rd(L, L_TAG));
    if (gemm) { VMCNT(11); BAR(); }
    if (lid >= 2) {
      u32x2 sv[3];
#pragma unroll
      for (int q = 2 * lid - 4; q < 2 * lid - 2; ++q) dma16x4(lbase + L_XT + q * 4096, vx, (vx ^ 64) + 15 * 1024, vx + 30 * 1024, (vx ^ 64) + 45 * 1024, mk_rsrc(P.x, S_ * D_ * 2), (pan2 * 128 + 32 * q) * 2048);
#pragma unroll
      for (int j = 0; j < 3; ++j) sv[j] = ld_sc1_64(rsWS, tq[j] & ~7u, WS_SG);
      SB();
      if (gemm) { VMCNT(11); BAR(); }
      _Pragma("nounroll") for (int sp = 0; sp < SPIN_CAP; ++sp) {
        if (__all(sv[0].y == tag && sv[1].y == tag && sv[2].y == tag)) break;
        __builtin_amdgcn_s_sleep(2);
#pragma unroll
        for (int j = 0; j < 3; ++j) sv[j] = ld_sc1_64(rsWS, tq[j] & ~7u, WS_SG);
      }
      f32x2 st[3];
      u32 ta = lbase + ((lid == 3) ? L_STO + lane * 8 : L_STQ + lane * 4), tb2 = ta + ((lid == 3) ? 4 : 704);
      u32 tc = (lid == 2 && lane < 48) ? ta + 512 : lbase + L_UPD + lane * 4, td = (lid == 2 && lane < 48) ? tb2 + 512 : tc;
      const u32 ts = (lid == 3) ? 512 : 256;
#pragma unroll
      for (int j = 0; j < 3; ++j) {
        st[j] = f32x2{__uint_as_float((sv[j].x << 16) ^ ((lid == 3) ? 0u : 0x80000000u)), __builtin_amdgcn_rcpf(bf_hi(sv[j].x))};
        if (tq[j] & 1u) st[j] = f32x2{0.f, 0.f};
        PIN(st[j]);
      }
      u32 te = ta + ts, tf = tb2 + ts;
      PIN(ta); PIN(tb2); PIN(tc); PIN(td); PIN(te); PIN(tf);
      VMCNT(0);
      if (gemm) BAR();
      *(LAS float*)LP(ta) = st[0].x; *(LAS float*)LP(tb2) = st[0].y;
      *(LAS float*)LP(te) = st[1].x; *(LAS float*)LP(tf) = st[1].y;
      *(LAS float*)LP(tc) = st[2].x; *(LAS float*)LP(td) = st[2].y;
      LGKM0();
    } else {
      if (gemm) { VMCNT(0); BAR(); }
      wo_grp(12 + lid); wo_grp(14 + lid);
#pragma unroll
      for (int q = 0; q < 4; ++q) wo_grp(16 + 2 * q + lid);
      if (gemm) BAR();
    }
    if (gemm) BAR();
    int l2 = lid;
    asm volatile("" : "+s"(l2));
    wo_grp(l2); wo_grp(4 + l2); wo_grp(8 + l2);
    if (!dwn) { wo_grp(24 + l2); wo_grp(28 + l2); }
    if (lid == 2) {
      u32x2 sk;
      _Pragma("nounroll") for (int sp = 0; sp < SPIN_CAP; ++sp) {
        sk = ld_sc1_64(rsWS, (lane & 7) * 8, WS_SEL);
        if (__all(sk.y == tag)) break;
        __builtin_amdgcn_s_sleep(8);
      }
      if (lane < 8) lds_wr(L, L_SEL + 4 * lane, sk.x);
      LGKM0();
    }
  };
  {
    u32 ln = threadIdx.x & 63;
    PIN(ln);
    if (ln < 2) warm_line(lbase + L_UPD, pc0 + min((loc * 8 + wave) * 2 + (int)ln, CODE_LINES - 1) * 128);
  }
  const int lid = wave - 4;
  const bool isx = lid < 2;
  u32 v0 = 0, v1 = 0, v2 = 0, v3 = 0, vh = 0, g0 = 0, g1 = 0, g2 = 0, kstep = 0, d0 = 0;
  {
    int wave_a = wave;
    asm volatile("" : "+s"(wave_a));
    if (gemm && wave_a >= 4) {
      u32 ln = threadIdx.x & 63;
      PIN(ln);
      const int lr = ln >> 3, lc = ln & 7;
      const u32 sw = (u32)(lc ^ (isx ? lr >> 1 : lr & 2)) << 4;
      const u32 vb = lr * (isx ? 4096 : 6144) + sw + (isx ? (r0 + lid) * 2048 : hp * 128);
      const u32 pp = (isx ? 32768 : 49152) - 1024;
      v0 = vb; v1 = (vb ^ 64) + pp; v2 = vb + 2 * pp; v3 = (vb ^ 64) + 3 * pp;
      g0 = (lid == 3) ? 198656 : 0; g1 = isx ? 131072 : (lid == 3) ? 4096 : 196608; g2 = isx ? 262144 : (lid == 3) ? 200704 : 2048;
      vh = max(r0 - 16 + 8 * lid + lr, 0) * 2048 + (sw ^ (lid << 6));
      kstep = isx ? 128u : 64u * 6144u; d0 = lbase + (isx ? 2 + 10 * lid : 22 + 12 * (lid - 2)) * 1024;
      PIN(v0); PIN(v1); PIN(v2); PIN(v3); PIN(vh);
      asm volatile("" : "+s"(g0), "+s"(g1), "+s"(g2), "+s"(kstep), "+s"(d0));
    }
  }
  u32 ev;
  asm volatile("s_waitcnt lgkmcnt(0)" : "+s"(ka), "+s"(kb), "+s"(kord));
  {
    auto ptr = [&](int k) { return (char*)(GAS char*)(((u64)ka[2 * k + 1] << 32) | ka[2 * k]); };
    P.x = (const u16*)ptr(0); P.wqkv = (const u16*)ptr(1); P.wo = (const u16*)ptr(2); P.wr = (const u16*)ptr(3);
    P.wg = (const u16*)ptr(4); P.wu = (const u16*)ptr(5); P.wd = (const u16*)ptr(6); P.out = (u16*)ptr(7);
    P.ws = (char*)(GAS char*)kb;
  }
  rsWS = mk_rsrc(P.ws, WS_END);
  STAMP(0);
  if (wave < 4) {
    asm volatile("s_load_dword %0, %1, 0x0" : "=s"(ev) : "s"(P.ws + WS_EPOCH) : "memory");
    u32 ln = threadIdx.x & 63;
    PIN(ln);
    const u32 d = lbase + 2 * SLOT + wave * 6144, v = ln * 16;
    dma16x4(d, v, v, v, v, mk_rsrc(P.x, S_ * D_ * 2), (pan2 * 128 + cg * 8 + wave * 2) * 2048);
    dma16x2(d + 4096, v, v, mk_rsrc(P.wr, D_ * 2), 0);
  }
  SB();
  int wave_b = wave;
  asm volatile("" : "+s"(wave_b));
  if (gemm && wave_b >= 4) {
    const rsrc_t rs = mk_rsrc(isx ? P.x : P.wqkv, isx ? S_ * D_ * 2 : D_ * 3 * D_ * 2);
    auto issue_k = [&](u32 ko, u32 so) {
      dma16x4(d0 + so, v0, v1, v2, v3, rs, ko + g0);
      dma16x4(d0 + so + 4096, v0, v1, v2, v3, rs, ko + g1);
      if (isx) { dma16x2(d0 + so + 8192, v0, v1, rs, ko + g2); dma16(lbase + so + lid * 1024, vh, rs, ko); }
      else dma16x4(d0 + so + 8192, v0, v1, v2, v3, rs, ko + g2);
    };
    auto issue = [&](int s, u32 so) { issue_k(((u32)(kord >> (4 * s)) & 15u) * kstep, so); };
    LSTAMP(20);
    issue(0, 0);
    LSTAMP(21);
    helper_offsets();
    issue(1, SLOT);
    LSTAMP(22);
    if (isx) VMCNT(11); else VMCNT(12);
    LSTAMP(23);
    BAR();
    const bool helps = !isx && !(xg & 2) && hl != 3;
    if (helps) {
      constexpr int HL = (HELPN * 192 + 35) / 36;
      const int n = min(((pl * 3 + hl) * 2 + lid - 2) * HL + min(lane, HL - 1), HELPN * 192 - 1), row = n / 3;
      warm_line(lbase + L_UPD, (const char*)P.wqkv + (size_t)((u32)((HELPK >> (4 * (row >> 6))) & 15) * 64 + (row & 63)) * 6144 + (n % 3) * 2048 + (hp | 3) * 128);
    }
    issue(2, 2 * SLOT);
    u32 so = 0, sdum = 0;
    const int sn = min((pl * 4 + hl) * 4 + lid, 95) * 2;
    const u32 so0 = (sn / 3) * 6144 + (sn % 3) * 2048, so1 = ((sn + 1) / 3) * 6144 + ((sn + 1) % 3) * 2048;
    const char* sbase = (const char*)P.wqkv + (hp | 3) * 128;
    _Pragma("nounroll") for (int s = 0; s < 13; ++s) {
      if (helps && s == 0) VMCNT(13); else if (!isx || s == WARM_AT + 1 || s == WARM_AT + 2) VMCNT(12); else VMCNT(11);
      if (s == 0) LSTAMP(24);
      if (s == 1) LSTAMP(25);
      if (s == 2) LSTAMP(26);
      BAR();
      issue(s + 3, so);
      if (!(xg & 2) && (u32)(s - RS0) < RSN) {
        const u32 ko = (u32)((ROLLK >> (4 * (s - RS0))) & 15) * (64u * 6144u), a0 = ko + so0, a1 = ko + so1;
        asm volatile("s_load_dword %0, %1, %2\n\ts_load_dword %0, %1, %2 offset:64\n\ts_load_dword %0, %1, %3\n\ts_load_dword %0, %1, %3 offset:64"
                     : "+s"(sdum) : "s"(sbase), "s"(a0), "s"(a1) : "memory");
      }
      if (s == WARM_AT && isx) warm_line(lbase + L_UPD, (const char*)P.wo + (size_t)min((pan * 16 + hp) * 80 + lid * 40 + min(lane, 39), 16383) * 128);
      so = (so == 2 * SLOT) ? 0 : so + SLOT;
    }
    asm volatile("s_waitcnt lgkmcnt(0)" : "+s"(sdum));
    STAMP(2);
    helper_fill();
  }
  if (wave < 4) {
    __builtin_amdgcn_s_setprio(1);
    if (wave == 1) lds_wr(L, L_ONE + lane * 4, lane < 32 ? 0x3f803f80u : 0u);
    if (wave == 2) lds_wr(L, L_GO, 0u);
    f32x4 acc[6][6];
#pragma unroll
    for (int w = 0; w < 6; ++w)
#pragma unroll
      for (int t = (w & 1); t < 6; ++t) { acc[w][t] = f32x4{0.f, 0.f, 0.f, 0.f}; PIN(acc[w][t]); }
    f32x4 bprev, bhalo;
    u32 mc[2];
    u64 cur[4];
#pragma unroll
    for (int r = 0; r < 4; ++r) {
      const int kap = 4 * g + r, d = 16 + (wave & 1) + 2 * i - kap;
      bprev[r] = (kap - i >= 9) ? 0.f : -1e30f;
      bhalo[r] = (((d & 1) == 0) & (d <= 14) & (pan > 0)) ? 0.f : -1e30f;
      cur[r] = __ballot((u32)(i - kap) <= 7u);
      asm volatile("" : "+s"(cur[r]));
    }
    mc[0] = (((u32)(i - 4 * g) <= 7u) ? 0xffffu : 0u) | (((u32)(i - 4 * g - 1) <= 7u) ? 0xffff0000u : 0u);
    mc[1] = (((u32)(i - 4 * g - 2) <= 7u) ? 0xffffu : 0u) | (((u32)(i - 4 * g - 3) <= 7u) ? 0xffff0000u : 0u);
    u32 hxl = lbase + L_HX + (wave & 2) * 2304 + lane * 16;
    u32 obase = WS_O + (((wave & 1) * 32 + hp * 2 + (wave >> 1)) * 64 + pan * 5) * 1024 + lane * 16;
    PIN(bprev); PIN(bhalo); PIN(mc[0]); PIN(mc[1]); PIN(hxl); PIN(obase);
    SB();
    {
      const int tok = pan2 * 128 + cg * 8 + wave * 2 + (lane >> 5);
      u32x4 xv[4], wv[4];
      VMCNT(0);
      {
        LAS char* sp = L + 2 * SLOT + wave * 6144 + (lane & 31) * 16;
#pragma unroll
        for (int j = 0; j < 4; ++j) { xv[j] = *(LAS u32x4*)(sp + (lane >> 5) * 2048 + j * 512); wv[j] = *(LAS u32x4*)(sp + 4096 + j * 512); }
      }
      asm volatile("s_waitcnt lgkmcnt(0)" : "+s"(ev) :: "memory");
      const u32 e = ev;
      tag = e * 0x9E3779B1u + 0x7F4A7C15u; tag ^= tag >> 15; tag = (u32)rfl((int)(tag ^ (u32)(tag == e)));
      if (wave == 1) lds_wr(L, L_TAG, tag);
      float sx = 0.f, sxx = 0.f, sxw = 0.f, sw = 0.f;
#pragma unroll
      for (int j = 0; j < 4; ++j)
#pragma unroll
        for (int e2 = 0; e2 < 4; ++e2) {
          const u32 a = xv[j][e2], b = wv[j][e2];
          sx = dot2(a, 0x3f803f80u, sx); sxx = dot2(a, a, sxx); sxw = dot2(a, b, sxw); sw = dot2(b, 0x3f803f80u, sw);
        }
      const float mean = half_sum(sx) * (1.f / D_);
      const float var = fmaxf(half_sum(sxx) * (1.f / D_) - mean * mean, 0.f);
      const float mb = rbf(mean), sb = rbf(__builtin_amdgcn_sqrtf(rbf(rbf(var) + 1e-5f)));
      const u32 ro = ord16(pack2((half_sum(sxw) - mb * half_sum(sw)) * __builtin_amdgcn_rcpf(sb), 0.f) & 0xffffu);
      const u32 rlo = (u32)__builtin_amdgcn_readlane((int)ro, 31);
      if ((lane & 31) == 31) st_sc1_64(u32x2{pack2(mb, sb), tag}, rsWS, WS_SG + tok * 8);
      if (lane == 63) st_sc1_64(u32x2{rlo | (ro << 16), tag}, rsWS, WS_RG + (tok >> 1) * 8);
      if (!gemm) { LGKM0(); BAR(); }
    }
    STAMP(1);
    if (gemm) {
      const int h = wave >> 1, par = wave & 1;
        const int q4 = i >> 2, p4 = i & 3;
        u32 a0 = lbase + i * 128 + ((g ^ (i >> 1)) << 4);
        const u32 wsw = ((q4 >> 1) & 1) | ((g & 1) << 1);
        u32 w0 = lbase + WOFF + (8 * g + q4) * 128 + (((2 * h + par) ^ wsw) << 5) + p4 * 8;
        const u32 tb = (1 + 5 * par) * 2048;
        const u32 one = lbase + L_ONE;
        u32 pH = (i == 0) ? one : a0, pT = a0 + tb, pW0 = w0, pW1 = w0 ^ 32;
        PIN(a0); PIN(w0); PIN(pH); PIN(pT); PIN(pW0); PIN(pW1);
        Frags f0, f1;
        const u32 e1 = (i == 0) ? 0x3f803f80u : 0u;
        s16x8 e0 = as_frag(u32x4{e1, e1, e1, e1});
        PIN(e0);
        __builtin_amdgcn_s_waitcnt(0xc07f);
        BAR();
        STAMP(2); CSTAMP(14);
        load_frags<0>(f0, LP(pH), LP(pT), LP(pW0), LP(pW1));
        pH ^= 64; pT ^= 64;
        __builtin_amdgcn_s_waitcnt(0xc07f);
        u32 so = 0, trc = 0;
        auto half1 = [&]() {
          SB();
          load_frags<1>(f1, LP(pH), LP(pT), LP(pW0), LP(pW1));
          mma_range<0, 24>(acc, f0, e0);
          interleave();
          SB();
          so = (so == 2 * SLOT) ? 0 : so + SLOT;
          mma_range<24, 25>(acc, f0, e0);
          SB();
          pT = so + a0; pW0 = so + w0; PIN(pT); PIN(pW0);
          mma_range<25, 26>(acc, f0, e0);
          SB();
          pH = (i == 0) ? one : pT; pW1 = pW0 ^ 32; PIN(pH); PIN(pW1);
          mma_range<26, 27>(acc, f0, e0);
          SB();
          pT += tb; PIN(pT);
          mma_range<27, 28>(acc, f0, e0);
          SB();
        };
        auto half1b = [&](int s) {
          const u64 tnow = TICK(), rnow = RT();
          mma_range<28, 30>(acc, f0, e0);
          SB();
          __builtin_amdgcn_s_waitcnt(0xc07f);
          if (STAMPS) { trc = (lane == s) ? (u32)tnow : trc; trc = (lane == s + 16) ? (u32)rnow : trc; }
          mma_range<30, 31>(acc, f0, e0);
          SB();
          BAR();
          SB();
          mma_range<31, 33>(acc, f0, e0);
          SB();
        };
        half1();
        for (int s = 0; s < 15; ++s) {
          half1b(s);
          load_frags<0>(f0, LP(pH), LP(pT), LP(pW0), LP(pW1));
          mma_range<0, 24>(acc, f1, e0);
          interleave();
          SB();
          pH ^= 64; pT ^= 64; PIN(pH); PIN(pT);
          mma_range<24, 31>(acc, f1, e0);
          SB();
          __builtin_amdgcn_s_waitcnt(0xc07f);
          mma_range<31, 33>(acc, f1, e0);
          half1();
        }
        half1b(15);
        if (STAMPS && lane < 32) ((GAS u32*)(P.ws + WS_STAMP + (1 << 20)))[(bid * 4 + wave) * 32 + lane] = trc;
      mma_range<5, 6>(acc, f1, e0); mma_range<16, 17>(acc, f1, e0); mma_range<27, 28>(acc, f1, e0);
      SB();
      mma_range<11, 16>(acc, f1, e0);
      u32 tz = threadIdx.x;
      PIN(tz);
      const u32 i = tz & 15, g = (tz >> 4) & 3;
      u32 sqi = lbase + L_STQ + i * 4, sqg = lbase + L_STQ + g * 16;
      PIN(sqi); PIN(sqg);
      const u32 hx = hxl + par * 2304, hy = hxl + (par ^ 1) * 2304;
      u32 hxg = hx - i * 16, hyg = hy - i * 16, hxi = hx - g * 256, hyi = hy - g * 256, sqi2 = sqi + par * 320, sqg2 = sqg + par * 320;
      const u32 hqa = hx + 2048 - g * 240, hqb = hxl + (L_UPD - L_HX) - (wave & 2) * 2304;
      u32 hq = __builtin_amdgcn_inverse_ballot_w64(0x0001000100010001ull) ? hqa : hqb;
      PIN(hxg); PIN(hyg); PIN(hxi); PIN(hyi); PIN(sqi2); PIN(sqg2); PIN(hq);
#pragma unroll
      for (int k = 0; k < 5; ++k) { __builtin_amdgcn_sched_group_barrier(0x008, 1, 0); __builtin_amdgcn_sched_group_barrier(0x002, 2, 0); }
      SB();
      *(LAS f32x4*)LP(hx) = acc[2][0];
      *(LAS f32x4*)LP(hx + 1024) = acc[4][0];
      *(LAS f32x4*)LP(hq) = acc[0][0];
      mma_range<17, 22>(acc, f1, e0);
#pragma unroll
      for (int k = 0; k < 3; ++k) { __builtin_amdgcn_sched_group_barrier(0x008, 1, 0); __builtin_amdgcn_sched_group_barrier(0x200, 1, 0); }
      SB();
      mma_range<0, 3>(acc, f1, e0);
      SB();
      __builtin_amdgcn_s_waitcnt(0xc07f);
      BAR();
      SB();
      f32x4 cs[4];
      cs[2] = *(LAS f32x4*)LP(hxg); cs[3] = *(LAS f32x4*)LP(hyg);
      cs[0] = *(LAS f32x4*)LP(hxg + 2048 - g * 240); cs[1] = *(LAS f32x4*)LP(hyg + 2048 - g * 240);
      float m[6], rs[6];
#pragma unroll
      for (int t = 0; t < 6; ++t) { m[t] = *(LAS float*)LP((t ? sqi2 : sqi) + t * 64); rs[t] = *(LAS float*)LP((t ? sqi2 : sqi) + t * 64 + 704); }
      acc[3][0] = *(LAS f32x4*)LP(hy);
      const float cv[2] = {*(LAS float*)LP(hxi + 1024), *(LAS float*)LP(hyi + 1024)};
      acc[5][0] = *(LAS f32x4*)LP(hy + 1024);
      mma_range<3, 5>(acc, f1, e0); mma_range<6, 11>(acc, f1, e0);
#pragma unroll
      for (int k = 0; k < 7; ++k) { __builtin_amdgcn_sched_group_barrier(0x008, 1, 0); __builtin_amdgcn_sched_group_barrier(0x100, 2, 0); }
      SB();
      STAMP(3); CSTAMP(15);
      const float QS = 0.17677669529663687f * 1.4426950408889634f;
      const f32x4 z4 = {0.f, 0.f, 0.f, 0.f};
      f32x4 s0[5], s1[5], m4[6], r4[6];
      {
        s16x8 kf[6];
#pragma unroll
        for (int t = 5; t >= 0; --t) {
          const f32x4 k0 = (acc[2][t] + m[t] * cs[2]) * rs[t], k1 = (acc[3][t] + m[t] * cs[3]) * rs[t];
          kf[t] = as_frag(u32x4{pack2(k0[0], k0[1]), pack2(k0[2], k0[3]), pack2(k1[0], k1[1]), pack2(k1[2], k1[3])});
        }
        mma_range<22, 27>(acc, f1, e0); mma_range<28, 33>(acc, f1, e0);
#pragma unroll
        for (int k = 0; k < 10; ++k) { __builtin_amdgcn_sched_group_barrier(0x008, 1, 0); __builtin_amdgcn_sched_group_barrier(0x002, 3, 0); }
        SB();
#pragma unroll
        for (int t = 0; t < 6; ++t) { m4[t] = *(LAS f32x4*)LP((t ? sqg2 : sqg) + t * 64); r4[t] = *(LAS f32x4*)LP((t ? sqg2 : sqg) + t * 64 + 704); }
#pragma unroll
        for (int t = 0; t < 5; ++t) {
          const f32x4 q0 = acc[0][t + 1] + m[t + 1] * cs[0], q1 = acc[1][t + 1] + m[t + 1] * cs[1];
          const s16x8 qf = as_frag(u32x4{pack2(q0[0], q0[1]), pack2(q0[2], q0[3]), pack2(q1[0], q1[1]), pack2(q1[2], q1[3])});
          s0[t] = mfma(kf[t], qf, t == 0 ? bhalo : bprev); s1[t] = mfma(kf[t + 1], qf, z4);
        }
      }
      SB();
      u32x16 vp[2];
#pragma unroll
      for (int t = 0; t < 6; ++t) {
#pragma unroll
        for (int c = 0; c < 2; ++c) {
          const f32x4 v = (acc[4 + c][t] + m4[t] * cv[c]) * r4[t];
          vp[c][2 * t] = pack2(v[0], v[1]); vp[c][2 * t + 1] = pack2(v[2], v[3]);
        }
      }
      PIN(vp[0]); PIN(vp[1]);
      float l[5][4], mx[5], rq[5];
#pragma unroll
      for (int t = 0; t < 5; ++t) {
#pragma unroll
        for (int r = 0; r < 4; ++r) l[t][r] = __builtin_amdgcn_inverse_ballot_w64(cur[r]) ? s1[t][r] : s0[t][r];
        mx[t] = vmax3(vmax(l[t][0], l[t][1]), l[t][2], l[t][3]);
        rq[t] = rs[t + 1] * QS;
      }
#pragma unroll
      for (int t = 0; t < 4; t += 2) {
        const auto r = __builtin_amdgcn_permlane16_swap(__float_as_uint(mx[t]), __float_as_uint(mx[t + 1]), false, false);
        const u32 r0 = r[0], r1 = r[1];
        const float z = max32(vmax(__uint_as_float(r0), __uint_as_float(r1)));
        const auto b = __builtin_amdgcn_permlane16_swap(__float_as_uint(z), __float_as_uint(z), false, false);
        const u32 b0 = b[0], b1 = b[1];
        mx[t] = -__uint_as_float(b0) * rq[t]; mx[t + 1] = -__uint_as_float(b1) * rq[t + 1];
      }
      mx[4] = -max32(max16(mx[4])) * rq[4];
      const s16x8 ones = as_frag(u32x4{0x3f803f80u, 0x3f803f80u, 0x3f803f80u, 0x3f803f80u});
      f32x4 sm[5], o0[5], o1[5];
#pragma unroll
      for (int t = 0; t < 5; ++t) {
#pragma unroll
        for (int r = 0; r < 4; ++r) l[t][r] = __builtin_amdgcn_exp2f(__builtin_fmaf(l[t][r], rq[t], mx[t]));
        const u32 p01 = pack2(l[t][0], l[t][1]), p23 = pack2(l[t][2], l[t][3]);
        const s16x8 pp = as_frag(u32x4{p01 & ~mc[0], p23 & ~mc[1], p01 & mc[0], p23 & mc[1]});
        sm[t] = mfma(ones, pp, z4);
        o0[t] = mfma(as_frag(u32x4{vp[0][2 * t], vp[0][2 * t + 1], vp[0][2 * t + 2], vp[0][2 * t + 3]}), pp, z4);
        o1[t] = mfma(as_frag(u32x4{vp[1][2 * t], vp[1][2 * t + 1], vp[1][2 * t + 2], vp[1][2 * t + 3]}), pp, z4);
      }
#pragma unroll
      for (int t = 0; t < 5; ++t) {
        const float inv = __builtin_amdgcn_rcpf(sm[t][0]);
        o0[t] *= inv; o1[t] *= inv;
        st_sc1_128(u32x4{pack2(o0[t][0], o0[t][1]), pack2(o0[t][2], o0[t][3]), pack2(o1[t][0], o1[t][1]), pack2(o1[t][2], o1[t][3])}, rsWS,
                   (t == 4) ? obase + ((pan == 12) ? 0x40000000u : 4096u) : obase + t * 1024);
      }
      STAMP(8);
    }
  }
  if (!gemm) {
  if (dwn) {
    const rsrc_t rsD = mk_rsrc(P.wd, F_ * D_ * 2);
    const int k0 = (du >> 1) * 14 + (wave >> 2) * 7;
    const u32 vd = (g * D_ + (du & 1) * 512 + (wave & 3) * 128 + 8 * i) * 2;
    auto drow = [&](int q, int j) -> u32 { return (u32)(64 * ((k0 + q) >> 1) + 4 * ((k0 + q) & 1) + 8 * j) * (D_ * 2); };
    constexpr u32 D_W = 24 * 4096, D_H = D_W + 8 * 4352;
    u32x4 wb[56];
#pragma unroll
    for (int j = 0; j < 8; ++j) wb[j] = ld_nt_128(rsD, vd, drow(0, j));
    SB();
    if (wave >= 4) { BAR(); tag = (u32)rfl((int)lds_rd(L, L_TAG)); }
#pragma unroll
    for (int j = 8; j < 48; ++j) wb[j] = ld_nt_128(rsD, vd, drow(j >> 3, j & 7));
    SB();
    transpose_grp(wb);
#pragma unroll
    for (int e = 0; e < 4; ++e) *(LAS u32x4*)(L + D_W + wave * 4352 + e * 1024 + lane * 16) = wb[e];
    SB();
#pragma unroll
    for (int q = 1; q < 6; ++q) transpose_grp(wb + 8 * q);
#pragma unroll
    for (int j = 48; j < 56; ++j) wb[j] = ld_nt_128(rsD, vd, drow(6, j & 7));
    SB();
    __builtin_amdgcn_s_setprio(0);
    if (wave >= 4) { helper_offsets(); helper_fill(); }
    transpose_grp(wb + 48);
    VMCNT(0);
    STAMP(12);
    BAR();
    STAMP(11);
    {
      wait_until(tstart, T_H);
      const int qa = 2 * (wave & 3), qb = min(qa + 1, 6);
      u32x4 va, vb;
      _Pragma("nounroll") for (int sp = 0; sp < SPIN_CAP; ++sp) {
        va = ld_sc1_128(rsWS, lane * 16, WS_H + (k0 + qa) * 1024);
        vb = ld_sc1_128(rsWS, lane * 16, WS_H + (k0 + qb) * 1024);
        if (__all(va.y == tag && va.w == tag && vb.y == tag && vb.w == tag)) break;
        __builtin_amdgcn_s_sleep(1);
      }
      *(LAS u32x2*)(L + D_H + ((wave >> 2) * 7 + qa) * 512 + (lane & 31) * 16 + (lane >> 5) * 8) = u32x2{va.x, va.z};
      *(LAS u32x2*)(L + D_H + ((wave >> 2) * 7 + qb) * 512 + (lane & 31) * 16 + (lane >> 5) * 8) = u32x2{vb.x, vb.z};
      LGKM0();
      lds_wr(L, L_HR + wave * 4, 1);
      lds_wait_set(L, L_HR + (wave & 4) * 4 + (lane & 3) * 4);
    }
    STAMP(2);
    f32x4 ga[8];
#pragma unroll
    for (int e = 0; e < 8; ++e) ga[e] = f32x4{0.f, 0.f, 0.f, 0.f};
    {
      s16x8 hf = *(LAS s16x8*)(L + D_H + ((wave >> 2) * 7 + 6) * 512 + (g * 8 + (i & 7)) * 16);
#pragma unroll
      for (int q = 6; q >= 0; --q) {
        s16x8 hn = hf;
        if (q > 0) hn = *(LAS s16x8*)(L + D_H + ((wave >> 2) * 7 + q - 1) * 512 + (g * 8 + (i & 7)) * 16);
        mma_frags8(ga, wb + q * 8, hf);
        if (q == 5) {
#pragma unroll
          for (int e = 0; e < 4; ++e) wb[e] = *(LAS u32x4*)(L + D_W + wave * 4352 + e * 1024 + lane * 16);
        }
        SB();
        hf = hn;
      }
    }
    STAMP(4);
    {
      u32 ln = threadIdx.x & 63;
      PIN(ln);
      if ((ln & 15) < 8) {
        const u32 o = D_W + wave * 4352 + (ln & 7) * 64 + (((ln >> 4) ^ ((ln >> 1) & 3)) << 4);
#pragma unroll
        for (int e = 0; e < 8; ++e) *(LAS f32x4*)(L + o + e * 512) = ga[e];
      }
    }
    LGKM0();
    lds_wr(L, L_PW + 4 * wave, 1);
    if (wave >= 4) {
      {
        u32 ln = threadIdx.x & 63;
        PIN(ln);
        const u32 gq = 2 * (ln >> 5) + ((ln >> 1) & 1), o = lbase + D_W + (wave - 4) * 4352 + ((ln >> 2) & 7) * 512 + (ln & 1) * 8;
        u32 a[4], oy = WS_YS + (du >> 1) * 65536 + (du & 1) * 4096 + (wave - 4) * 1024 + ln * 16;
#pragma unroll
        for (int k = 0; k < 4; ++k) { a[k] = o + k * 128 + ((gq ^ k) << 4); PIN(a[k]); }
        PIN(oy);
        lds_wait_set(L, L_PW + (wave - 4) * 4);
        f32x2 p[8], q[8];
#pragma unroll
        for (int tk = 0; tk < 8; ++tk) { p[tk] = *(LAS f32x2*)(LP(a[tk >> 1]) + (tk & 1) * 64); q[tk] = *(LAS f32x2*)(LP(a[tk >> 1]) + (tk & 1) * 64 + 4 * 4352); }
        SB();
#pragma unroll
        for (int tk = 0; tk < 8; ++tk) {
          const f32x2 y = p[tk] + q[tk];
          st_sc1_128(u32x4{__float_as_uint(y.x), tag, __float_as_uint(y.y), tag}, rsWS, oy + tk * 8192);
        }
      }
      LGKM0();
      lds_wr(L, L_HS + 4 * (wave - 4), 1);
      lds_wait_set(L, L_HS + (lane & 3) * 4);
      u32 ln = threadIdx.x & 63;
      PIN(ln);
      vO = (ln >> 3) * 2048 + (((ln & 7) ^ (((ln >> 4) & 3) << 1)) << 4) + cg * 128;
      wo_grp(20 + wave); wo_grp(24 + wave);
      VMCNT(0);
      lds_wr(L, L_HS + 4 * (wave - 4), 2);
    }
    STAMP(8);
  } else {
    const int lr = lane >> 3, lc = lane & 7;
    const int fv = fu - (fu > 2);
    const bool two = fu != 2 && fv < 24;
    const int la = (fu == 2) ? 50 : (fv >= 24) ? (fv - 24) * 8 + 3 : (fv >= 21) ? (fv - 21) * 16 + 2 : (fv / 3) * 8 + ((fv % 3) ? 2 + 2 * (fv % 3) : 0);
    const int lb = la + ((fv >= 21) ? 8 : 1);
    const rsrc_t rsG = mk_rsrc(P.wg, D_ * F_ * 2), rsU = mk_rsrc(P.wu, D_ * F_ * 2), rsX = mk_rsrc(P.x, S_ * D_ * 2);
    const u32 vg = (i < 8 || two) ? ((wave * 128 + 8 * g) * F_ + ((i < 8) ? la : lb) * 64 + 8 * (i & 7)) * 2 : 0x7f000000u;
    u32x4 wb[8 * GWIN];
    auto ldgrp = [&](int q) {
#pragma unroll
      for (int j = 0; j < 8; ++j) wb[(q % GWIN) * 8 + j] = ld_nt_128((q & 1) ? rsU : rsG, vg, ((q >> 1) * 32 + j) * (F_ * 2));
      SB();
    };
    __builtin_amdgcn_s_setprio(0);
    ldgrp(0);
    ldgrp(1);
    if (wave >= 4) { BAR(); tag = (u32)rfl((int)lds_rd(L, L_TAG)); }
    if (fu == 2) {
      *(LAS u32x4*)(L + 32 + threadIdx.x * 16) = u32x4{0u, 0u, 0u, 0u};
      if (threadIdx.x < 4) *(LAS u32x4*)(L + 32 + 8192 + threadIdx.x * 16) = u32x4{0u, 0u, 0u, 0u};
      wait_until(tstart, T_GATHER);
      u32x4 v;
      _Pragma("nounroll") for (int sp = 0; sp < SPIN_CAP; ++sp) {
        v = ld_sc1_128(rsWS, lane * 16, WS_RG + wave * 1024);
        if (__all(v.y == tag && v.w == tag)) break;
        __builtin_amdgcn_s_sleep(2);
      }
      const u32 ib = 2047 - 256 * wave - 4 * lane;
      const u32 key[4] = {(v.x << 16) | ib, (v.x & 0xffff0000u) | (ib - 1), (v.z << 16) | (ib - 2), (v.z & 0xffff0000u) | (ib - 3)};
      lds_wr(L, wave * 4, wave_max_u32(max(max(key[0], key[1]), max(key[2], key[3]))));
      LGKM0();
      BAR();
      STAMP(9);
      const u32x4 m0 = *(LAS u32x4*)L, m1 = *(LAS u32x4*)(L + 16);
      const u32 thr = min(min(min(m0.x, m0.y), min(m0.z, m0.w)), min(min(m1.x, m1.y), min(m1.z, m1.w)));
      const u32 c0 = key[0] >= thr, c1 = key[1] >= thr, c2 = key[2] >= thr, c3 = key[3] >= thr;
      if (c0 | c1 | c2 | c3) {
        const u32 p = 64 + 4 * __hip_atomic_fetch_add((LAS u32*)(L + 32), c0 + c1 + c2 + c3, __ATOMIC_RELAXED, __HIP_MEMORY_SCOPE_WORKGROUP);
        lds_wr(L, c0 ? p : 36, key[0]); lds_wr(L, c1 ? p + 4 * c0 : 36, key[1]);
        lds_wr(L, c2 ? p + 4 * (c0 + c1) : 36, key[2]); lds_wr(L, c3 ? p + 4 * (c0 + c1 + c2) : 36, key[3]);
      }
      LGKM0();
      BAR();
      const int n = rfl((int)lds_rd(L, 32));
      _Pragma("nounroll") for (int b = threadIdx.x; b < n; b += 512) {
        const u32 my = lds_rd(L, 64 + 4 * b);
        int rank = 0;
        _Pragma("nounroll") for (int t = 0; t < n; t += 8) {
          const u32x4 o = *(LAS u32x4*)(L + 64 + 4 * t), q = *(LAS u32x4*)(L + 80 + 4 * t);
          rank += (o.x > my) + (o.y > my) + (o.z > my) + (o.w > my) + (q.x > my) + (q.y > my) + (q.z > my) + (q.w > my);
        }
        if (rank < 8) st_sc1_64(u32x2{my ^ 0x7ffu, tag}, rsWS, WS_SEL + rank * 8);
      }
      if (threadIdx.x == 0) st_sc1_32(tag, rsWS, WS_EPOCH);
      STAMP(10);
      LGKM0();
      BAR();
    }
    constexpr u32 F_XS = 81920;
    ldgrp(2);
    if (wave == 0) {
      wait_until(tstart, T_SEL);
      u32x2 sk;
      _Pragma("nounroll") for (int sp = 0; sp < SPIN_CAP; ++sp) {
        sk = ld_sc1_64(rsWS, lr * 8, WS_SEL);
        if (__all(sk.y == tag)) break;
        __builtin_amdgcn_s_sleep(4);
      }
      STAMP(2);
      u32x4 xr[16];
#pragma unroll
      for (int j = 0; j < 16; ++j) xr[j] = __builtin_amdgcn_raw_buffer_load_b128(rsX, (sk.x & 0x7ffu) * 2048 + lc * 16, j * 128, 0);
      u32x2 ms;
      _Pragma("nounroll") for (int sp = 0; sp < SPIN_CAP; ++sp) {
        ms = ld_sc1_64(rsWS, (sk.x & 0x7ffu) * 8, WS_SG);
        if (__all(ms.y == tag)) break;
      }
#pragma unroll
      for (int j = 0; j < 16; ++j) *(LAS u32x4*)(L + F_XS + lr * 2064 + j * 128 + lc * 16) = xr[j];
      *(LAS u32*)(L + F_XS + 8 * 2064 + lr * 4) = ms.x;
      LGKM0();
      lds_wr(L, L_HR, 1);
    }
#pragma unroll
    for (int q = 3; q < GWIN; ++q) ldgrp(q);
#pragma unroll
    for (int q = 0; q < GPRE; ++q) transpose_grp(wb + 8 * q);
    lds_wait_set(L, L_HR);
    f32x4 ga[16];
#pragma unroll
    for (int e = 0; e < 16; ++e) ga[e] = f32x4{0.f, 0.f, 0.f, 0.f};
    const u32 ms = *(LAS u32*)(L + F_XS + 8 * 2064 + (i & 7) * 4);
    const float rsn = __builtin_amdgcn_rcpf(bf_hi(ms)), mrs = -bf_lo(ms) * rsn;
    s16x8 xf;
#pragma unroll
    for (int q = 0; q < 8; ++q) {
      if ((q & 1) == 0) {
        const u32x4 xv = *(LAS u32x4*)(L + F_XS + (i & 7) * 2064 + (wave * 128 + (q >> 1) * 32 + 8 * g) * 2);
        u32x4 xn;
#pragma unroll
        for (int e = 0; e < 4; ++e) xn[e] = pack2(__builtin_fmaf(bf_lo(xv[e]), rsn, mrs), __builtin_fmaf(bf_hi(xv[e]), rsn, mrs));
        xf = as_frag(xn);
      }
      if (q < GPRE) mma_frags8(ga + 8 * (q & 1), wb + q * 8, xf); else mma_cols(ga + 8 * (q & 1), wb + (q % GWIN) * 8, xf);
      SB();
      if (q == 1 || q == 2) ldgrp(q + 5);
    }
    STAMP(4);
    {
      u32 ln = threadIdx.x & 63;
      PIN(ln);
      if ((ln & 15) < 8) {
        const u32 o = wave * 10240 + (ln & 7) * 80 + (ln >> 4) * 16;
#pragma unroll
        for (int e = 0; e < 16; ++e) *(LAS f32x4*)(L + o + e * 640) = ga[e];
      }
    }
    {
      u32 ln = threadIdx.x & 63;
      PIN(ln);
      const u32 e = 4 * ((wave >> 1) & 1) + (ln >> 4), tk = (ln >> 1) & 7, m = 8 * (wave >> 2) + 4 * (wave & 1) + 2 * (ln & 1);
      u32 o0 = lbase + (e * 8 + tk) * 80 + m * 4, o1 = o0 + 40960, oh = WS_H + ((wave < 4) ? la : lb) * 2048 + (wave & 3) * 512 + ln * 8;
      PIN(o0); PIN(o1); PIN(oh);
      LGKM0();
      BAR();
      f32x2 pg[8], pu[8];
#pragma unroll
      for (int w = 0; w < 8; ++w) { pg[w] = *(LAS f32x2*)(LP((w < 4) ? o0 : o1) + (w & 3) * 10240); pu[w] = *(LAS f32x2*)(LP((w < 4) ? o0 : o1) + (w & 3) * 10240 + 5120); }
      SB();
      const f32x2 gs = ((pg[0] + pg[1]) + (pg[2] + pg[3])) + ((pg[4] + pg[5]) + (pg[6] + pg[7])), us = ((pu[0] + pu[1]) + (pu[2] + pu[3])) + ((pu[4] + pu[5]) + (pu[6] + pu[7]));
      const float g0 = rbf(gs.x), g1 = rbf(gs.y);
      const u32 hv = pack2(rbf(g0 * sigm(g0)) * rbf(us.x), rbf(g1 * sigm(g1)) * rbf(us.y));
      if (wave < 4 || two) st_sc1_64(u32x2{hv, tag}, rsWS, oh);
    }
    LGKM0();
    BAR();
    STAMP(8);
    if (wave >= 4) { helper_offsets(); helper_fill(); }
  }
  }

  if (wave >= 4) {
    const int lid = wave - 4;
    if (!dwn) {
      VMCNT(0);
      STAMP(3);
      BAR();
    }
    if (lid >= 2) {
      const u32 sk = lds_rd(L, L_SEL + 4 * (lane & 7));
      bool late = false;
      _Pragma("nounroll") for (int c = lid - 2; c < 8; c += 2) {
        const u32 tk = (u32)__builtin_amdgcn_readlane((int)sk, c) & 0x7ffu;
        if ((int)(tk >> 7) != pan2) continue;
        if (!late) {
          u32 tgo = 0;
          _Pragma("nounroll") for (int sp = 0; sp < SPIN_CAP; ++sp) { tgo = (u32)rfl((int)lds_rd(L, L_GO)); if (tgo) break; __builtin_amdgcn_s_sleep(4); }
          while ((int)((u32)__builtin_amdgcn_s_memrealtime() - tgo) < T_Y) __builtin_amdgcn_s_sleep(4);
          late = true;
        }
        float y0, y1;
        _Pragma("nounroll") for (int sp = 0; sp < SPIN_CAP; ++sp) {
          u32 bad = 0;
          y0 = 0.f; y1 = 0.f;
#pragma unroll
          for (int j = 0; j < 4; ++j) {
            const u32x4 v = ld_sc1_128(rsWS, (lane >> 5) * 65536 + (lane & 31) * 16, WS_YS + j * 131072 + c * 8192 + cg * 512);
            bad |= (v.y ^ tag) | (v.w ^ tag);
            y0 += __uint_as_float(v.x); y1 += __uint_as_float(v.z);
          }
          if (__all(bad == 0)) break;
          __builtin_amdgcn_s_sleep(8);
        }
        y0 = sum32(y0); y1 = sum32(y1);
        if (lane < 32) {
          const u32 o = L_UPD + (c * 64 + 16 * (lane & 3) + (lane >> 2)) * 4;
          *(LAS float*)(L + o) = y0; *(LAS float*)(L + o + 32) = y1;
        }
      }
      LGKM0();
      lds_wr(L, L_UPDF + 4 * (lid - 2), 1);
      STAMP(7);
    }
    return;
  }

  {
    const bool pol = wave == 0;
    const u32 fo = WS_OF + (((pan2 * 4 + 3 * (lane >> 5)) / 5) * 16 + (lane & 15)) * 128;
    u32 fv = ~tag;
    if (dwn && pol) fv = ld_sc1_32(rsWS, fo, 0);
    SB();
    if (!gemm && !dwn) BAR();
    const int par2 = wave >> 1, t2 = (wave & 1) * 2;
    u32 wa[4], wc[4];
    s16x8 wf[3][4];
    auto wo_addr = [&]() {
      u32 ln = threadIdx.x & 63;
      PIN(ln);
      const u32 g = ln >> 4, i = ln & 15, q4 = i >> 2, p4 = i & 3;
      const u32 wb = lbase + (4 * g + q4) * 128 + p4 * 8 + par2 * 2048, wsw = (2 * g + (q4 >> 1)) & 3;
#pragma unroll
      for (int c = 0; c < 4; ++c) { wa[c] = wb + ((c ^ wsw) << 5); wc[c] = wa[c] ^ 2048; PIN(wa[c]); PIN(wc[c]); }
    };
    auto wf_first = [&]() {
#pragma unroll
      for (int k = 0; k < 2; ++k)
#pragma unroll
        for (int c = 0; c < 4; ++c) wf[k][c] = tr_frag(LP(wa[c]), LP(wc[c]), k * 4096, k * 4096);
    };
    if (gemm) oc_init();
    SB();
    if (gemm) {
      VMCNT(0);
      if (lane == 0 && __hip_atomic_fetch_add((LAS u32*)(L + L_ARR), 1u, __ATOMIC_RELAXED, __HIP_MEMORY_SCOPE_WORKGROUP) == 3)
        st_sc1_32(tag, rsWS, WS_OF + (pan * 16 + hp) * 128);
      STAMP(4);
      SB();
      wo_addr();
      BAR();
      wf_first();
    }
    const int T0 = pan2 * 4 + t2;
    const u32 sk = lds_rd(L, L_SEL + 4 * (lane & 7));
    __builtin_amdgcn_s_waitcnt(0xc07f);
    SB();
    if (!gemm) wait_until(tstart, T_O);
    if (pol) {
      _Pragma("nounroll") for (int sp = 0; sp < SPIN_CAP; ++sp) {
        if (__all(fv == tag)) break;
        if (sp) __builtin_amdgcn_s_sleep(2);
        fv = ld_sc1_32(rsWS, fo, 0);
      }
      lds_wr(L, L_GO, (u32)__builtin_amdgcn_s_memrealtime() | 1u);
    } else {
      _Pragma("nounroll") for (int sp = 0; sp < SPIN_CAP; ++sp) if (rfl((int)lds_rd(L, L_GO))) break;
    }
    CBAR();
    STAMP(5);
    const u32 abase = WS_O + (par2 * 32 * 64 + T0) * 1024;
    u32x4 af[OWIN][2];
#pragma unroll
    for (int hh = 0; hh < OWIN; ++hh) {
#pragma unroll
      for (int tt = 0; tt < 2; ++tt) af[hh][tt] = ld_sc1_128(rsWS, lane * 16 + tt * 1024, abase + ((hh + hrot) & 31) * 65536);
      SB();
    }
    if (!gemm) {
      wo_addr(); wf_first();
      SB();
      oc_init();
      __builtin_amdgcn_s_waitcnt(0xc07f);
      SB();
    }
#pragma unroll
    for (int hh = 0; hh < 32; ++hh) {
      if (hh == 21 && dwn) {
        _Pragma("nounroll") for (int sp = 0; sp < SPIN_CAP; ++sp) if (__all(lds_rd(L, L_HS + (lane & 3) * 4) == 2u)) break;
        CBAR(); SB();
      }
      if (hh == 14) {
#pragma unroll
        for (int c = 0; c < 4; ++c) { wa[c] += 65536; wc[c] += 65536; PIN(wa[c]); PIN(wc[c]); }
        SB();
      }
      if (hh == 31) __builtin_amdgcn_s_waitcnt(0xc07f); else __builtin_amdgcn_s_waitcnt(0xc87f);
      SB();
      if (hh + 2 < 32) {
#pragma unroll
        for (int c = 0; c < 4; ++c) wf[(hh + 2) % 3][c] = tr_frag(LP(wa[c]), LP(wc[c]), ((hh + 2) & 15) * 4096, ((hh + 2) & 15) * 4096);
      }
#pragma unroll
      for (int tt = 0; tt < 2; ++tt) {
#pragma unroll
        for (int c = 0; c < 4; ++c) oc[tt][c] = mfma(wf[hh % 3][c], as_frag(af[hh % OWIN][tt]), oc[tt][c]);
        if (hh + OWIN < 32) af[hh % OWIN][tt] = ld_sc1_128(rsWS, lane * 16 + tt * 1024, abase + ((hh + OWIN + hrot) & 31) * 65536);
      }
#pragma unroll
      for (int k = 0; k < 2; ++k) {
        __builtin_amdgcn_sched_group_barrier(0x008, 1, 0); __builtin_amdgcn_sched_group_barrier(0x100, 2, 0);
        __builtin_amdgcn_sched_group_barrier(0x008, 1, 0); __builtin_amdgcn_sched_group_barrier(0x100, 1, 0);
        __builtin_amdgcn_sched_group_barrier(0x008, 1, 0); __builtin_amdgcn_sched_group_barrier(0x100, 1, 0);
        __builtin_amdgcn_sched_group_barrier(0x008, 1, 0); __builtin_amdgcn_sched_group_barrier(0x020, 1, 0);
      }
      SB();
    }
    STAMP(6);
    {
      const u32 stl = (sk & 0x7ffu) - (u32)(pan2 * 128 + par2 + 32 * t2);
      u64 hit = __ballot(stl < 64u && (stl & 1u) == 0u) & 0xffull;
      if (hit) {
        lds_wait_set(L, L_UPDF + (lane & 1) * 4);
        while (hit) {
          const int c = __builtin_ctzll(hit);
          hit &= hit - 1;
          const u32 tl = (u32)__builtin_amdgcn_readlane((int)stl, c), ob = (u32)__builtin_amdgcn_readlane((int)sk, c) >> 16;
          const float sg = rbf(sigm(bf_lo((ob & 0x8000u) ? (ob & 0x7fffu) : (~ob & 0xffffu))));
          const float f0 = ((tl >> 1) == (u32)i) ? sg : 0.f, f1 = ((tl >> 1) == (u32)i + 16u) ? sg : 0.f;
#pragma unroll
          for (int cc = 0; cc < 4; ++cc) {
            const f32x4 u = *(LAS f32x4*)(L + L_UPD + (c * 64 + 16 * cc + 4 * g) * 4);
            oc[0][cc] += u * f0; oc[1][cc] += u * f1;
          }
        }
      }
    }
    const rsrc_t rsO = mk_rsrc(P.out, S_ * D_ * 2);
    const u32 xo = ((pan2 * 128 + par2 + 32 * t2 + 2 * i) * D_ + cg * 64 + (g & 1) * 16 + (g >> 1) * 8) * 2;
#pragma unroll
    for (int tt = 0; tt < 2; ++tt)
#pragma unroll
      for (int cp = 0; cp < 2; ++cp) {
        const u32 a0 = pack2(oc[tt][2 * cp][0], oc[tt][2 * cp][1]), a1 = pack2(oc[tt][2 * cp][2], oc[tt][2 * cp][3]);
        const u32 b0 = pack2(oc[tt][2 * cp + 1][0], oc[tt][2 * cp + 1][1]), b1 = pack2(oc[tt][2 * cp + 1][2], oc[tt][2 * cp + 1][3]);
        const auto e0 = __builtin_amdgcn_permlane16_swap(a0, b0, false, false);
        const auto e1 = __builtin_amdgcn_permlane16_swap(a1, b1, false, false);
        st_sc1_128(u32x4{e0[0], e1[0], e0[1], e1[1]}, rsO, xo + tt * 65536 + cp * 64);
      }
    STAMP(7);
  }
}

static u64 k_order(int odd, int pl, int hl) {
  const int np = 7 - odd;
  int slot[16], used[16];
  for (int k = 0; k < 16; ++k) { slot[k] = -1; used[k] = 0; }
  for (int k = 2; k < 16; ++k) {
    const int h0 = k & 3, p0 = (k * (odd ? 5 : 3)) % np;
    int want = -1;
    if (hl == h0 && pl % np == p0) want = k - 2; else if (hl == h0 || pl % np == p0) want = k - 1;
    if (want >= 0) {
      if (want < 1) want = 1;
      while (want > 1 && used[want]) --want;
      if (!used[want]) { slot[k] = want; used[want] = 1; }
    }
  }
  int s = 0;
  for (int k = 0; k < 16; ++k) if (slot[k] < 0) { while (used[s]) ++s; slot[k] = s; used[s] = 1; }
  u64 r = 0;
  for (int k = 0; k < 16; ++k) r |= (u64)k << (4 * slot[k]);
  return r;
}

extern "C" void kernel_launch(void* const* d_in, const int* in_sizes, int n_in, void* d_out, int out_size, void* d_ws, size_t ws_size,
                              hipStream_t stream) {
  Args A;
  Params& P = A.p;
  P.x = (const u16*)d_in[0]; P.wqkv = (const u16*)d_in[1]; P.wo = (const u16*)d_in[2]; P.wr = (const u16*)d_in[4];
  P.wg = (const u16*)d_in[5]; P.wu = (const u16*)d_in[6]; P.wd = (const u16*)d_in[7];
  P.out = (u16*)d_out; P.ws = (char*)d_ws;
  for (int e = 0; e < 64; ++e) A.kt[e] = k_order(e >> 5, (e >> 2) & 7, e & 3);
  (void)hipFuncSetAttribute((const void*)fused_kernel, hipFuncAttributeMaxDynamicSharedMemorySize, L_END + 64);
  hipLaunchKernelGGL(fused_kernel, dim3(256), dim3(512), L_END + 64, stream, A);
}
```

```cpp
#include <hip/hip_runtime.h>
#include <stdint.h>

typedef unsigned short u16;
typedef unsigned u32;
typedef unsigned long long u64;
typedef __attribute__((ext_vector_type(4))) short s16x4;
typedef __attribute__((ext_vector_type(8))) short s16x8;
typedef __attribute__((ext_vector_type(2))) float f32x2;
typedef __attribute__((ext_vector_type(4))) float f32x4;
typedef __attribute__((ext_vector_type(4))) unsigned u32x4;
typedef __attribute__((ext_vector_type(2))) unsigned u32x2;
typedef __attribute__((ext_vector_type(16))) unsigned u32x16;
typedef __attribute__((ext_vector_type(2))) __bf16 bf16x2;
typedef __amdgpu_buffer_rsrc_t rsrc_t;
#define LAS __attribute__((address_space(3)))
#define GAS __attribute__((address_space(1)))
#define DEV __device__ __forceinline__

#define STAMPS 0
constexpr int OWIN = 6;
constexpr int GWIN = 5, GPRE = 3;
constexpr int T_GATHER = 350, T_SEL = 580, T_H = 1100, T_O = 1300;
constexpr int WARM_AT = 10;
constexpr u64 HELPK = 0x765432ull, ROLLK = 0xfedcba98ull;
constexpr int HELPN = 6, RS0 = 1, RSN = 8;
constexpr int CODE_LINES = 326;

constexpr int S_ = 2048, D_ = 1024, F_ = 3584;
constexpr u32 WS_EPOCH = 0;
constexpr u32 WS_SEL = 4096;
constexpr u32 WS_RG = 16384;
constexpr u32 WS_SG = 32768;
constexpr u32 WS_OF = 65536;
constexpr u32 WS_H = WS_OF + 131072;
constexpr u32 WS_YS = WS_H + 131072;
constexpr u32 WS_O = WS_YS + 524288;
constexpr u32 WS_END = WS_O + S_ * D_ * 2;
constexpr u32 WS_STAMP = 16u << 20;
constexpr u32 WOFF = 22528, SLOT = 47104;
constexpr u32 L_HX = 131072;
constexpr u32 L_XT = 3 * SLOT;
constexpr u32 L_STQ = L_XT + 16384;
constexpr u32 L_STO = L_STQ + 1408;
constexpr u32 L_UPD = L_STO + 1024;
constexpr u32 L_ONE = L_UPD + 2048;
constexpr u32 L_CTL = L_ONE + 128;
constexpr u32 L_TAG = L_CTL, L_ARR = L_CTL + 4, L_UPDF = L_CTL + 8  , L_HS = L_CTL + 16  , L_SEL = L_CTL + 32  ;
constexpr u32 L_PW = L_CTL + 64, L_HR = L_CTL + 96;
constexpr u32 L_GO = L_CTL + 128;
constexpr u32 L_END = L_CTL + 192;
constexpr int SPIN_CAP = 1 << 22;
static_assert(L_END <= 163840 && L_ONE % 128 == 0, "LDS");

struct Params {
  const u16 *x, *wqkv, *wo, *wr, *wg, *wu, *wd;
  u16* out;
  char* ws;
};
struct Args {
  Params p;
  u64 kt[64];
};

DEV float bf_lo(u32 v) { return __uint_as_float(v << 16); }
DEV float bf_hi(u32 v) { return __uint_as_float(v & 0xffff0000u); }
DEV u32 pack2(float a, float b) { bf16x2 p = {(__bf16)a, (__bf16)b}; return __builtin_bit_cast(u32, p); }
DEV float rbf(float a) { return bf_lo(pack2(a, a)); }
DEV float dot2(u32 a, u32 b, float c) { return __builtin_amdgcn_fdot2_f32_bf16(__builtin_bit_cast(bf16x2, a), __builtin_bit_cast(bf16x2, b), c, false); }
DEV s16x8 as_frag(u32x4 v) { return __builtin_bit_cast(s16x8, v); }
DEV f32x4 mfma(s16x8 a, s16x8 b, f32x4 c) { return __builtin_amdgcn_mfma_f32_16x16x32_bf16(a, b, c, 0, 0, 0); }
DEV int rfl(int v) { return __builtin_amdgcn_readfirstlane(v); }
template <int CTRL, int RM, bool BC> DEV float dppf(float v) { return __int_as_float(__builtin_amdgcn_update_dpp(0, __float_as_int(v), CTRL, RM, 0xf, BC)); }
template <int CTRL, int RM, bool BC> DEV u32 dppu(u32 v) { return (u32)__builtin_amdgcn_update_dpp(0, (int)v, CTRL, RM, 0xf, BC); }
DEV float half_sum(float v) {
  v += dppf<0x111, 0xf, true>(v); v += dppf<0x112, 0xf, true>(v); v += dppf<0x114, 0xf, true>(v); v += dppf<0x118, 0xf, true>(v);
  v += dppf<0x142, 0xa, false>(v);
  return v;
}
DEV u32 wave_max_u32(u32 v) {
  v = max(v, dppu<0x111, 0xf, true>(v)); v = max(v, dppu<0x112, 0xf, true>(v)); v = max(v, dppu<0x114, 0xf, true>(v)); v = max(v, dppu<0x118, 0xf, true>(v));
  v = max(v, dppu<0x142, 0xa, false>(v)); v = max(v, dppu<0x143, 0xc, false>(v));
  return (u32)__builtin_amdgcn_readlane((int)v, 63);
}
DEV float vmax(float a, float b) { return __builtin_elementwise_maximum(a, b); }
DEV float vmax3(float a, float b, float c) { return vmax(vmax(a, b), c); }
DEV float max16(float v) { u32 a = __float_as_uint(v); auto r = __builtin_amdgcn_permlane16_swap(a, a, false, false); u32 x = r[0], y = r[1]; return vmax(__uint_as_float(x), __uint_as_float(y)); }
DEV float max32(float v) { u32 a = __float_as_uint(v); auto r = __builtin_amdgcn_permlane32_swap(a, a, false, false); u32 x = r[0], y = r[1]; return vmax(__uint_as_float(x), __uint_as_float(y)); }
DEV float sum32(float v) { u32 a = __float_as_uint(v); auto r = __builtin_amdgcn_permlane32_swap(a, a, false, false); u32 x = r[0], y = r[1]; return __uint_as_float(x) + __uint_as_float(y); }
DEV u32 ord16(u32 b) { return (b & 0x8000u) ? (~b & 0xffffu) : (b | 0x8000u); }
DEV float sigm(float v) { return __builtin_amdgcn_rcpf(1.f + __builtin_amdgcn_exp2f(-v * 1.4426950408889634f)); }

DEV rsrc_t mk_rsrc(const void* p, u32 bytes) { return __builtin_amdgcn_make_buffer_rsrc((void*)p, 0, (int)bytes, 0x00020000); }
DEV u32x4 ld_nt_128(rsrc_t rs, u32 voff, u32 soff) { return __builtin_amdgcn_raw_buffer_load_b128(rs, voff, soff, 2); }
DEV u32x4 ld_sc1_128(rsrc_t rs, u32 voff, u32 soff) { return __builtin_amdgcn_raw_buffer_load_b128(rs, voff, soff, 16); }
DEV u32x2 ld_sc1_64(rsrc_t rs, u32 voff, u32 soff) { return __builtin_amdgcn_raw_buffer_load_b64(rs, voff, soff, 16); }
DEV u32 ld_sc1_32(rsrc_t rs, u32 voff, u32 soff) { return __builtin_amdgcn_raw_buffer_load_b32(rs, voff, soff, 16); }
DEV void st_sc1_128(u32x4 v, rsrc_t rs, u32 voff) { __builtin_amdgcn_raw_buffer_store_b128(v, rs, voff, 0, 16); }
DEV void st_sc1_64(u32x2 v, rsrc_t rs, u32 voff) { __builtin_amdgcn_raw_buffer_store_b64(v, rs, voff, 0, 16); }
DEV void st_sc1_32(u32 v, rsrc_t rs, u32 voff) { __builtin_amdgcn_raw_buffer_store_b32(v, rs, voff, 0, 16); }
#define VMCNT(n) asm volatile("s_waitcnt vmcnt(" #n ")" ::: "memory")
#define LGKM0() asm volatile("s_waitcnt lgkmcnt(0)" ::: "memory")
#define CBAR() asm volatile("" ::: "memory")
#define BAR() do { CBAR(); __builtin_amdgcn_s_barrier(); CBAR(); } while (0)
#define SB() __builtin_amdgcn_sched_barrier(0)
#define LP(a) ((LAS char*)(uintptr_t)(a))
#define PIN(x) asm volatile("" : "+v"(x))

#define TICK() 0ull
#define RT() 0ull
#define STAMP(k) do {} while (0)
#define CSTAMP(k) do {} while (0)
#define LSTAMP(k) do {} while (0)

DEV u32 lds_rd(LAS char* L, u32 off) { return *(volatile LAS u32*)(L + off); }
DEV void lds_wr(LAS char* L, u32 off, u32 v) { *(volatile LAS u32*)(L + off) = v; }
DEV void lds_wait_set(LAS char* L, u32 off) {
  _Pragma("nounroll") for (int sp = 0; sp < SPIN_CAP; ++sp) {
    if (__all(lds_rd(L, off) != 0u)) break;
    __builtin_amdgcn_s_sleep(1);
  }
  CBAR();
}
DEV void wait_until(u64 t0, u32 ticks) {
  while (__builtin_amdgcn_s_memrealtime() - t0 < ticks) __builtin_amdgcn_s_sleep(4);
}
#define DMA_HEAD "s_mov_b32 m0, %0\n\ts_nop 0\n\t"
DEV void dma16(u32 lds, u32 voff, rsrc_t rs, u32 soff) {
  asm volatile(DMA_HEAD "buffer_load_dwordx4 %1, %2, %3 offen lds" ::"s"(lds), "v"(voff), "s"(rs), "s"(soff) : "memory");
}
DEV void dma16x4(u32 lds, u32 v0, u32 v1, u32 v2, u32 v3, rsrc_t rs, u32 soff) {
  asm volatile(DMA_HEAD "buffer_load_dwordx4 %1, %5, %6 offen lds\n\tbuffer_load_dwordx4 %2, %5, %6 offen offset:1024 lds\n\t"
               "buffer_load_dwordx4 %3, %5, %6 offen offset:2048 lds\n\tbuffer_load_dwordx4 %4, %5, %6 offen offset:3072 lds"
               ::"s"(lds), "v"(v0), "v"(v1), "v"(v2), "v"(v3), "s"(rs), "s"(soff) : "memory");
}
DEV void dma16x2(u32 lds, u32 v0, u32 v1, rsrc_t rs, u32 soff) {
  asm volatile(DMA_HEAD "buffer_load_dwordx4 %1, %3, %4 offen lds\n\tbuffer_load_dwordx4 %2, %3, %4 offen offset:1024 lds"
               ::"s"(lds), "v"(v0), "v"(v1), "s"(rs), "s"(soff) : "memory");
}

struct Frags { s16x8 x[6]; s16x8 w[6]; };

DEV s16x8 tr_frag(LAS char* p, LAS char* q, u32 o0, u32 o1) {
  s16x4 a = __builtin_amdgcn_ds_read_tr16_b64_v4i16((LAS s16x4*)(p + o0));
  s16x4 b = __builtin_amdgcn_ds_read_tr16_b64_v4i16((LAS s16x4*)(q + o1));
  return __builtin_shufflevector(a, b, 0, 1, 2, 3, 4, 5, 6, 7);
}
template <int KK> DEV void load_frags(Frags& f, LAS char* aH, LAS char* aT, LAS char* w0, LAS char* w1) {
#pragma unroll
  for (int t = 0; t < 5; ++t) f.x[1 + t] = *(LAS s16x8*)(aT + t * 2048);
  f.x[0] = *(LAS s16x8*)aH;
#pragma unroll
  for (int sec = 0; sec < 3; ++sec) {
    f.w[2 * sec] = tr_frag(w0, w0, sec * 8192 + KK * 4096, sec * 8192 + KK * 4096 + 512);
    f.w[2 * sec + 1] = tr_frag(w1, w1, sec * 8192 + KK * 4096, sec * 8192 + KK * 4096 + 512);
  }
}
template <int A, int B> DEV void mma_range(f32x4 (&acc)[6][6], const Frags& f, s16x8 e0) {
#pragma unroll
  for (int n = A; n < B; ++n) {
    const int p = n / 11, r = n % 11, w = 2 * p + (r >= 6), t = (r >= 6) ? 11 - r : 5 - r;
    acc[w][t] = (w < 4) ? mfma(f.w[w], (w == 0 && t == 0) ? e0 : f.x[t], acc[w][t]) : mfma(f.x[t], f.w[w], acc[w][t]);
  }
}
DEV void interleave() {
#pragma unroll
  for (int k = 0; k < 6; ++k) { __builtin_amdgcn_sched_group_barrier(0x008, 1, 0); __builtin_amdgcn_sched_group_barrier(0x100, 1, 0); }
#pragma unroll
  for (int k = 0; k < 6; ++k) {
    __builtin_amdgcn_sched_group_barrier(0x008, 1, 0); __builtin_amdgcn_sched_group_barrier(0x100, 1, 0);
    __builtin_amdgcn_sched_group_barrier(0x008, 1, 0); __builtin_amdgcn_sched_group_barrier(0x100, 1, 0);
    __builtin_amdgcn_sched_group_barrier(0x008, 1, 0);
  }
}

template <int E> DEV s16x8 col_frag(const u32x4* L) {
  constexpr u32 sel = (E & 1) ? 0x07060302u : 0x05040100u;
  u32x4 r;
  r.x = __builtin_amdgcn_perm(L[1][E >> 1], L[0][E >> 1], sel);
  r.y = __builtin_amdgcn_perm(L[3][E >> 1], L[2][E >> 1], sel);
  r.z = __builtin_amdgcn_perm(L[5][E >> 1], L[4][E >> 1], sel);
  r.w = __builtin_amdgcn_perm(L[7][E >> 1], L[6][E >> 1], sel);
  return as_frag(r);
}
DEV void mma_cols(f32x4* acc, const u32x4* L, s16x8 b) {
  acc[0] = mfma(col_frag<0>(L), b, acc[0]); acc[1] = mfma(col_frag<1>(L), b, acc[1]);
  acc[2] = mfma(col_frag<2>(L), b, acc[2]); acc[3] = mfma(col_frag<3>(L), b, acc[3]);
  acc[4] = mfma(col_frag<4>(L), b, acc[4]); acc[5] = mfma(col_frag<5>(L), b, acc[5]);
  acc[6] = mfma(col_frag<6>(L), b, acc[6]); acc[7] = mfma(col_frag<7>(L), b, acc[7]);
}

DEV void transpose_grp(u32x4* L) {
  const s16x8 f0 = col_frag<0>(L), f1 = col_frag<1>(L), f2 = col_frag<2>(L), f3 = col_frag<3>(L), f4 = col_frag<4>(L), f5 = col_frag<5>(L), f6 = col_frag<6>(L), f7 = col_frag<7>(L);
  L[0] = __builtin_bit_cast(u32x4, f0); L[1] = __builtin_bit_cast(u32x4, f1); L[2] = __builtin_bit_cast(u32x4, f2); L[3] = __builtin_bit_cast(u32x4, f3);
  L[4] = __builtin_bit_cast(u32x4, f4); L[5] = __builtin_bit_cast(u32x4, f5); L[6] = __builtin_bit_cast(u32x4, f6); L[7] = __builtin_bit_cast(u32x4, f7);
#pragma unroll
  for (int e = 0; e < 8; ++e) PIN(L[e]);
  SB();
}
DEV void mma_frags8(f32x4* acc, const u32x4* L, s16x8 b) {
#pragma unroll
  for (int e = 0; e < 8; ++e) acc[e] = mfma(as_frag(L[e]), b, acc[e]);
}
DEV void warm_line(u32 lds, const char* a) {
  asm volatile(DMA_HEAD "global_load_lds_dword %1, off" ::"s"(lds), "v"(a) : "memory");
}

__global__ void __launch_bounds__(512, 2) fused_kernel(Args Parg) {
  extern __shared__ __attribute__((aligned(128))) char smem[];
  u32x16 ka;
  u64 kb;
  asm volatile("s_load_dwordx16 %0, %2, 0x0\n\ts_load_dwordx2 %1, %2, 0x40" : "=&s"(ka), "=&s"(kb) : "s"(__builtin_amdgcn_kernarg_segment_ptr()));
  Params P;
  const char* pc0;
  asm volatile("s_getpc_b64 %0" : "=s"(pc0));
  LAS char* L = (LAS char*)smem;
  const u32 lbase = (u32)(uintptr_t)L;
  const int lane = threadIdx.x & 63;
  const int wave = rfl(threadIdx.x >> 6);
  const int bid = blockIdx.x, xg = bid & 7, loc = bid >> 3;
  const int np = 7 - (xg & 1);
  const bool gemm = loc < 4 * np;
  const int pl = loc >> 2, hl = loc & 3;
  const int pan = (xg & 1) * 7 + pl, hp = (xg >> 1) * 4 + hl, r0 = pan * 160;
  const int fi = loc - 4 * np;
  const bool dwn = (u32)(fi - 2 - 3 * (xg & 1)) <= 2u * (xg & 1);
  const int fu = (xg & 1) * 12 + (xg >> 1) * (3 + 2 * (xg & 1)) + min(fi, 2 + 2 * (xg & 1));
  const int du = (xg & 1) ? 4 + (xg >> 1) * 3 + fi - 5 : (xg >> 1);
  const int pan2 = (xg & 1) * 8 + (xg >> 1) * 2 + (loc >> 4), cg = loc & 15, hrot = 2 * cg;
  const int g = lane >> 4, i = lane & 15;
  u64 kord;
  asm volatile("s_load_dwordx2 %0, %1, %2" : "=&s"(kord) : "s"(__builtin_amdgcn_kernarg_segment_ptr()), "s"(72 + (((xg & 1) * 32 + loc) & 63) * 8));
  rsrc_t rsWS;
  const u64 tstart = __builtin_amdgcn_s_memrealtime();
  u32 tag = 0;
  u64 cyc0 = 0;
  f32x4 oc[2][4];
  auto oc_init = [&]() {
    const int tl = (wave >> 1) + 64 * (wave & 1) + 2 * i;
    f32x2 st[2];
    u32x2 v[2][4];
#pragma unroll
    for (int tt = 0; tt < 2; ++tt) {
      st[tt] = *(LAS f32x2*)(L + L_STO + tl * 8 + tt * 256);
#pragma unroll
      for (int c = 0; c < 4; ++c) v[tt][c] = *(LAS u32x2*)(L + L_XT + tl * 128 + (((2 * c + (g >> 1)) ^ (i & 7)) << 4) + (g & 1) * 8 + tt * 4096);
    }
    SB();
#pragma unroll
    for (int tt = 0; tt < 2; ++tt) {
      const float k1 = 1.f + st[tt].y, k0 = -st[tt].x * st[tt].y;
#pragma unroll
      for (int c = 0; c < 4; ++c) oc[tt][c] = f32x4{bf_lo(v[tt][c].x), bf_hi(v[tt][c].x), bf_lo(v[tt][c].y), bf_hi(v[tt][c].y)} * k1 + k0;
    }
#pragma unroll
    for (int q = 0; q < 8; ++q) PIN(oc[q >> 2][q & 3]);
  };

  u32 tq[3], vO, vx;
  auto helper_offsets = [&]() {
    u32 lane = threadIdx.x & 63;
    PIN(lane);
    const int lr = lane >> 3, lc = lane & 7;
#pragma unroll
    for (int j = 0; j < 3; ++j) {
      const int rho = min(64 * j + (int)lane, 175), pr = rho >= 96;
      const int tk = r0 + ((rho < 16) ? rho - 16 : pr + 2 * (rho - 16 - 80 * pr));
      const u32 a = (u32)(pan2 * 128 + (rho & 127)) * 8, b = (((u32)tk >= (u32)S_) | (rho == 0)) ? 1u : (u32)tk * 8;
      tq[j] = (wave == 7) ? a : b;
      PIN(tq[j]);
    }
    vO = lr * 2048 + ((u32)(lc ^ (((lr >> 1) & 3) << 1)) << 4) + cg * 128;
    vx = lr * 2048 + ((u32)(lc ^ (lr >> 1)) << 4) + cg * 128;
    PIN(vO); PIN(vx);
  };
  auto wo_grp = [&](int q) { dma16x4(lbase + q * 4096, vO, vO + 15 * 1024, vO + 30 * 1024, vO + 45 * 1024, mk_rsrc(P.wo, D_ * D_ * 2), ((q + hrot) & 31) * 65536); };
  auto helper_fill = [&]() {
    const int lid = wave - 4;
    tag = (u32)rfl((int)lds_rd(L, L_TAG));
    if (gemm) { VMCNT(11); BAR(); }
    if (lid >= 2) {
      u32x2 sv[3];
#pragma unroll
      for (int q = 2 * lid - 4; q < 2 * lid - 2; ++q) dma16x4(lbase + L_XT + q * 4096, vx, (vx ^ 64) + 15 * 1024, vx + 30 * 1024, (vx ^ 64) + 45 * 1024, mk_rsrc(P.x, S_ * D_ * 2), (pan2 * 128 + 32 * q) * 2048);
#pragma unroll
      for (int j = 0; j < 3; ++j) sv[j] = ld_sc1_64(rsWS, tq[j] & ~7u, WS_SG);
      SB();
      if (gemm) { VMCNT(11); BAR(); }
      _Pragma("nounroll") for (int sp = 0; sp < SPIN_CAP; ++sp) {
        if (__all(sv[0].y == tag && sv[1].y == tag && sv[2].y == tag)) break;
        __builtin_amdgcn_s_sleep(2);
#pragma unroll
        for (int j = 0; j < 3; ++j) sv[j] = ld_sc1_64(rsWS, tq[j] & ~7u, WS_SG);
      }
      f32x2 st[3];
      u32 ta = lbase + ((lid == 3) ? L_STO + lane * 8 : L_STQ + lane * 4), tb2 = ta + ((lid == 3) ? 4 : 704);
      u32 tc = (lid == 2 && lane < 48) ? ta + 512 : lbase + L_UPD + lane * 4, td = (lid == 2 && lane < 48) ? tb2 + 512 : tc;
      const u32 ts = (lid == 3) ? 512 : 256;
#pragma unroll
      for (int j = 0; j < 3; ++j) {
        st[j] = f32x2{__uint_as_float((sv[j].x << 16) ^ ((lid == 3) ? 0u : 0x80000000u)), __builtin_amdgcn_rcpf(bf_hi(sv[j].x))};
        if (tq[j] & 1u) st[j] = f32x2{0.f, 0.f};
        PIN(st[j]);
      }
      u32 te = ta + ts, tf = tb2 + ts;
      PIN(ta); PIN(tb2); PIN(tc); PIN(td); PIN(te); PIN(tf);
      VMCNT(0);
      if (gemm) BAR();
      *(LAS float*)LP(ta) = st[0].x; *(LAS float*)LP(tb2) = st[0].y;
      *(LAS float*)LP(te) = st[1].x; *(LAS float*)LP(tf) = st[1].y;
      *(LAS float*)LP(tc) = st[2].x; *(LAS float*)LP(td) = st[2].y;
      LGKM0();
    } else {
      if (gemm) { VMCNT(0); BAR(); }
      wo_grp(12 + lid); wo_grp(14 + lid);
#pragma unroll
      for (int q = 0; q < 4; ++q) wo_grp(16 + 2 * q + lid);
      if (gemm) BAR();
    }
    if (gemm) BAR();
    int l2 = lid;
    asm volatile("" : "+s"(l2));
    wo_grp(l2); wo_grp(4 + l2); wo_grp(8 + l2);
    if (!dwn) { wo_grp(24 + l2); wo_grp(28 + l2); }
    if (lid == 2) {
      u32x2 sk;
      _Pragma("nounroll") for (int sp = 0; sp < SPIN_CAP; ++sp) {
        sk = ld_sc1_64(rsWS, (lane & 7) * 8, WS_SEL);
        if (__all(sk.y == tag)) break;
        __builtin_amdgcn_s_sleep(8);
      }
      if (lane < 8) lds_wr(L, L_SEL + 4 * lane, sk.x);
      LGKM0();
    }
  };
  {
    u32 ln = threadIdx.x & 63;
    PIN(ln);
    if (ln < 2) warm_line(lbase + L_UPD, pc0 + min((loc * 8 + wave) * 2 + (int)ln, CODE_LINES - 1) * 128);
  }
  const int lid = wave - 4;
  const bool isx = lid < 2;
  u32 v0 = 0, v1 = 0, v2 = 0, v3 = 0, vh = 0, g0 = 0, g1 = 0, g2 = 0, kstep = 0, d0 = 0;
  {
    int wave_a = wave;
    asm volatile("" : "+s"(wave_a));
    if (gemm && wave_a >= 4) {
      u32 ln = threadIdx.x & 63;
      PIN(ln);
      const int lr = ln >> 3, lc = ln & 7;
      const u32 sw = (u32)(lc ^ (isx ? lr >> 1 : lr & 2)) << 4;
      const u32 vb = lr * (isx ? 4096 : 6144) + sw + (isx ? (r0 + lid) * 2048 : hp * 128);
      const u32 pp = (isx ? 32768 : 49152) - 1024;
      v0 = vb; v1 = (vb ^ 64) + pp; v2 = vb + 2 * pp; v3 = (vb ^ 64) + 3 * pp;
      g0 = (lid == 3) ? 198656 : 0; g1 = isx ? 131072 : (lid == 3) ? 4096 : 196608; g2 = isx ? 262144 : (lid == 3) ? 200704 : 2048;
      vh = max(r0 - 16 + 8 * lid + lr, 0) * 2048 + (sw ^ (lid << 6));
      kstep = isx ? 128u : 64u * 6144u; d0 = lbase + (isx ? 2 + 10 * lid : 22 + 12 * (lid - 2)) * 1024;
      PIN(v0); PIN(v1); PIN(v2); PIN(v3); PIN(vh);
      asm volatile("" : "+s"(g0), "+s"(g1), "+s"(g2), "+s"(kstep), "+s"(d0));
    }
  }
  u32 ev;
  asm volatile("s_waitcnt lgkmcnt(0)" : "+s"(ka), "+s"(kb), "+s"(kord));
  {
    auto ptr = [&](int k) { return (char*)(GAS char*)(((u64)ka[2 * k + 1] << 32) | ka[2 * k]); };
    P.x = (const u16*)ptr(0); P.wqkv = (const u16*)ptr(1); P.wo = (const u16*)ptr(2); P.wr = (const u16*)ptr(3);
    P.wg = (const u16*)ptr(4); P.wu = (const u16*)ptr(5); P.wd = (const u16*)ptr(6); P.out = (u16*)ptr(7);
    P.ws = (char*)(GAS char*)kb;
  }
  rsWS = mk_rsrc(P.ws, WS_END);
  STAMP(0);
  if (wave < 4) {
    asm volatile("s_load_dword %0, %1, 0x0" : "=s"(ev) : "s"(P.ws + WS_EPOCH) : "memory");
    u32 ln = threadIdx.x & 63;
    PIN(ln);
    const u32 d = lbase + 2 * SLOT + wave * 6144, v = ln * 16;
    dma16x4(d, v, v, v, v, mk_rsrc(P.x, S_ * D_ * 2), (pan2 * 128 + cg * 8 + wave * 2) * 2048);
    dma16x2(d + 4096, v, v, mk_rsrc(P.wr, D_ * 2), 0);
  }
  SB();
  int wave_b = wave;
  asm volatile("" : "+s"(wave_b));
  if (gemm && wave_b >= 4) {
    const rsrc_t rs = mk_rsrc(isx ? P.x : P.wqkv, isx ? S_ * D_ * 2 : D_ * 3 * D_ * 2);
    auto issue_k = [&](u32 ko, u32 so) {
      dma16x4(d0 + so, v0, v1, v2, v3, rs, ko + g0);
      dma16x4(d0 + so + 4096, v0, v1, v2, v3, rs, ko + g1);
      if (isx) { dma16x2(d0 + so + 8192, v0, v1, rs, ko + g2); dma16(lbase + so + lid * 1024, vh, rs, ko); }
      else dma16x4(d0 + so + 8192, v0, v1, v2, v3, rs, ko + g2);
    };
    auto issue = [&](int s, u32 so) { issue_k(((u32)(kord >> (4 * s)) & 15u) * kstep, so); };
    LSTAMP(20);
    issue(0, 0);
    LSTAMP(21);
    helper_offsets();
    issue(1, SLOT);
    LSTAMP(22);
    if (isx) VMCNT(11); else VMCNT(12);
    LSTAMP(23);
    BAR();
    const bool helps = !isx && !(xg & 2) && hl != 3;
    if (helps) {
      constexpr int HL = (HELPN * 192 + 35) / 36;
      const int n = min(((pl * 3 + hl) * 2 + lid - 2) * HL + min(lane, HL - 1), HELPN * 192 - 1), row = n / 3;
      warm_line(lbase + L_UPD, (const char*)P.wqkv + (size_t)((u32)((HELPK >> (4 * (row >> 6))) & 15) * 64 + (row & 63)) * 6144 + (n % 3) * 2048 + (hp | 3) * 128);
    }
    issue(2, 2 * SLOT);
    u32 so = 0, sdum = 0;
    const int sn = min((pl * 4 + hl) * 4 + lid, 95) * 2;
    const u32 so0 = (sn / 3) * 6144 + (sn % 3) * 2048, so1 = ((sn + 1) / 3) * 6144 + ((sn + 1) % 3) * 2048;
    const char* sbase = (const char*)P.wqkv + (hp | 3) * 128;
    _Pragma("nounroll") for (int s = 0; s < 13; ++s) {
      if (helps && s == 0) VMCNT(13); else if (!isx || s == WARM_AT + 1 || s == WARM_AT + 2) VMCNT(12); else VMCNT(11);
      if (s == 0) LSTAMP(24);
      if (s == 1) LSTAMP(25);
      if (s == 2) LSTAMP(26);
      BAR();
      issue(s + 3, so);
      if (!(xg & 2) && (u32)(s - RS0) < RSN) {
        const u32 ko = (u32)((ROLLK >> (4 * (s - RS0))) & 15) * (64u * 6144u), a0 = ko + so0, a1 = ko + so1;
        asm volatile("s_load_dword %0, %1, %2\n\ts_load_dword %0, %1, %2 offset:64\n\ts_load_dword %0, %1, %3\n\ts_load_dword %0, %1, %3 offset:64"
                     : "+s"(sdum) : "s"(sbase), "s"(a0), "s"(a1) : "memory");
      }
      if (s == WARM_AT && isx) warm_line(lbase + L_UPD, (const char*)P.wo + (size_t)min((pan * 16 + hp) * 80 + lid * 40 + min(lane, 39), 16383) * 128);
      so = (so == 2 * SLOT) ? 0 : so + SLOT;
    }
    asm volatile("s_waitcnt lgkmcnt(0)" : "+s"(sdum));
    STAMP(2);
    helper_fill();
  }
  if (wave < 4) {
    __builtin_amdgcn_s_setprio(1);
    if (wave == 1) lds_wr(L, L_ONE + lane * 4, lane < 32 ? 0x3f803f80u : 0u);
    if (wave == 2) lds_wr(L, L_GO, 0u);
    f32x4 acc[6][6];
#pragma unroll
    for (int w = 0; w < 6; ++w)
#pragma unroll
      for (int t = (w & 1); t < 6; ++t) { acc[w][t] = f32x4{0.f, 0.f, 0.f, 0.f}; PIN(acc[w][t]); }
    f32x4 bprev, bhalo;
    u32 mc[2];
    u64 cur[4];
#pragma unroll
    for (int r = 0; r < 4; ++r) {
      const int kap = 4 * g + r, d = 16 + (wave & 1) + 2 * i - kap;
      bprev[r] = (kap - i >= 9) ? 0.f : -1e30f;
      bhalo[r] = (((d & 1) == 0) & (d <= 14) & (pan > 0)) ? 0.f : -1e30f;
      cur[r] = __ballot((u32)(i - kap) <= 7u);
      asm volatile("" : "+s"(cur[r]));
    }
    mc[0] = (((u32)(i - 4 * g) <= 7u) ? 0xffffu : 0u) | (((u32)(i - 4 * g - 1) <= 7u) ? 0xffff0000u : 0u);
    mc[1] = (((u32)(i - 4 * g - 2) <= 7u) ? 0xffffu : 0u) | (((u32)(i - 4 * g - 3) <= 7u) ? 0xffff0000u : 0u);
    u32 hxl = lbase + L_HX + (wave & 2) * 2304 + lane * 16;
    u32 obase = WS_O + (((wave & 1) * 32 + hp * 2 + (wave >> 1)) * 64 + pan * 5) * 1024 + lane * 16;
    PIN(bprev); PIN(bhalo); PIN(mc[0]); PIN(mc[1]); PIN(hxl); PIN(obase);
    SB();
    {
      const int tok = pan2 * 128 + cg * 8 + wave * 2 + (lane >> 5);
      u32x4 xv[4], wv[4];
      VMCNT(0);
      {
        LAS char* sp = L + 2 * SLOT + wave * 6144 + (lane & 31) * 16;
#pragma unroll
        for (int j = 0; j < 4; ++j) { xv[j] = *(LAS u32x4*)(sp + (lane >> 5) * 2048 + j * 512); wv[j] = *(LAS u32x4*)(sp + 4096 + j * 512); }
      }
      asm volatile("s_waitcnt lgkmcnt(0)" : "+s"(ev) :: "memory");
      const u32 e = ev;
      tag = e * 0x9E3779B1u + 0x7F4A7C15u; tag ^= tag >> 15; tag = (u32)rfl((int)(tag ^ (u32)(tag == e)));
      if (wave == 1) lds_wr(L, L_TAG, tag);
      float sx = 0.f, sxx = 0.f, sxw = 0.f, sw = 0.f;
#pragma unroll
      for (int j = 0; j < 4; ++j)
#pragma unroll
        for (int e2 = 0; e2 < 4; ++e2) {
          const u32 a = xv[j][e2], b = wv[j][e2];
          sx = dot2(a, 0x3f803f80u, sx); sxx = dot2(a, a, sxx); sxw = dot2(a, b, sxw); sw = dot2(b, 0x3f803f80u, sw);
        }
      const float mean = half_sum(sx) * (1.f / D_);
      const float var = fmaxf(half_sum(sxx) * (1.f / D_) - mean * mean, 0.f);
      const float mb = rbf(mean), sb = rbf(__builtin_amdgcn_sqrtf(rbf(rbf(var) + 1e-5f)));
      const u32 ro = ord16(pack2((half_sum(sxw) - mb * half_sum(sw)) * __builtin_amdgcn_rcpf(sb), 0.f) & 0xffffu);
      const u32 rlo = (u32)__builtin_amdgcn_readlane((int)ro, 31);
      if ((lane & 31) == 31) st_sc1_64(u32x2{pack2(mb, sb), tag}, rsWS, WS_SG + tok * 8);
      if (lane == 63) st_sc1_64(u32x2{rlo | (ro << 16), tag}, rsWS, WS_RG + (tok >> 1) * 8);
      if (!gemm) { LGKM0(); BAR(); }
    }
    STAMP(1);
    if (gemm) {
      const int h = wave >> 1, par = wave & 1;
        const int q4 = i >> 2, p4 = i & 3;
        u32 a0 = lbase + i * 128 + ((g ^ (i >> 1)) << 4);
        const u32 wsw = ((q4 >> 1) & 1) | ((g & 1) << 1);
        u32 w0 = lbase + WOFF + (8 * g + q4) * 128 + (((2 * h + par) ^ wsw) << 5) + p4 * 8;
        const u32 tb = (1 + 5 * par) * 2048;
        const u32 one = lbase + L_ONE;
        u32 pH = (i == 0) ? one : a0, pT = a0 + tb, pW0 = w0, pW1 = w0 ^ 32;
        PIN(a0); PIN(w0); PIN(pH); PIN(pT); PIN(pW0); PIN(pW1);
        Frags f0, f1;
        const u32 e1 = (i == 0) ? 0x3f803f80u : 0u;
        s16x8 e0 = as_frag(u32x4{e1, e1, e1, e1});
        PIN(e0);
        __builtin_amdgcn_s_waitcnt(0xc07f);
        BAR();
        STAMP(2); CSTAMP(14);
        load_frags<0>(f0, LP(pH), LP(pT), LP(pW0), LP(pW1));
        pH ^= 64; pT ^= 64;
        __builtin_amdgcn_s_waitcnt(0xc07f);
        u32 so = 0, trc = 0;
        auto half1 = [&]() {
          SB();
          load_frags<1>(f1, LP(pH), LP(pT), LP(pW0), LP(pW1));
          mma_range<0, 24>(acc, f0, e0);
          interleave();
          SB();
          so = (so == 2 * SLOT) ? 0 : so + SLOT;
          mma_range<24, 25>(acc, f0, e0);
          SB();
          pT = so + a0; pW0 = so + w0; PIN(pT); PIN(pW0);
          mma_range<25, 26>(acc, f0, e0);
          SB();
          pH = (i == 0) ? one : pT; pW1 = pW0 ^ 32; PIN(pH); PIN(pW1);
          mma_range<26, 27>(acc, f0, e0);
          SB();
          pT += tb; PIN(pT);
          mma_range<27, 28>(acc, f0, e0);
          SB();
        };
        auto half1b = [&](int s) {
          const u64 tnow = TICK(), rnow = RT();
          mma_range<28, 30>(acc, f0, e0);
          SB();
          __builtin_amdgcn_s_waitcnt(0xc07f);
          if (STAMPS) { trc = (lane == s) ? (u32)tnow : trc; trc = (lane == s + 16) ? (u32)rnow : trc; }
          mma_range<30, 31>(acc, f0, e0);
          SB();
          BAR();
          SB();
          mma_range<31, 33>(acc, f0, e0);
          SB();
        };
        half1();
        for (int s = 0; s < 15; ++s) {
          half1b(s);
          load_frags<0>(f0, LP(pH), LP(pT), LP(pW0), LP(pW1));
          mma_range<0, 24>(acc, f1, e0);
          interleave();
          SB();
          pH ^= 64; pT ^= 64; PIN(pH); PIN(pT);
          mma_range<24, 31>(acc, f1, e0);
          SB();
          __builtin_amdgcn_s_waitcnt(0xc07f);
          mma_range<31, 33>(acc, f1, e0);
          half1();
        }
        half1b(15);
        if (STAMPS && lane < 32) ((GAS u32*)(P.ws + WS_STAMP + (1 << 20)))[(bid * 4 + wave) * 32 + lane] = trc;
      mma_range<5, 6>(acc, f1, e0); mma_range<16, 17>(acc, f1, e0); mma_range<27, 28>(acc, f1, e0);
      SB();
      mma_range<11, 16>(acc, f1, e0);
      u32 tz = threadIdx.x;
      PIN(tz);
      const u32 i = tz & 15, g = (tz >> 4) & 3;
      u32 sqi = lbase + L_STQ + i * 4, sqg = lbase + L_STQ + g * 16;
      PIN(sqi); PIN(sqg);
      const u32 hx = hxl + par * 2304, hy = hxl + (par ^ 1) * 2304;
      u32 hxg = hx - i * 16, hyg = hy - i * 16, hxi = hx - g * 256, hyi = hy - g * 256, sqi2 = sqi + par * 320, sqg2 = sqg + par * 320;
      const u32 hqa = hx + 2048 - g * 240, hqb = hxl + (L_UPD - L_HX) - (wave & 2) * 2304;
      u32 hq = __builtin_amdgcn_inverse_ballot_w64(0x0001000100010001ull) ? hqa : hqb;
      PIN(hxg); PIN(hyg); PIN(hxi); PIN(hyi); PIN(sqi2); PIN(sqg2); PIN(hq);
#pragma unroll
      for (int k = 0; k < 5; ++k) { __builtin_amdgcn_sched_group_barrier(0x008, 1, 0); __builtin_amdgcn_sched_group_barrier(0x002, 2, 0); }
      SB();
      *(LAS f32x4*)LP(hx) = acc[2][0];
      *(LAS f32x4*)LP(hx + 1024) = acc[4][0];
      *(LAS f32x4*)LP(hq) = acc[0][0];
      mma_range<17, 22>(acc, f1, e0);
#pragma unroll
      for (int k = 0; k < 3; ++k) { __builtin_amdgcn_sched_group_barrier(0x008, 1, 0); __builtin_amdgcn_sched_group_barrier(0x200, 1, 0); }
      SB();
      mma_range<0, 3>(acc, f1, e0);
      SB();
      __builtin_amdgcn_s_waitcnt(0xc07f);
      BAR();
      SB();
      f32x4 cs[4];
      cs[2] = *(LAS f32x4*)LP(hxg); cs[3] = *(LAS f32x4*)LP(hyg);
      cs[0] = *(LAS f32x4*)LP(hxg + 2048 - g * 240); cs[1] = *(LAS f32x4*)LP(hyg + 2048 - g * 240);
      float m[6], rs[6];
#pragma unroll
      for (int t = 0; t < 6; ++t) { m[t] = *(LAS float*)LP((t ? sqi2 : sqi) + t * 64); rs[t] = *(LAS float*)LP((t ? sqi2 : sqi) + t * 64 + 704); }
      acc[3][0] = *(LAS f32x4*)LP(hy);
      const float cv[2] = {*(LAS float*)LP(hxi + 1024), *(LAS float*)LP(hyi + 1024)};
      acc[5][0] = *(LAS f32x4*)LP(hy + 1024);
      mma_range<3, 5>(acc, f1, e0); mma_range<6, 11>(acc, f1, e0);
#pragma unroll
      for (int k = 0; k < 7; ++k) { __builtin_amdgcn_sched_group_barrier(0x008, 1, 0); __builtin_amdgcn_sched_group_barrier(0x100, 2, 0); }
      SB();
      STAMP(3); CSTAMP(15);
      const float QS = 0.17677669529663687f * 1.4426950408889634f;
      const f32x4 z4 = {0.f, 0.f, 0.f, 0.f};
      f32x4 s0[5], s1[5];
      {
        s16x8 kf[6];
#pragma unroll
        for (int t = 5; t >= 0; --t) {
          const f32x4 k0 = (acc[2][t] + m[t] * cs[2]) * rs[t], k1 = (acc[3][t] + m[t] * cs[3]) * rs[t];
          kf[t] = as_frag(u32x4{pack2(k0[0], k0[1]), pack2(k0[2], k0[3]), pack2(k1[0], k1[1]), pack2(k1[2], k1[3])});
        }
        mma_range<22, 27>(acc, f1, e0); mma_range<28, 33>(acc, f1, e0);
#pragma unroll
        for (int k = 0; k < 10; ++k) { __builtin_amdgcn_sched_group_barrier(0x008, 1, 0); __builtin_amdgcn_sched_group_barrier(0x002, 3, 0); }
        SB();
#pragma unroll
        for (int t = 0; t < 5; ++t) {
          const float rq = rs[t + 1] * QS;
          const f32x4 q0 = (acc[0][t + 1] + m[t + 1] * cs[0]) * rq, q1 = (acc[1][t + 1] + m[t + 1] * cs[1]) * rq;
          const s16x8 qf = as_frag(u32x4{pack2(q0[0], q0[1]), pack2(q0[2], q0[3]), pack2(q1[0], q1[1]), pack2(q1[2], q1[3])});
          s0[t] = mfma(kf[t], qf, t == 0 ? bhalo : bprev); s1[t] = mfma(kf[t + 1], qf, z4);
        }
      }
      SB();
      u32x16 vp[2];
#pragma unroll
      for (int t = 0; t < 6; ++t) {
        const f32x4 m4 = *(LAS f32x4*)LP((t ? sqg2 : sqg) + t * 64), r4 = *(LAS f32x4*)LP((t ? sqg2 : sqg) + t * 64 + 704);
#pragma unroll
        for (int c = 0; c < 2; ++c) {
          const f32x4 v = (acc[4 + c][t] + m4 * cv[c]) * r4;
          vp[c][2 * t] = pack2(v[0], v[1]); vp[c][2 * t + 1] = pack2(v[2], v[3]);
        }
      }
      float l[5][4], mx[5];
#pragma unroll
      for (int t = 0; t < 5; ++t) {
#pragma unroll
        for (int r = 0; r < 4; ++r) l[t][r] = __builtin_amdgcn_inverse_ballot_w64(cur[r]) ? s1[t][r] : s0[t][r];
        mx[t] = vmax3(vmax(l[t][0], l[t][1]), l[t][2], l[t][3]);
      }
#pragma unroll
      for (int t = 0; t < 5; ++t) mx[t] = max16(mx[t]);
#pragma unroll
      for (int t = 0; t < 5; ++t) mx[t] = max32(mx[t]);
      const s16x8 ones = as_frag(u32x4{0x3f803f80u, 0x3f803f80u, 0x3f803f80u, 0x3f803f80u});
      f32x4 sm[5], o0[5], o1[5];
#pragma unroll
      for (int t = 0; t < 5; ++t) {
#pragma unroll
        for (int r = 0; r < 4; ++r) l[t][r] = __builtin_amdgcn_exp2f(l[t][r] - mx[t]);
        const u32 p01 = pack2(l[t][0], l[t][1]), p23 = pack2(l[t][2], l[t][3]);
        const s16x8 pp = as_frag(u32x4{p01 & ~mc[0], p23 & ~mc[1], p01 & mc[0], p23 & mc[1]});
        sm[t] = mfma(ones, pp, z4);
        o0[t] = mfma(as_frag(u32x4{vp[0][2 * t], vp[0][2 * t + 1], vp[0][2 * t + 2], vp[0][2 * t + 3]}), pp, z4);
        o1[t] = mfma(as_frag(u32x4{vp[1][2 * t], vp[1][2 * t + 1], vp[1][2 * t + 2], vp[1][2 * t + 3]}), pp, z4);
      }
#pragma unroll
      for (int t = 0; t < 5; ++t) {
        const float inv = __builtin_amdgcn_rcpf(sm[t][0]);
        o0[t] *= inv; o1[t] *= inv;
        st_sc1_128(u32x4{pack2(o0[t][0], o0[t][1]), pack2(o0[t][2], o0[t][3]), pack2(o1[t][0], o1[t][1]), pack2(o1[t][2], o1[t][3])}, rsWS,
                   (t == 4) ? obase + ((pan == 12) ? 0x40000000u : 4096u) : obase + t * 1024);
      }
      STAMP(8);
    }
  }
  if (!gemm) {
  if (dwn) {
    const rsrc_t rsD = mk_rsrc(P.wd, F_ * D_ * 2);
    const int k0 = (du >> 1) * 14 + (wave >> 2) * 7;
    const u32 vd = ((k0 * 32 + 8 * g) * D_ + (du & 1) * 512 + (wave & 3) * 128 + 8 * i) * 2;
    constexpr u32 D_W = 24 * 4096, D_H = D_W + 8 * 4352;
    u32x4 wb[56];
#pragma unroll
    for (int j = 0; j < 8; ++j) wb[j] = ld_nt_128(rsD, vd, j * (D_ * 2));
    SB();
    if (wave >= 4) { BAR(); tag = (u32)rfl((int)lds_rd(L, L_TAG)); }
#pragma unroll
    for (int j = 8; j < 48; ++j) wb[j] = ld_nt_128(rsD, vd, ((j >> 3) * 32 + (j & 7)) * (D_ * 2));
    SB();
    transpose_grp(wb);
#pragma unroll
    for (int e = 0; e < 4; ++e) *(LAS u32x4*)(L + D_W + wave * 4352 + e * 1024 + lane * 16) = wb[e];
    SB();
#pragma unroll
    for (int q = 1; q < 6; ++q) transpose_grp(wb + 8 * q);
#pragma unroll
    for (int j = 48; j < 56; ++j) wb[j] = ld_nt_128(rsD, vd, (192 + (j & 7)) * (D_ * 2));
    SB();
    __builtin_amdgcn_s_setprio(0);
    if (wave >= 4) { helper_offsets(); helper_fill(); }
    transpose_grp(wb + 48);
    VMCNT(0);
    STAMP(12);
    BAR();
    STAMP(11);
    {
      wait_until(tstart, T_H);
      const int qa = 2 * (wave & 3), qb = min(qa + 1, 6);
      u32x4 va, vb;
      _Pragma("nounroll") for (int sp = 0; sp < SPIN_CAP; ++sp) {
        va = ld_sc1_128(rsWS, lane * 16, WS_H + (k0 + qa) * 1024);
        vb = ld_sc1_128(rsWS, lane * 16, WS_H + (k0 + qb) * 1024);
        if (__all(va.y == tag && va.w == tag && vb.y == tag && vb.w == tag)) break;
        __builtin_amdgcn_s_sleep(1);
      }
      *(LAS u32x2*)(L + D_H + ((wave >> 2) * 7 + qa) * 512 + (lane & 31) * 16 + (lane >> 5) * 8) = u32x2{va.x, va.z};
      *(LAS u32x2*)(L + D_H + ((wave >> 2) * 7 + qb) * 512 + (lane & 31) * 16 + (lane >> 5) * 8) = u32x2{vb.x, vb.z};
      LGKM0();
      lds_wr(L, L_HR + wave * 4, 1);
      lds_wait_set(L, L_HR + (wave & 4) * 4 + (lane & 3) * 4);
    }
    STAMP(2);
    f32x4 ga[8];
#pragma unroll
    for (int e = 0; e < 8; ++e) ga[e] = f32x4{0.f, 0.f, 0.f, 0.f};
#pragma unroll
    for (int q = 6; q >= 0; --q) {
      const s16x8 hf = *(LAS s16x8*)(L + D_H + ((wave >> 2) * 7 + q) * 512 + (g * 8 + (i & 7)) * 16);
      mma_frags8(ga, wb + q * 8, hf);
      if (q == 5) {
#pragma unroll
        for (int e = 0; e < 4; ++e) wb[e] = *(LAS u32x4*)(L + D_W + wave * 4352 + e * 1024 + lane * 16);
      }
      if (q >= 5) SB();
    }
    STAMP(4);
    if (i < 8) {
#pragma unroll
      for (int r = 0; r < 4; ++r) {
        const u32 o = D_W + wave * 4352 + (i * 132 + 8 * (4 * g + r)) * 4;
        *(LAS f32x4*)(L + o) = f32x4{ga[0][r], ga[1][r], ga[2][r], ga[3][r]};
        *(LAS f32x4*)(L + o + 16) = f32x4{ga[4][r], ga[5][r], ga[6][r], ga[7][r]};
      }
    }
    LGKM0();
    lds_wr(L, L_PW + 4 * wave, 1);
    if (wave >= 4) {
      lds_wait_set(L, L_PW + (wave - 4) * 4);
#pragma unroll
      for (int tk = 0; tk < 8; ++tk) {
        const f32x2 y = *(LAS f32x2*)(L + D_W + (wave - 4) * 4352 + (tk * 132 + 2 * lane) * 4) + *(LAS f32x2*)(L + D_W + wave * 4352 + (tk * 132 + 2 * lane) * 4);
        st_sc1_128(u32x4{__float_as_uint(y.x), tag, __float_as_uint(y.y), tag}, rsWS, WS_YS + (du >> 1) * 65536 + tk * 8192 + (du & 1) * 4096 + (wave - 4) * 1024 + lane * 16);
      }
      LGKM0();
      lds_wr(L, L_HS + 4 * (wave - 4), 1);
      lds_wait_set(L, L_HS + (lane & 3) * 4);
      u32 ln = threadIdx.x & 63;
      PIN(ln);
      vO = (ln >> 3) * 2048 + (((ln & 7) ^ (((ln >> 4) & 3) << 1)) << 4) + cg * 128;
      wo_grp(20 + wave); wo_grp(24 + wave);
      VMCNT(0);
      lds_wr(L, L_HS + 4 * (wave - 4), 2);
    }
    STAMP(8);
  } else {
    const int lr = lane >> 3, lc = lane & 7;
    const int fv = fu - (fu > 2);
    const bool two = fu != 2 && fv < 24;
    const int la = (fu == 2) ? 50 : (fv >= 24) ? (fv - 24) * 8 + 3 : (fv >= 21) ? (fv - 21) * 16 + 2 : (fv / 3) * 8 + ((fv % 3) ? 2 + 2 * (fv % 3) : 0);
    const int lb = la + ((fv >= 21) ? 8 : 1);
    const rsrc_t rsG = mk_rsrc(P.wg, D_ * F_ * 2), rsU = mk_rsrc(P.wu, D_ * F_ * 2), rsX = mk_rsrc(P.x, S_ * D_ * 2);
    const u32 vg = (i < 8 || two) ? ((wave * 128 + 8 * g) * F_ + ((i < 8) ? la : lb) * 64 + 8 * (i & 7)) * 2 : 0x7f000000u;
    u32x4 wb[8 * GWIN];
    auto ldgrp = [&](int q) {
#pragma unroll
      for (int j = 0; j < 8; ++j) wb[(q % GWIN) * 8 + j] = ld_nt_128((q & 1) ? rsU : rsG, vg, ((q >> 1) * 32 + j) * (F_ * 2));
      SB();
    };
    __builtin_amdgcn_s_setprio(0);
    ldgrp(0);
    ldgrp(1);
    if (wave >= 4) { BAR(); tag = (u32)rfl((int)lds_rd(L, L_TAG)); }
    if (fu == 2) {
      *(LAS u32x4*)(L + 32 + threadIdx.x * 16) = u32x4{0u, 0u, 0u, 0u};
      if (threadIdx.x < 4) *(LAS u32x4*)(L + 32 + 8192 + threadIdx.x * 16) = u32x4{0u, 0u, 0u, 0u};
      wait_until(tstart, T_GATHER);
      u32x4 v;
      _Pragma("nounroll") for (int sp = 0; sp < SPIN_CAP; ++sp) {
        v = ld_sc1_128(rsWS, lane * 16, WS_RG + wave * 1024);
        if (__all(v.y == tag && v.w == tag)) break;
        __builtin_amdgcn_s_sleep(2);
      }
      const u32 ib = 2047 - 256 * wave - 4 * lane;
      const u32 key[4] = {(v.x << 16) | ib, (v.x & 0xffff0000u) | (ib - 1), (v.z << 16) | (ib - 2), (v.z & 0xffff0000u) | (ib - 3)};
      lds_wr(L, wave * 4, wave_max_u32(max(max(key[0], key[1]), max(key[2], key[3]))));
      LGKM0();
      BAR();
      STAMP(9);
      const u32x4 m0 = *(LAS u32x4*)L, m1 = *(LAS u32x4*)(L + 16);
      const u32 thr = min(min(min(m0.x, m0.y), min(m0.z, m0.w)), min(min(m1.x, m1.y), min(m1.z, m1.w)));
      const u32 c0 = key[0] >= thr, c1 = key[1] >= thr, c2 = key[2] >= thr, c3 = key[3] >= thr;
      if (c0 | c1 | c2 | c3) {
        const u32 p = 64 + 4 * __hip_atomic_fetch_add((LAS u32*)(L + 32), c0 + c1 + c2 + c3, __ATOMIC_RELAXED, __HIP_MEMORY_SCOPE_WORKGROUP);
        lds_wr(L, c0 ? p : 36, key[0]); lds_wr(L, c1 ? p + 4 * c0 : 36, key[1]);
        lds_wr(L, c2 ? p + 4 * (c0 + c1) : 36, key[2]); lds_wr(L, c3 ? p + 4 * (c0 + c1 + c2) : 36, key[3]);
      }
      LGKM0();
      BAR();
      const int n = rfl((int)lds_rd(L, 32));
      _Pragma("nounroll") for (int b = threadIdx.x; b < n; b += 512) {
        const u32 my = lds_rd(L, 64 + 4 * b);
        int rank = 0;
        _Pragma("nounroll") for (int t = 0; t < n; t += 8) {
          const u32x4 o = *(LAS u32x4*)(L + 64 + 4 * t), q = *(LAS u32x4*)(L + 80 + 4 * t);
          rank += (o.x > my) + (o.y > my) + (o.z > my) + (o.w > my) + (q.x > my) + (q.y > my) + (q.z > my) + (q.w > my);
        }
        if (rank < 8) st_sc1_64(u32x2{my ^ 0x7ffu, tag}, rsWS, WS_SEL + rank * 8);
      }
      if (threadIdx.x == 0) st_sc1_32(tag, rsWS, WS_EPOCH);
      STAMP(10);
      LGKM0();
      BAR();
    }
    constexpr u32 F_XS = 81920;
    ldgrp(2);
    if (wave == 0) {
      wait_until(tstart, T_SEL);
      u32x2 sk;
      _Pragma("nounroll") for (int sp = 0; sp < SPIN_CAP; ++sp) {
        sk = ld_sc1_64(rsWS, lr * 8, WS_SEL);
        if (__all(sk.y == tag)) break;
        __builtin_amdgcn_s_sleep(4);
      }
      STAMP(2);
      u32x4 xr[16];
#pragma unroll
      for (int j = 0; j < 16; ++j) xr[j] = __builtin_amdgcn_raw_buffer_load_b128(rsX, (sk.x & 0x7ffu) * 2048 + lc * 16, j * 128, 0);
      u32x2 ms;
      _Pragma("nounroll") for (int sp = 0; sp < SPIN_CAP; ++sp) {
        ms = ld_sc1_64(rsWS, (sk.x & 0x7ffu) * 8, WS_SG);
        if (__all(ms.y == tag)) break;
      }
#pragma unroll
      for (int j = 0; j < 16; ++j) *(LAS u32x4*)(L + F_XS + lr * 2064 + j * 128 + lc * 16) = xr[j];
      *(LAS u32*)(L + F_XS + 8 * 2064 + lr * 4) = ms.x;
      LGKM0();
      lds_wr(L, L_HR, 1);
    }
#pragma unroll
    for (int q = 3; q < GWIN; ++q) ldgrp(q);
#pragma unroll
    for (int q = 0; q < GPRE; ++q) transpose_grp(wb + 8 * q);
    lds_wait_set(L, L_HR);
    f32x4 ga[16];
#pragma unroll
    for (int e = 0; e < 16; ++e) ga[e] = f32x4{0.f, 0.f, 0.f, 0.f};
#pragma unroll
    for (int q = 0; q < 8; ++q) {
      const s16x8 xf = *(LAS s16x8*)(L + ((i < 8) ? F_XS + i * 2064 + (wave * 128 + (q >> 1) * 32 + 8 * g) * 2 : L_ONE));
      if (q < GPRE) mma_frags8(ga + 8 * (q & 1), wb + q * 8, xf); else mma_cols(ga + 8 * (q & 1), wb + (q % GWIN) * 8, xf);
      SB();
      if (q + GWIN < 8) ldgrp(q + GWIN);
    }
    STAMP(4);
    if (i < 9) {
#pragma unroll
      for (int m = 0; m < 2; ++m)
#pragma unroll
        for (int r = 0; r < 4; ++r) {
          const u32 o = wave * 9504 + ((m * 9 + i) * 132 + 8 * (4 * g + r)) * 4;
          *(LAS f32x4*)(L + o) = f32x4{ga[8 * m][r], ga[8 * m + 1][r], ga[8 * m + 2][r], ga[8 * m + 3][r]};
          *(LAS f32x4*)(L + o + 16) = f32x4{ga[8 * m + 4][r], ga[8 * m + 5][r], ga[8 * m + 6][r], ga[8 * m + 7][r]};
          SB();
        }
    }
    LGKM0();
    BAR();
    {
      const int fl = (wave >> 1) * 32 + 8 * g + 4 * (wave & 1) + 2 * (lane & 1), tk = (lane >> 1) & 7;
      f32x2 gs = {0.f, 0.f}, us = {0.f, 0.f}, gc = {0.f, 0.f}, uc = {0.f, 0.f};
#pragma unroll
      for (int w = 0; w < 8; ++w) {
        gs += *(LAS f32x2*)(L + w * 9504 + (tk * 132 + fl) * 4);
        gc += *(LAS f32x2*)(L + w * 9504 + (8 * 132 + fl) * 4);
        us += *(LAS f32x2*)(L + w * 9504 + 4752 + (tk * 132 + fl) * 4);
        uc += *(LAS f32x2*)(L + w * 9504 + 4752 + (8 * 132 + fl) * 4);
      }
      const u32 ms = *(LAS u32*)(L + F_XS + 8 * 2064 + tk * 4);
      const float m = bf_lo(ms), rs = __builtin_amdgcn_rcpf(bf_hi(ms));
      gs = (gs - m * gc) * rs; us = (us - m * uc) * rs;
      const float g0 = rbf(gs.x), g1 = rbf(gs.y);
      const u32 hv = pack2(rbf(g0 * sigm(g0)) * rbf(us.x), rbf(g1 * sigm(g1)) * rbf(us.y));
      if (wave < 4 || two) st_sc1_64(u32x2{hv, tag}, rsWS, WS_H + ((wave < 4) ? la : lb) * 2048 + (wave & 3) * 512 + lane * 8);
    }
    LGKM0();
    BAR();
    STAMP(8);
    if (wave >= 4) { helper_offsets(); helper_fill(); }
  }
  }

  if (wave >= 4) {
    const int lid = wave - 4;
    if (!dwn) {
      VMCNT(0);
      STAMP(3);
      BAR();
    }
    if (lid >= 2) {
      const u32 sk = lds_rd(L, L_SEL + 4 * (lane & 7));
      _Pragma("nounroll") for (int c = lid - 2; c < 8; c += 2) {
        const u32 tk = (u32)__builtin_amdgcn_readlane((int)sk, c) & 0x7ffu;
        if ((int)(tk >> 7) != pan2) continue;
        float y0, y1;
        _Pragma("nounroll") for (int sp = 0; sp < SPIN_CAP; ++sp) {
          u32 bad = 0;
          y0 = 0.f; y1 = 0.f;
#pragma unroll
          for (int j = 0; j < 4; ++j) {
            const u32x4 v = ld_sc1_128(rsWS, (lane >> 5) * 65536 + (lane & 31) * 16, WS_YS + j * 131072 + c * 8192 + cg * 512);
            bad |= (v.y ^ tag) | (v.w ^ tag);
            y0 += __uint_as_float(v.x); y1 += __uint_as_float(v.z);
          }
          if (__all(bad == 0)) break;
          __builtin_amdgcn_s_sleep(8);
        }
        y0 = sum32(y0); y1 = sum32(y1);
        if (lane < 32) *(LAS f32x2*)(L + L_UPD + (c * 64 + lane * 2) * 4) = f32x2{y0, y1};
      }
      LGKM0();
      lds_wr(L, L_UPDF + 4 * (lid - 2), 1);
      STAMP(7);
    }
    return;
  }

  {
    const bool pol = wave == 0;
    const u32 fo = WS_OF + (((pan2 * 4 + 3 * (lane >> 5)) / 5) * 16 + (lane & 15)) * 128;
    u32 fv = ~tag;
    if (dwn && pol) fv = ld_sc1_32(rsWS, fo, 0);
    SB();
    if (!gemm && !dwn) BAR();
    oc_init();
    SB();
    if (gemm) {
      VMCNT(0);
      if (lane == 0 && __hip_atomic_fetch_add((LAS u32*)(L + L_ARR), 1u, __ATOMIC_RELAXED, __HIP_MEMORY_SCOPE_WORKGROUP) == 3)
        st_sc1_32(tag, rsWS, WS_OF + (pan * 16 + hp) * 128);
      STAMP(4);
      SB();
    }
    const int par2 = wave >> 1, t2 = (wave & 1) * 2;
    const int q4 = i >> 2, p4 = i & 3;
    const u32 wb = lbase + (4 * g + q4) * 128 + p4 * 8 + par2 * 2048, wsw = (2 * g + (q4 >> 1)) & 3;
    u32 wa[4], wc[4];
#pragma unroll
    for (int c = 0; c < 4; ++c) { wa[c] = wb + ((c ^ wsw) << 5); wc[c] = wa[c] ^ 2048; PIN(wa[c]); PIN(wc[c]); }
    const int T0 = pan2 * 4 + t2;
    if (gemm) BAR();
    s16x8 wf[3][4];
#pragma unroll
    for (int k = 0; k < 2; ++k)
#pragma unroll
      for (int c = 0; c < 4; ++c) wf[k][c] = tr_frag(LP(wa[c]), LP(wc[c]), k * 4096, k * 4096);
    const u32 sk = lds_rd(L, L_SEL + 4 * (lane & 7));
    __builtin_amdgcn_s_waitcnt(0xc07f);
    SB();
    if (!gemm) wait_until(tstart, T_O);
    if (pol) {
      _Pragma("nounroll") for (int sp = 0; sp < SPIN_CAP; ++sp) {
        if (__all(fv == tag)) break;
        if (sp) __builtin_amdgcn_s_sleep(2);
        fv = ld_sc1_32(rsWS, fo, 0);
      }
      lds_wr(L, L_GO, 1);
    } else {
      _Pragma("nounroll") for (int sp = 0; sp < SPIN_CAP; ++sp) if (rfl((int)lds_rd(L, L_GO))) break;
    }
    CBAR();
    STAMP(5);
    const u32 abase = WS_O + (par2 * 32 * 64 + T0) * 1024;
    u32x4 af[OWIN][2];
#pragma unroll
    for (int hh = 0; hh < OWIN; ++hh) {
#pragma unroll
      for (int tt = 0; tt < 2; ++tt) af[hh][tt] = ld_sc1_128(rsWS, lane * 16 + tt * 1024, abase + ((hh + hrot) & 31) * 65536);
      SB();
    }
#pragma unroll
    for (int hh = 0; hh < 32; ++hh) {
      if (hh == 21 && dwn) {
        _Pragma("nounroll") for (int sp = 0; sp < SPIN_CAP; ++sp) if (__all(lds_rd(L, L_HS + (lane & 3) * 4) == 2u)) break;
        CBAR(); SB();
      }
      if (hh == 14) {
#pragma unroll
        for (int c = 0; c < 4; ++c) { wa[c] += 65536; wc[c] += 65536; PIN(wa[c]); PIN(wc[c]); }
        SB();
      }
      if (hh == 31) __builtin_amdgcn_s_waitcnt(0xc07f); else __builtin_amdgcn_s_waitcnt(0xc87f);
      SB();
      if (hh + 2 < 32) {
#pragma unroll
        for (int c = 0; c < 4; ++c) wf[(hh + 2) % 3][c] = tr_frag(LP(wa[c]), LP(wc[c]), ((hh + 2) & 15) * 4096, ((hh + 2) & 15) * 4096);
      }
#pragma unroll
      for (int tt = 0; tt < 2; ++tt) {
#pragma unroll
        for (int c = 0; c < 4; ++c) oc[tt][c] = mfma(wf[hh % 3][c], as_frag(af[hh % OWIN][tt]), oc[tt][c]);
        if (hh + OWIN < 32) af[hh % OWIN][tt] = ld_sc1_128(rsWS, lane * 16 + tt * 1024, abase + ((hh + OWIN + hrot) & 31) * 65536);
      }
#pragma unroll
      for (int k = 0; k < 2; ++k) {
        __builtin_amdgcn_sched_group_barrier(0x008, 1, 0); __builtin_amdgcn_sched_group_barrier(0x100, 2, 0);
        __builtin_amdgcn_sched_group_barrier(0x008, 1, 0); __builtin_amdgcn_sched_group_barrier(0x100, 1, 0);
        __builtin_amdgcn_sched_group_barrier(0x008, 1, 0); __builtin_amdgcn_sched_group_barrier(0x100, 1, 0);
        __builtin_amdgcn_sched_group_barrier(0x008, 1, 0); __builtin_amdgcn_sched_group_barrier(0x020, 1, 0);
      }
      SB();
    }
    STAMP(6);
    {
      const u32 stl = (sk & 0x7ffu) - (u32)(pan2 * 128 + par2 + 32 * t2);
      u64 hit = __ballot(stl < 64u && (stl & 1u) == 0u) & 0xffull;
      if (hit) {
        lds_wait_set(L, L_UPDF + (lane & 1) * 4);
        while (hit) {
          const int c = __builtin_ctzll(hit);
          hit &= hit - 1;
          const u32 tl = (u32)__builtin_amdgcn_readlane((int)stl, c), ob = (u32)__builtin_amdgcn_readlane((int)sk, c) >> 16;
          const float sg = rbf(sigm(bf_lo((ob & 0x8000u) ? (ob & 0x7fffu) : (~ob & 0xffffu))));
          const float f0 = ((tl >> 1) == (u32)i) ? sg : 0.f, f1 = ((tl >> 1) == (u32)i + 16u) ? sg : 0.f;
#pragma unroll
          for (int cc = 0; cc < 4; ++cc) {
            const f32x4 u = *(LAS f32x4*)(L + L_UPD + (c * 64 + 16 * cc + 4 * g) * 4);
            oc[0][cc] += u * f0; oc[1][cc] += u * f1;
          }
        }
      }
    }
    const rsrc_t rsO = mk_rsrc(P.out, S_ * D_ * 2);
    const u32 xo = ((pan2 * 128 + par2 + 32 * t2 + 2 * i) * D_ + cg * 64 + (g & 1) * 16 + (g >> 1) * 8) * 2;
#pragma unroll
    for (int tt = 0; tt < 2; ++tt)
#pragma unroll
      for (int cp = 0; cp < 2; ++cp) {
        const u32 a0 = pack2(oc[tt][2 * cp][0], oc[tt][2 * cp][1]), a1 = pack2(oc[tt][2 * cp][2], oc[tt][2 * cp][3]);
        const u32 b0 = pack2(oc[tt][2 * cp + 1][0], oc[tt][2 * cp + 1][1]), b1 = pack2(oc[tt][2 * cp + 1][2], oc[tt][2 * cp + 1][3]);
        const auto e0 = __builtin_amdgcn_permlane16_swap(a0, b0, false, false);
        const auto e1 = __builtin_amdgcn_permlane16_swap(a1, b1, false, false);
        st_sc1_128(u32x4{e0[0], e1[0], e0[1], e1[1]}, rsO, xo + tt * 65536 + cp * 64);
      }
    STAMP(7);
  }
}

static u64 k_order(int odd, int pl, int hl) {
  const int np = 7 - odd;
  int slot[16], used[16];
  for (int k = 0; k < 16; ++k) { slot[k] = -1; used[k] = 0; }
  for (int k = 2; k < 16; ++k) {
    const int h0 = k & 3, p0 = (k * 3) % np;
    int want = -1;
    if (hl == h0 && pl % np == p0) want = k - 2; else if (hl == h0 || pl % np == p0) want = k - 1;
    if (want >= 0) {
      if (want < 1) want = 1;
      while (want > 1 && used[want]) --want;
      if (!used[want]) { slot[k] = want; used[want] = 1; }
    }
  }
  int s = 0;
  for (int k = 0; k < 16; ++k) if (slot[k] < 0) { while (used[s]) ++s; slot[k] = s; used[s] = 1; }
  u64 r = 0;
  for (int k = 0; k < 16; ++k) r |= (u64)k << (4 * slot[k]);
  return r;
}

extern "C" void kernel_launch(void* const* d_in, const int* in_sizes, int n_in, void* d_out, int out_size, void* d_ws, size_t ws_size,
                              hipStream_t stream) {
  Args A;
  Params& P = A.p;
  P.x = (const u16*)d_in[0]; P.wqkv = (const u16*)d_in[1]; P.wo = (const u16*)d_in[2]; P.wr = (const u16*)d_in[4];
  P.wg = (const u16*)d_in[5]; P.wu = (const u16*)d_in[6]; P.wd = (const u16*)d_in[7];
  P.out = (u16*)d_out; P.ws = (char*)d_ws;
  for (int e = 0; e < 64; ++e) A.kt[e] = k_order(e >> 5, (e >> 2) & 7, e & 3);
  (void)hipFuncSetAttribute((const void*)fused_kernel, hipFuncAttributeMaxDynamicSharedMemorySize, L_END + 64);
  hipLaunchKernelGGL(fused_kernel, dim3(256), dim3(512), L_END + 64, stream, A);
}
```

```cpp
#include <hip/hip_runtime.h>
#include <stdint.h>

typedef unsigned short u16;
typedef unsigned u32;
typedef unsigned long long u64;
typedef __attribute__((ext_vector_type(4))) short s16x4;
typedef __attribute__((ext_vector_type(8))) short s16x8;
typedef __attribute__((ext_vector_type(2))) float f32x2;
typedef __attribute__((ext_vector_type(4))) float f32x4;
typedef __attribute__((ext_vector_type(4))) unsigned u32x4;
typedef __attribute__((ext_vector_type(2))) unsigned u32x2;
typedef __attribute__((ext_vector_type(16))) unsigned u32x16;
typedef __attribute__((ext_vector_type(2))) __bf16 bf16x2;
typedef __amdgpu_buffer_rsrc_t rsrc_t;
#define LAS __attribute__((address_space(3)))
#define GAS __attribute__((address_space(1)))
#define DEV __device__ __forceinline__

#define STAMPS 0
constexpr int OWIN = 6;
constexpr int GWIN = 6, GPRE = 4;
constexpr int T_GATHER = 350, T_SEL = 580, T_H = 1200, T_O = 1520;
constexpr int T_Y = 100;
constexpr int WARM_AT = 10;
constexpr u64 HELPK = 0x765432ull, ROLLK = 0xfedcba98ull;
constexpr int HELPN = 6, RS0 = 1, RSN = 8;
constexpr int CODE_LINES = 347;

constexpr int S_ = 2048, D_ = 1024, F_ = 3584;
constexpr u32 WS_EPOCH = 0;
constexpr u32 WS_SEL = 4096;
constexpr u32 WS_RG = 16384;
constexpr u32 WS_SG = 32768;
constexpr u32 WS_OF = 65536;
constexpr u32 WS_H = WS_OF + 131072;
constexpr u32 WS_YS = WS_H + 131072;
constexpr u32 WS_O = WS_YS + 524288;
constexpr u32 WS_END = WS_O + S_ * D_ * 2;
constexpr u32 WS_STAMP = 16u << 20;
constexpr u32 WOFF = 22528, SLOT = 47104;
constexpr u32 L_HX = 131072;
constexpr u32 L_XT = 3 * SLOT;
constexpr u32 L_STQ = L_XT + 16384;
constexpr u32 L_STO = L_STQ + 1408;
constexpr u32 L_UPD = L_STO + 1024;
constexpr u32 L_ONE = L_UPD + 2048;
constexpr u32 L_CTL = L_ONE + 128;
constexpr u32 L_TAG = L_CTL, L_ARR = L_CTL + 4, L_UPDF = L_CTL + 8  , L_HS = L_CTL + 16  , L_SEL = L_CTL + 32  ;
constexpr u32 L_PW = L_CTL + 64, L_HR = L_CTL + 96;
constexpr u32 L_GO = L_CTL + 128;
constexpr u32 L_END = L_CTL + 192;
constexpr int SPIN_CAP = 1 << 22;
static_assert(L_END <= 163840 && L_ONE % 128 == 0, "LDS");

struct Params {
  const u16 *x, *wqkv, *wo, *wr, *wg, *wu, *wd;
  u16* out;
  char* ws;
};
struct Args {
  Params p;
  u64 kt[64];
};

DEV float bf_lo(u32 v) { return __uint_as_float(v << 16); }
DEV float bf_hi(u32 v) { return __uint_as_float(v & 0xffff0000u); }
DEV u32 pack2(float a, float b) { bf16x2 p = {(__bf16)a, (__bf16)b}; return __builtin_bit_cast(u32, p); }
DEV float rbf(float a) { return bf_lo(pack2(a, a)); }
DEV float dot2(u32 a, u32 b, float c) { return __builtin_amdgcn_fdot2_f32_bf16(__builtin_bit_cast(bf16x2, a), __builtin_bit_cast(bf16x2, b), c, false); }
DEV s16x8 as_frag(u32x4 v) { return __builtin_bit_cast(s16x8, v); }
DEV f32x4 mfma(s16x8 a, s16x8 b, f32x4 c) { return __builtin_amdgcn_mfma_f32_16x16x32_bf16(a, b, c, 0, 0, 0); }
DEV int rfl(int v) { return __builtin_amdgcn_readfirstlane(v); }
template <int CTRL, int RM, bool BC> DEV float dppf(float v) { return __int_as_float(__builtin_amdgcn_update_dpp(0, __float_as_int(v), CTRL, RM, 0xf, BC)); }
template <int CTRL, int RM, bool BC> DEV u32 dppu(u32 v) { return (u32)__builtin_amdgcn_update_dpp(0, (int)v, CTRL, RM, 0xf, BC); }
DEV float half_sum(float v) {
  v += dppf<0x111, 0xf, true>(v); v += dppf<0x112, 0xf, true>(v); v += dppf<0x114, 0xf, true>(v); v += dppf<0x118, 0xf, true>(v);
  v += dppf<0x142, 0xa, false>(v);
  return v;
}
DEV u32 wave_max_u32(u32 v) {
  v = max(v, dppu<0x111, 0xf, true>(v)); v = max(v, dppu<0x112, 0xf, true>(v)); v = max(v, dppu<0x114, 0xf, true>(v)); v = max(v, dppu<0x118, 0xf, true>(v));
  v = max(v, dppu<0x142, 0xa, false>(v)); v = max(v, dppu<0x143, 0xc, false>(v));
  return (u32)__builtin_amdgcn_readlane((int)v, 63);
}
DEV float vmax(float a, float b) { return __builtin_elementwise_maximum(a, b); }
DEV float vmax3(float a, float b, float c) { return vmax(vmax(a, b), c); }
DEV float max16(float v) { u32 a = __float_as_uint(v); auto r = __builtin_amdgcn_permlane16_swap(a, a, false, false); u32 x = r[0], y = r[1]; return vmax(__uint_as_float(x), __uint_as_float(y)); }
DEV float max32(float v) { u32 a = __float_as_uint(v); auto r = __builtin_amdgcn_permlane32_swap(a, a, false, false); u32 x = r[0], y = r[1]; return vmax(__uint_as_float(x), __uint_as_float(y)); }
DEV float sum32(float v) { u32 a = __float_as_uint(v); auto r = __builtin_amdgcn_permlane32_swap(a, a, false, false); u32 x = r[0], y = r[1]; return __uint_as_float(x) + __uint_as_float(y); }
DEV u32 ord16(u32 b) { return (b & 0x8000u) ? (~b & 0xffffu) : (b | 0x8000u); }
DEV float sigm(float v) { return __builtin_amdgcn_rcpf(1.f + __builtin_amdgcn_exp2f(-v * 1.4426950408889634f)); }

DEV rsrc_t mk_rsrc(const void* p, u32 bytes) { return __builtin_amdgcn_make_buffer_rsrc((void*)p, 0, (int)bytes, 0x00020000); }
DEV u32x4 ld_nt_128(rsrc_t rs, u32 voff, u32 soff) { return __builtin_amdgcn_raw_buffer_load_b128(rs, voff, soff, 2); }
DEV u32x4 ld_sc1_128(rsrc_t rs, u32 voff, u32 soff) { return __builtin_amdgcn_raw_buffer_load_b128(rs, voff, soff, 16); }
DEV u32x2 ld_sc1_64(rsrc_t rs, u32 voff, u32 soff) { return __builtin_amdgcn_raw_buffer_load_b64(rs, voff, soff, 16); }
DEV u32 ld_sc1_32(rsrc_t rs, u32 voff, u32 soff) { return __builtin_amdgcn_raw_buffer_load_b32(rs, voff, soff, 16); }
DEV void st_sc1_128(u32x4 v, rsrc_t rs, u32 voff) { __builtin_amdgcn_raw_buffer_store_b128(v, rs, voff, 0, 16); }
DEV void st_sc1_64(u32x2 v, rsrc_t rs, u32 voff) { __builtin_amdgcn_raw_buffer_store_b64(v, rs, voff, 0, 16); }
DEV void st_sc1_32(u32 v, rsrc_t rs, u32 voff) { __builtin_amdgcn_raw_buffer_store_b32(v, rs, voff, 0, 16); }
#define VMCNT(n) asm volatile("s_waitcnt vmcnt(" #n ")" ::: "memory")
#define LGKM0() asm volatile("s_waitcnt lgkmcnt(0)" ::: "memory")
#define CBAR() asm volatile("" ::: "memory")
#define BAR() do { CBAR(); __builtin_amdgcn_s_barrier(); CBAR(); } while (0)
#define SB() __builtin_amdgcn_sched_barrier(0)
#define LP(a) ((LAS char*)(uintptr_t)(a))
#define PIN(x) asm volatile("" : "+v"(x))

#define TICK() 0ull
#define RT() 0ull
#define STAMP(k) do {} while (0)
#define CSTAMP(k) do {} while (0)
#define LSTAMP(k) do {} while (0)

DEV u32 lds_rd(LAS char* L, u32 off) { return *(volatile LAS u32*)(L + off); }
DEV void lds_wr(LAS char* L, u32 off, u32 v) { *(volatile LAS u32*)(L + off) = v; }
DEV void lds_wait_set(LAS char* L, u32 off) {
  _Pragma("nounroll") for (int sp = 0; sp < SPIN_CAP; ++sp) {
    if (__all(lds_rd(L, off) != 0u)) break;
    __builtin_amdgcn_s_sleep(1);
  }
  CBAR();
}
DEV void wait_until(u64 t0, u32 ticks) {
  while (__builtin_amdgcn_s_memrealtime() - t0 < ticks) __builtin_amdgcn_s_sleep(4);
}
#define DMA_HEAD "s_mov_b32 m0, %0\n\ts_nop 0\n\t"
DEV void dma16(u32 lds, u32 voff, rsrc_t rs, u32 soff) {
  asm volatile(DMA_HEAD "buffer_load_dwordx4 %1, %2, %3 offen lds" ::"s"(lds), "v"(voff), "s"(rs), "s"(soff) : "memory");
}
DEV void dma16x4(u32 lds, u32 v0, u32 v1, u32 v2, u32 v3, rsrc_t rs, u32 soff) {
  asm volatile(DMA_HEAD "buffer_load_dwordx4 %1, %5, %6 offen lds\n\tbuffer_load_dwordx4 %2, %5, %6 offen offset:1024 lds\n\t"
               "buffer_load_dwordx4 %3, %5, %6 offen offset:2048 lds\n\tbuffer_load_dwordx4 %4, %5, %6 offen offset:3072 lds"
               ::"s"(lds), "v"(v0), "v"(v1), "v"(v2), "v"(v3), "s"(rs), "s"(soff) : "memory");
}
DEV void dma16x2(u32 lds, u32 v0, u32 v1, rsrc_t rs, u32 soff) {
  asm volatile(DMA_HEAD "buffer_load_dwordx4 %1, %3, %4 offen lds\n\tbuffer_load_dwordx4 %2, %3, %4 offen offset:1024 lds"
               ::"s"(lds), "v"(v0), "v"(v1), "s"(rs), "s"(soff) : "memory");
}

struct Frags { s16x8 x[6]; s16x8 w[6]; };

DEV s16x8 tr_frag(LAS char* p, LAS char* q, u32 o0, u32 o1) {
  s16x4 a = __builtin_amdgcn_ds_read_tr16_b64_v4i16((LAS s16x4*)(p + o0));
  s16x4 b = __builtin_amdgcn_ds_read_tr16_b64_v4i16((LAS s16x4*)(q + o1));
  return __builtin_shufflevector(a, b, 0, 1, 2, 3, 4, 5, 6, 7);
}
template <int KK> DEV void load_frags(Frags& f, LAS char* aH, LAS char* aT, LAS char* w0, LAS char* w1) {
#pragma unroll
  for (int t = 0; t < 5; ++t) f.x[1 + t] = *(LAS s16x8*)(aT + t * 2048);
  f.x[0] = *(LAS s16x8*)aH;
#pragma unroll
  for (int sec = 0; sec < 3; ++sec) {
    f.w[2 * sec] = tr_frag(w0, w0, sec * 8192 + KK * 4096, sec * 8192 + KK * 4096 + 512);
    f.w[2 * sec + 1] = tr_frag(w1, w1, sec * 8192 + KK * 4096, sec * 8192 + KK * 4096 + 512);
  }
}
template <int A, int B> DEV void mma_range(f32x4 (&acc)[6][6], const Frags& f, s16x8 e0) {
#pragma unroll
  for (int n = A; n < B; ++n) {
    const int p = n / 11, r = n % 11, w = 2 * p + (r >= 6), t = (r >= 6) ? 11 - r : 5 - r;
    acc[w][t] = (w < 4) ? mfma(f.w[w], (w == 0 && t == 0) ? e0 : f.x[t], acc[w][t]) : mfma(f.x[t], f.w[w], acc[w][t]);
  }
}
DEV void interleave() {
#pragma unroll
  for (int k = 0; k < 6; ++k) { __builtin_amdgcn_sched_group_barrier(0x008, 1, 0); __builtin_amdgcn_sched_group_barrier(0x100, 1, 0); }
#pragma unroll
  for (int k = 0; k < 6; ++k) {
    __builtin_amdgcn_sched_group_barrier(0x008, 1, 0); __builtin_amdgcn_sched_group_barrier(0x100, 1, 0);
    __builtin_amdgcn_sched_group_barrier(0x008, 1, 0); __builtin_amdgcn_sched_group_barrier(0x100, 1, 0);
    __builtin_amdgcn_sched_group_barrier(0x008, 1, 0);
  }
}

template <int E> DEV s16x8 col_frag(const u32x4* L) {
  constexpr u32 sel = (E & 1) ? 0x07060302u : 0x05040100u;
  u32x4 r;
  r.x = __builtin_amdgcn_perm(L[1][E >> 1], L[0][E >> 1], sel);
  r.y = __builtin_amdgcn_perm(L[3][E >> 1], L[2][E >> 1], sel);
  r.z = __builtin_amdgcn_perm(L[5][E >> 1], L[4][E >> 1], sel);
  r.w = __builtin_amdgcn_perm(L[7][E >> 1], L[6][E >> 1], sel);
  return as_frag(r);
}
DEV void mma_cols(f32x4* acc, const u32x4* L, s16x8 b) {
  acc[0] = mfma(col_frag<0>(L), b, acc[0]); acc[1] = mfma(col_frag<1>(L), b, acc[1]);
  acc[2] = mfma(col_frag<2>(L), b, acc[2]); acc[3] = mfma(col_frag<3>(L), b, acc[3]);
  acc[4] = mfma(col_frag<4>(L), b, acc[4]); acc[5] = mfma(col_frag<5>(L), b, acc[5]);
  acc[6] = mfma(col_frag<6>(L), b, acc[6]); acc[7] = mfma(col_frag<7>(L), b, acc[7]);
}

DEV void transpose_grp(u32x4* L) {
  const s16x8 f0 = col_frag<0>(L), f1 = col_frag<1>(L), f2 = col_frag<2>(L), f3 = col_frag<3>(L), f4 = col_frag<4>(L), f5 = col_frag<5>(L), f6 = col_frag<6>(L), f7 = col_frag<7>(L);
  L[0] = __builtin_bit_cast(u32x4, f0); L[1] = __builtin_bit_cast(u32x4, f1); L[2] = __builtin_bit_cast(u32x4, f2); L[3] = __builtin_bit_cast(u32x4, f3);
  L[4] = __builtin_bit_cast(u32x4, f4); L[5] = __builtin_bit_cast(u32x4, f5); L[6] = __builtin_bit_cast(u32x4, f6); L[7] = __builtin_bit_cast(u32x4, f7);
#pragma unroll
  for (int e = 0; e < 8; ++e) PIN(L[e]);
  SB();
}
DEV void mma_frags8(f32x4* acc, const u32x4* L, s16x8 b) {
#pragma unroll
  for (int e = 0; e < 8; ++e) acc[e] = mfma(as_frag(L[e]), b, acc[e]);
}
DEV void warm_line(u32 lds, const char* a) {
  asm volatile(DMA_HEAD "global_load_lds_dword %1, off" ::"s"(lds), "v"(a) : "memory");
}

__global__ void __launch_bounds__(512, 2) fused_kernel(Args Parg) {
  extern __shared__ __attribute__((aligned(128))) char smem[];
  u32x16 ka;
  u64 kb;
  asm volatile("s_load_dwordx16 %0, %2, 0x0\n\ts_load_dwordx2 %1, %2, 0x40" : "=&s"(ka), "=&s"(kb) : "s"(__builtin_amdgcn_kernarg_segment_ptr()));
  Params P;
  const char* pc0;
  asm volatile("s_getpc_b64 %0" : "=s"(pc0));
  LAS char* L = (LAS char*)smem;
  const u32 lbase = (u32)(uintptr_t)L;
  const int lane = threadIdx.x & 63;
  const int wave = rfl(threadIdx.x >> 6);
  const int bid = blockIdx.x, xg = bid & 7, loc = bid >> 3;
  const int np = 7 - (xg & 1);
  const bool gemm = loc < 4 * np;
  const int pl = loc >> 2, hl = loc & 3;
  const int pan = (xg & 1) * 7 + pl, hp = (xg >> 1) * 4 + hl, r0 = pan * 160;
  const int fi = loc - 4 * np;
  const bool dwn = (u32)(fi - 2 - 3 * (xg & 1)) <= 2u * (xg & 1);
  const int fu = (xg & 1) * 12 + (xg >> 1) * (3 + 2 * (xg & 1)) + min(fi, 2 + 2 * (xg & 1));
  const int du = (xg & 1) ? 4 + (xg >> 1) * 3 + fi - 5 : (xg >> 1);
  const int pan2 = (xg & 1) * 8 + (xg >> 1) * 2 + (loc >> 4);
  const int cg = (int)((((loc & 16) ? 0xfedc0a987653421bull : 0xfedc8a9b76534210ull) >> (4 * (loc & 15))) & 15), hrot = 2 * cg;
  const int g = lane >> 4, i = lane & 15;
  u64 kord;
  asm volatile("s_load_dwordx2 %0, %1, %2" : "=&s"(kord) : "s"(__builtin_amdgcn_kernarg_segment_ptr()), "s"(72 + (((xg & 1) * 32 + loc) & 63) * 8));
  rsrc_t rsWS;
  const u64 tstart = __builtin_amdgcn_s_memrealtime();
  u32 tag = 0;
  f32x4 oc[2][4];
  auto oc_init = [&]() {
    const int tl = (wave >> 1) + 64 * (wave & 1) + 2 * i;
    f32x2 st[2];
    u32x2 v[2][4];
#pragma unroll
    for (int tt = 0; tt < 2; ++tt) {
      st[tt] = *(LAS f32x2*)(L + L_STO + tl * 8 + tt * 256);
#pragma unroll
      for (int c = 0; c < 4; ++c) v[tt][c] = *(LAS u32x2*)(L + L_XT + tl * 128 + (((2 * c + (g >> 1)) ^ (i & 7)) << 4) + (g & 1) * 8 + tt * 4096);
    }
    SB();
#pragma unroll
    for (int tt = 0; tt < 2; ++tt) {
      const float k1 = 1.f + st[tt].y, k0 = -st[tt].x * st[tt].y;
#pragma unroll
      for (int c = 0; c < 4; ++c) oc[tt][c] = f32x4{bf_lo(v[tt][c].x), bf_hi(v[tt][c].x), bf_lo(v[tt][c].y), bf_hi(v[tt][c].y)} * k1 + k0;
    }
#pragma unroll
    for (int q = 0; q < 8; ++q) PIN(oc[q >> 2][q & 3]);
  };

  u32 tq[3], vO, vx;
  auto helper_offsets = [&]() {
    u32 lane = threadIdx.x & 63;
    PIN(lane);
    const int lr = lane >> 3, lc = lane & 7;
#pragma unroll
    for (int j = 0; j < 3; ++j) {
      const int rho = min(64 * j + (int)lane, 175), pr = rho >= 96;
      const int tk = r0 + ((rho < 16) ? rho - 16 : pr + 2 * (rho - 16 - 80 * pr));
      const u32 a = (u32)(pan2 * 128 + (rho & 127)) * 8, b = (((u32)tk >= (u32)S_) | (rho == 0)) ? 1u : (u32)tk * 8;
      tq[j] = (wave == 7) ? a : b;
      PIN(tq[j]);
    }
    vO = lr * 2048 + ((u32)(lc ^ (((lr >> 1) & 3) << 1)) << 4) + cg * 128;
    vx = lr * 2048 + ((u32)(lc ^ (lr >> 1)) << 4) + cg * 128;
    PIN(vO); PIN(vx);
  };
  auto wo_grp = [&](int q) { dma16x4(lbase + q * 4096, vO, vO + 15 * 1024, vO + 30 * 1024, vO + 45 * 1024, mk_rsrc(P.wo, D_ * D_ * 2), ((q + hrot) & 31) * 65536); };
  auto helper_fill = [&]() {
    const int lid = wave - 4;
    tag = (u32)rfl((int)lds_rd(L, L_TAG));
    if (gemm) { VMCNT(11); BAR(); }
    if (lid >= 2) {
      u32x2 sv[3];
#pragma unroll
      for (int q = 2 * lid - 4; q < 2 * lid - 2; ++q) dma16x4(lbase + L_XT + q * 4096, vx, (vx ^ 64) + 15 * 1024, vx + 30 * 1024, (vx ^ 64) + 45 * 1024, mk_rsrc(P.x, S_ * D_ * 2), (pan2 * 128 + 32 * q) * 2048);
#pragma unroll
      for (int j = 0; j < 3; ++j) sv[j] = ld_sc1_64(rsWS, tq[j] & ~7u, WS_SG);
      SB();
      if (gemm) { VMCNT(11); BAR(); }
      _Pragma("nounroll") for (int sp = 0; sp < SPIN_CAP; ++sp) {
        if (__all(sv[0].y == tag && sv[1].y == tag && sv[2].y == tag)) break;
        __builtin_amdgcn_s_sleep(2);
#pragma unroll
        for (int j = 0; j < 3; ++j) sv[j] = ld_sc1_64(rsWS, tq[j] & ~7u, WS_SG);
      }
      f32x2 st[3];
      u32 ta = lbase + ((lid == 3) ? L_STO + lane * 8 : L_STQ + lane * 4), tb2 = ta + ((lid == 3) ? 4 : 704);
      u32 tc = (lid == 2 && lane < 48) ? ta + 512 : lbase + L_UPD + lane * 4, td = (lid == 2 && lane < 48) ? tb2 + 512 : tc;
      const u32 ts = (lid == 3) ? 512 : 256;
#pragma unroll
      for (int j = 0; j < 3; ++j) {
        st[j] = f32x2{__uint_as_float((sv[j].x << 16) ^ ((lid == 3) ? 0u : 0x80000000u)), __builtin_amdgcn_rcpf(bf_hi(sv[j].x))};
        if (tq[j] & 1u) st[j] = f32x2{0.f, 0.f};
        PIN(st[j]);
      }
      u32 te = ta + ts, tf = tb2 + ts;
      PIN(ta); PIN(tb2); PIN(tc); PIN(td); PIN(te); PIN(tf);
      VMCNT(0);
      if (gemm) BAR();
      *(LAS float*)LP(ta) = st[0].x; *(LAS float*)LP(tb2) = st[0].y;
      *(LAS float*)LP(te) = st[1].x; *(LAS float*)LP(tf) = st[1].y;
      *(LAS float*)LP(tc) = st[2].x; *(LAS float*)LP(td) = st[2].y;
      LGKM0();
    } else {
      if (gemm) { VMCNT(0); BAR(); }
      wo_grp(12 + lid); wo_grp(14 + lid);
#pragma unroll
      for (int q = 0; q < 4; ++q) wo_grp(16 + 2 * q + lid);
      if (gemm) BAR();
    }
    if (gemm) BAR();
    int l2 = lid;
    asm volatile("" : "+s"(l2));
    wo_grp(l2); wo_grp(4 + l2); wo_grp(8 + l2);
    if (!dwn) { wo_grp(24 + l2); wo_grp(28 + l2); }
    if (lid == 2) {
      u32x2 sk;
      _Pragma("nounroll") for (int sp = 0; sp < SPIN_CAP; ++sp) {
        sk = ld_sc1_64(rsWS, (lane & 7) * 8, WS_SEL);
        if (__all(sk.y == tag)) break;
        __builtin_amdgcn_s_sleep(8);
      }
      if (lane < 8) lds_wr(L, L_SEL + 4 * lane, sk.x);
      LGKM0();
    }
  };
  {
    u32 ln = threadIdx.x & 63;
    PIN(ln);
    if (ln < 2) warm_line(lbase + L_UPD, pc0 + min((loc * 8 + wave) * 2 + (int)ln, CODE_LINES - 1) * 128);
  }
  const int lid = wave - 4;
  const bool isx = lid < 2;
  u32 v0 = 0, v1 = 0, v2 = 0, v3 = 0, vh = 0, g0 = 0, g1 = 0, g2 = 0, kstep = 0, d0 = 0;
  {
    int wave_a = wave;
    asm volatile("" : "+s"(wave_a));
    if (gemm && wave_a >= 4) {
      u32 ln = threadIdx.x & 63;
      PIN(ln);
      const int lr = ln >> 3, lc = ln & 7;
      const u32 sw = (u32)(lc ^ (isx ? lr >> 1 : lr & 2)) << 4;
      const u32 vb = lr * (isx ? 4096 : 6144) + sw + (isx ? (r0 + lid) * 2048 : hp * 128);
      const u32 pp = (isx ? 32768 : 49152) - 1024;
      v0 = vb; v1 = (vb ^ 64) + pp; v2 = vb + 2 * pp; v3 = (vb ^ 64) + 3 * pp;
      g0 = (lid == 3) ? 198656 : 0; g1 = isx ? 131072 : (lid == 3) ? 4096 : 196608; g2 = isx ? 262144 : (lid == 3) ? 200704 : 2048;
      vh = max(r0 - 16 + 8 * lid + lr, 0) * 2048 + (sw ^ (lid << 6));
      kstep = isx ? 128u : 64u * 6144u; d0 = lbase + (isx ? 2 + 10 * lid : 22 + 12 * (lid - 2)) * 1024;
      PIN(v0); PIN(v1); PIN(v2); PIN(v3); PIN(vh);
      asm volatile("" : "+s"(g0), "+s"(g1), "+s"(g2), "+s"(kstep), "+s"(d0));
    }
  }
  u32 ev;
  asm volatile("s_waitcnt lgkmcnt(0)" : "+s"(ka), "+s"(kb), "+s"(kord));
  {
    auto ptr = [&](int k) { return (char*)(GAS char*)(((u64)ka[2 * k + 1] << 32) | ka[2 * k]); };
    P.x = (const u16*)ptr(0); P.wqkv = (const u16*)ptr(1); P.wo = (const u16*)ptr(2); P.wr = (const u16*)ptr(3);
    P.wg = (const u16*)ptr(4); P.wu = (const u16*)ptr(5); P.wd = (const u16*)ptr(6); P.out = (u16*)ptr(7);
    P.ws = (char*)(GAS char*)kb;
  }
  rsWS = mk_rsrc(P.ws, WS_END);
  STAMP(0);
  if (wave < 4) {
    asm volatile("s_load_dword %0, %1, 0x0" : "=s"(ev) : "s"(P.ws + WS_EPOCH) : "memory");
    u32 ln = threadIdx.x & 63;
    PIN(ln);
    const u32 d = lbase + 2 * SLOT + wave * 6144, v = ln * 16;
    dma16x4(d, v, v, v, v, mk_rsrc(P.x, S_ * D_ * 2), (pan2 * 128 + cg * 8 + wave * 2) * 2048);
    dma16x2(d + 4096, v, v, mk_rsrc(P.wr, D_ * 2), 0);
  }
  SB();
  int wave_b = wave;
  asm volatile("" : "+s"(wave_b));
  if (gemm && wave_b >= 4) {
    const rsrc_t rs = mk_rsrc(isx ? P.x : P.wqkv, isx ? S_ * D_ * 2 : D_ * 3 * D_ * 2);
    auto issue_k = [&](u32 ko, u32 so) {
      dma16x4(d0 + so, v0, v1, v2, v3, rs, ko + g0);
      dma16x4(d0 + so + 4096, v0, v1, v2, v3, rs, ko + g1);
      if (isx) { dma16x2(d0 + so + 8192, v0, v1, rs, ko + g2); dma16(lbase + so + lid * 1024, vh, rs, ko); }
      else dma16x4(d0 + so + 8192, v0, v1, v2, v3, rs, ko + g2);
    };
    auto issue = [&](int s, u32 so) { issue_k(((u32)(kord >> (4 * s)) & 15u) * kstep, so); };
    LSTAMP(20);
    issue(0, 0);
    LSTAMP(21);
    helper_offsets();
    issue(1, SLOT);
    LSTAMP(22);
    if (isx) VMCNT(11); else VMCNT(12);
    LSTAMP(23);
    BAR();
    const bool helps = !isx && !(xg & 2) && hl != 3;
    if (helps) {
      constexpr int HL = (HELPN * 192 + 35) / 36;
      const int n = min(((pl * 3 + hl) * 2 + lid - 2) * HL + min(lane, HL - 1), HELPN * 192 - 1), row = n / 3;
      warm_line(lbase + L_UPD, (const char*)P.wqkv + (size_t)((u32)((HELPK >> (4 * (row >> 6))) & 15) * 64 + (row & 63)) * 6144 + (n % 3) * 2048 + (hp | 3) * 128);
    }
    issue(2, 2 * SLOT);
    u32 so = 0, sdum = 0;
    const int sn = min((pl * 4 + hl) * 4 + lid, 95) * 2;
    const u32 so0 = (sn / 3) * 6144 + (sn % 3) * 2048, so1 = ((sn + 1) / 3) * 6144 + ((sn + 1) % 3) * 2048;
    const char* sbase = (const char*)P.wqkv + (hp | 3) * 128;
    _Pragma("nounroll") for (int s = 0; s < 13; ++s) {
      if (helps && s == 0) VMCNT(13); else if (!isx || s == WARM_AT + 1 || s == WARM_AT + 2) VMCNT(12); else VMCNT(11);
      if (s == 0) LSTAMP(24);
      if (s == 1) LSTAMP(25);
      if (s == 2) LSTAMP(26);
      BAR();
      issue(s + 3, so);
      if (!(xg & 2) && (u32)(s - RS0) < RSN) {
        const u32 ko = (u32)((ROLLK >> (4 * (s - RS0))) & 15) * (64u * 6144u), a0 = ko + so0, a1 = ko + so1;
        asm volatile("s_load_dword %0, %1, %2\n\ts_load_dword %0, %1, %2 offset:64\n\ts_load_dword %0, %1, %3\n\ts_load_dword %0, %1, %3 offset:64"
                     : "+s"(sdum) : "s"(sbase), "s"(a0), "s"(a1) : "memory");
      }
      if (s == WARM_AT && isx) warm_line(lbase + L_UPD, (const char*)P.wo + (size_t)min((pan * 16 + hp) * 80 + lid * 40 + min(lane, 39), 16383) * 128);
      so = (so == 2 * SLOT) ? 0 : so + SLOT;
    }
    asm volatile("s_waitcnt lgkmcnt(0)" : "+s"(sdum));
    STAMP(2);
    helper_fill();
  }
  if (wave < 4) {
    __builtin_amdgcn_s_setprio(1);
    if (wave == 1) lds_wr(L, L_ONE + lane * 4, lane < 32 ? 0x3f803f80u : 0u);
    if (wave == 2) lds_wr(L, L_GO, 0u);
    f32x4 acc[6][6];
#pragma unroll
    for (int w = 0; w < 6; ++w)
#pragma unroll
      for (int t = (w & 1); t < 6; ++t) { acc[w][t] = f32x4{0.f, 0.f, 0.f, 0.f}; PIN(acc[w][t]); }
    f32x4 bprev, bhalo;
    u32 mc[2];
    u64 cur[4];
#pragma unroll
    for (int r = 0; r < 4; ++r) {
      const int kap = 4 * g + r, d = 16 + (wave & 1) + 2 * i - kap;
      bprev[r] = (kap - i >= 9) ? 0.f : -1e30f;
      bhalo[r] = (((d & 1) == 0) & (d <= 14) & (pan > 0)) ? 0.f : -1e30f;
      cur[r] = __ballot((u32)(i - kap) <= 7u);
      asm volatile("" : "+s"(cur[r]));
    }
    mc[0] = (((u32)(i - 4 * g) <= 7u) ? 0xffffu : 0u) | (((u32)(i - 4 * g - 1) <= 7u) ? 0xffff0000u : 0u);
    mc[1] = (((u32)(i - 4 * g - 2) <= 7u) ? 0xffffu : 0u) | (((u32)(i - 4 * g - 3) <= 7u) ? 0xffff0000u : 0u);
    u32 hxl = lbase + L_HX + (wave & 2) * 2304 + lane * 16;
    u32 obase = WS_O + (((wave & 1) * 32 + hp * 2 + (wave >> 1)) * 64 + pan * 5) * 1024 + lane * 16;
    PIN(bprev); PIN(bhalo); PIN(mc[0]); PIN(mc[1]); PIN(hxl); PIN(obase);
    SB();
    {
      const int tok = pan2 * 128 + cg * 8 + wave * 2 + (lane >> 5);
      u32x4 xv[4], wv[4];
      VMCNT(0);
      {
        LAS char* sp = L + 2 * SLOT + wave * 6144 + (lane & 31) * 16;
#pragma unroll
        for (int j = 0; j < 4; ++j) { xv[j] = *(LAS u32x4*)(sp + (lane >> 5) * 2048 + j * 512); wv[j] = *(LAS u32x4*)(sp + 4096 + j * 512); }
      }
      asm volatile("s_waitcnt lgkmcnt(0)" : "+s"(ev) :: "memory");
      const u32 e = ev;
      tag = e * 0x9E3779B1u + 0x7F4A7C15u; tag ^= tag >> 15; tag = (u32)rfl((int)(tag ^ (u32)(tag == e)));
      if (wave == 1) lds_wr(L, L_TAG, tag);
      float sx = 0.f, sxx = 0.f, sxw = 0.f, sw = 0.f;
#pragma unroll
      for (int j = 0; j < 4; ++j)
#pragma unroll
        for (int e2 = 0; e2 < 4; ++e2) {
          const u32 a = xv[j][e2], b = wv[j][e2];
          sx = dot2(a, 0x3f803f80u, sx); sxx = dot2(a, a, sxx); sxw = dot2(a, b, sxw); sw = dot2(b, 0x3f803f80u, sw);
        }
      const float mean = half_sum(sx) * (1.f / D_);
      const float var = fmaxf(half_sum(sxx) * (1.f / D_) - mean * mean, 0.f);
      const float mb = rbf(mean), sb = rbf(__builtin_amdgcn_sqrtf(rbf(rbf(var) + 1e-5f)));
      const u32 ro = ord16(pack2((half_sum(sxw) - mb * half_sum(sw)) * __builtin_amdgcn_rcpf(sb), 0.f) & 0xffffu);
      const u32 rlo = (u32)__builtin_amdgcn_readlane((int)ro, 31);
      if ((lane & 31) == 31) st_sc1_64(u32x2{pack2(mb, sb), tag}, rsWS, WS_SG + tok * 8);
      if (lane == 63) st_sc1_64(u32x2{rlo | (ro << 16), tag}, rsWS, WS_RG + (tok >> 1) * 8);
      if (!gemm) { LGKM0(); BAR(); }
    }
    STAMP(1);
    if (gemm) {
      const int h = wave >> 1, par = wave & 1;
        const int q4 = i >> 2, p4 = i & 3;
        u32 a0 = lbase + i * 128 + ((g ^ (i >> 1)) << 4);
        const u32 wsw = ((q4 >> 1) & 1) | ((g & 1) << 1);
        u32 w0 = lbase + WOFF + (8 * g + q4) * 128 + (((2 * h + par) ^ wsw) << 5) + p4 * 8;
        const u32 tb = (1 + 5 * par) * 2048;
        const u32 one = lbase + L_ONE;
        u32 pH = (i == 0) ? one : a0, pT = a0 + tb, pW0 = w0, pW1 = w0 ^ 32;
        PIN(a0); PIN(w0); PIN(pH); PIN(pT); PIN(pW0); PIN(pW1);
        Frags f0, f1;
        const u32 e1 = (i == 0) ? 0x3f803f80u : 0u;
        s16x8 e0 = as_frag(u32x4{e1, e1, e1, e1});
        PIN(e0);
        __builtin_amdgcn_s_waitcnt(0xc07f);
        BAR();
        STAMP(2); CSTAMP(14);
        load_frags<0>(f0, LP(pH), LP(pT), LP(pW0), LP(pW1));
        pH ^= 64; pT ^= 64;
        __builtin_amdgcn_s_waitcnt(0xc07f);
        u32 so = 0, trc = 0;
        auto half1 = [&]() {
          SB();
          load_frags<1>(f1, LP(pH), LP(pT), LP(pW0), LP(pW1));
          mma_range<0, 24>(acc, f0, e0);
          interleave();
          SB();
          so = (so == 2 * SLOT) ? 0 : so + SLOT;
          mma_range<24, 25>(acc, f0, e0);
          SB();
          pT = so + a0; pW0 = so + w0; PIN(pT); PIN(pW0);
          mma_range<25, 26>(acc, f0, e0);
          SB();
          pH = (i == 0) ? one : pT; pW1 = pW0 ^ 32; PIN(pH); PIN(pW1);
          mma_range<26, 27>(acc, f0, e0);
          SB();
          pT += tb; PIN(pT);
          mma_range<27, 28>(acc, f0, e0);
          SB();
        };
        auto half1b = [&](int s) {
          const u64 tnow = TICK(), rnow = RT();
          mma_range<28, 30>(acc, f0, e0);
          SB();
          __builtin_amdgcn_s_waitcnt(0xc07f);
          if (STAMPS) { trc = (lane == s) ? (u32)tnow : trc; trc = (lane == s + 16) ? (u32)rnow : trc; }
          mma_range<30, 31>(acc, f0, e0);
          SB();
          BAR();
          SB();
          mma_range<31, 33>(acc, f0, e0);
          SB();
        };
        half1();
        for (int s = 0; s < 15; ++s) {
          half1b(s);
          load_frags<0>(f0, LP(pH), LP(pT), LP(pW0), LP(pW1));
          mma_range<0, 24>(acc, f1, e0);
          interleave();
          SB();
          pH ^= 64; pT ^= 64; PIN(pH); PIN(pT);
          mma_range<24, 31>(acc, f1, e0);
          SB();
          __builtin_amdgcn_s_waitcnt(0xc07f);
          mma_range<31, 33>(acc, f1, e0);
          half1();
        }
        half1b(15);
        if (STAMPS && lane < 32) ((GAS u32*)(P.ws + WS_STAMP + (1 << 20)))[(bid * 4 + wave) * 32 + lane] = trc;
      mma_range<5, 6>(acc, f1, e0); mma_range<16, 17>(acc, f1, e0); mma_range<27, 28>(acc, f1, e0);
      SB();
      mma_range<11, 16>(acc, f1, e0);
      u32 tz = threadIdx.x;
      PIN(tz);
      const u32 i = tz & 15, g = (tz >> 4) & 3;
      u32 sqi = lbase + L_STQ + i * 4, sqg = lbase + L_STQ + g * 16;
      PIN(sqi); PIN(sqg);
      const u32 hx = hxl + par * 2304, hy = hxl + (par ^ 1) * 2304;
      u32 hxg = hx - i * 16, hyg = hy - i * 16, hxi = hx - g * 256, hyi = hy - g * 256, sqi2 = sqi + par * 320, sqg2 = sqg + par * 320;
      const u32 hqa = hx + 2048 - g * 240, hqb = hxl + (L_UPD - L_HX) - (wave & 2) * 2304;
      u32 hq = __builtin_amdgcn_inverse_ballot_w64(0x0001000100010001ull) ? hqa : hqb;
      PIN(hxg); PIN(hyg); PIN(hxi); PIN(hyi); PIN(sqi2); PIN(sqg2); PIN(hq);
#pragma unroll
      for (int k = 0; k < 5; ++k) { __builtin_amdgcn_sched_group_barrier(0x008, 1, 0); __builtin_amdgcn_sched_group_barrier(0x002, 2, 0); }
      SB();
      *(LAS f32x4*)LP(hx) = acc[2][0];
      *(LAS f32x4*)LP(hx + 1024) = acc[4][0];
      *(LAS f32x4*)LP(hq) = acc[0][0];
      mma_range<17, 22>(acc, f1, e0);
#pragma unroll
      for (int k = 0; k < 3; ++k) { __builtin_amdgcn_sched_group_barrier(0x008, 1, 0); __builtin_amdgcn_sched_group_barrier(0x200, 1, 0); }
      SB();
      mma_range<0, 3>(acc, f1, e0);
      SB();
      __builtin_amdgcn_s_waitcnt(0xc07f);
      BAR();
      SB();
      f32x4 cs[4];
      cs[2] = *(LAS f32x4*)LP(hxg); cs[3] = *(LAS f32x4*)LP(hyg);
      cs[0] = *(LAS f32x4*)LP(hxg + 2048 - g * 240); cs[1] = *(LAS f32x4*)LP(hyg + 2048 - g * 240);
      float m[6], rs[6];
#pragma unroll
      for (int t = 0; t < 6; ++t) { m[t] = *(LAS float*)LP((t ? sqi2 : sqi) + t * 64); rs[t] = *(LAS float*)LP((t ? sqi2 : sqi) + t * 64 + 704); }
      acc[3][0] = *(LAS f32x4*)LP(hy);
      const float cv[2] = {*(LAS float*)LP(hxi + 1024), *(LAS float*)LP(hyi + 1024)};
      acc[5][0] = *(LAS f32x4*)LP(hy + 1024);
      mma_range<3, 5>(acc, f1, e0); mma_range<6, 11>(acc, f1, e0);
#pragma unroll
      for (int k = 0; k < 7; ++k) { __builtin_amdgcn_sched_group_barrier(0x008, 1, 0); __builtin_amdgcn_sched_group_barrier(0x100, 2, 0); }
      SB();
      STAMP(3); CSTAMP(15);
      const float QS = 0.17677669529663687f * 1.4426950408889634f;
      const f32x4 z4 = {0.f, 0.f, 0.f, 0.f};
      f32x4 s0[5], s1[5], m4[6], r4[6];
      {
        s16x8 kf[6];
#pragma unroll
        for (int t = 5; t >= 0; --t) {
          const f32x4 k0 = (acc[2][t] + m[t] * cs[2]) * rs[t], k1 = (acc[3][t] + m[t] * cs[3]) * rs[t];
          kf[t] = as_frag(u32x4{pack2(k0[0], k0[1]), pack2(k0[2], k0[3]), pack2(k1[0], k1[1]), pack2(k1[2], k1[3])});
        }
        mma_range<22, 27>(acc, f1, e0); mma_range<28, 33>(acc, f1, e0);
#pragma unroll
        for (int k = 0; k < 10; ++k) { __builtin_amdgcn_sched_group_barrier(0x008, 1, 0); __builtin_amdgcn_sched_group_barrier(0x002, 3, 0); }
        SB();
#pragma unroll
        for (int t = 0; t < 6; ++t) { m4[t] = *(LAS f32x4*)LP((t ? sqg2 : sqg) + t * 64); r4[t] = *(LAS f32x4*)LP((t ? sqg2 : sqg) + t * 64 + 704); }
#pragma unroll
        for (int t = 0; t < 5; ++t) {
          const f32x4 q0 = acc[0][t + 1] + m[t + 1] * cs[0], q1 = acc[1][t + 1] + m[t + 1] * cs[1];
          const s16x8 qf = as_frag(u32x4{pack2(q0[0], q0[1]), pack2(q0[2], q0[3]), pack2(q1[0], q1[1]), pack2(q1[2], q1[3])});
          s0[t] = mfma(kf[t], qf, t == 0 ? bhalo : bprev); s1[t] = mfma(kf[t + 1], qf, z4);
        }
      }
      SB();
      u32x16 vp[2];
#pragma unroll
      for (int t = 0; t < 6; ++t) {
#pragma unroll
        for (int c = 0; c < 2; ++c) {
          const f32x4 v = (acc[4 + c][t] + m4[t] * cv[c]) * r4[t];
          vp[c][2 * t] = pack2(v[0], v[1]); vp[c][2 * t + 1] = pack2(v[2], v[3]);
        }
      }
      PIN(vp[0]); PIN(vp[1]);
      float l[5][4], mx[5], rq[5];
#pragma unroll
      for (int t = 0; t < 5; ++t) {
#pragma unroll
        for (int r = 0; r < 4; ++r) l[t][r] = __builtin_amdgcn_inverse_ballot_w64(cur[r]) ? s1[t][r] : s0[t][r];
        mx[t] = vmax3(vmax(l[t][0], l[t][1]), l[t][2], l[t][3]);
        rq[t] = rs[t + 1] * QS;
      }
#pragma unroll
      for (int t = 0; t < 4; t += 2) {
        const auto r = __builtin_amdgcn_permlane16_swap(__float_as_uint(mx[t]), __float_as_uint(mx[t + 1]), false, false);
        const u32 r0 = r[0], r1 = r[1];
        const float z = max32(vmax(__uint_as_float(r0), __uint_as_float(r1)));
        const auto b = __builtin_amdgcn_permlane16_swap(__float_as_uint(z), __float_as_uint(z), false, false);
        const u32 b0 = b[0], b1 = b[1];
        mx[t] = -__uint_as_float(b0) * rq[t]; mx[t + 1] = -__uint_as_float(b1) * rq[t + 1];
      }
      mx[4] = -max32(max16(mx[4])) * rq[4];
      const s16x8 ones = as_frag(u32x4{0x3f803f80u, 0x3f803f80u, 0x3f803f80u, 0x3f803f80u});
      f32x4 sm[5], o0[5], o1[5];
#pragma unroll
      for (int t = 0; t < 5; ++t) {
#pragma unroll
        for (int r = 0; r < 4; ++r) l[t][r] = __builtin_amdgcn_exp2f(__builtin_fmaf(l[t][r], rq[t], mx[t]));
        const u32 p01 = pack2(l[t][0], l[t][1]), p23 = pack2(l[t][2], l[t][3]);
        const s16x8 pp = as_frag(u32x4{p01 & ~mc[0], p23 & ~mc[1], p01 & mc[0], p23 & mc[1]});
        sm[t] = mfma(ones, pp, z4);
        o0[t] = mfma(as_frag(u32x4{vp[0][2 * t], vp[0][2 * t + 1], vp[0][2 * t + 2], vp[0][2 * t + 3]}), pp, z4);
        o1[t] = mfma(as_frag(u32x4{vp[1][2 * t], vp[1][2 * t + 1], vp[1][2 * t + 2], vp[1][2 * t + 3]}), pp, z4);
      }
#pragma unroll
      for (int t = 0; t < 5; ++t) {
        const float inv = __builtin_amdgcn_rcpf(sm[t][0]);
        o0[t] *= inv; o1[t] *= inv;
        st_sc1_128(u32x4{pack2(o0[t][0], o0[t][1]), pack2(o0[t][2], o0[t][3]), pack2(o1[t][0], o1[t][1]), pack2(o1[t][2], o1[t][3])}, rsWS,
                   (t == 4) ? obase + ((pan == 12) ? 0x40000000u : 4096u) : obase + t * 1024);
      }
      STAMP(8);
    }
  }
  if (!gemm) {
  if (dwn) {
    const rsrc_t rsD = mk_rsrc(P.wd, F_ * D_ * 2);
    const int k0 = (du >> 1) * 14 + (wave >> 2) * 7;
    const u32 vd = (g * D_ + (du & 1) * 512 + (wave & 3) * 128 + 8 * i) * 2;
    auto drow = [&](int q, int j) -> u32 { return (u32)(64 * ((k0 + q) >> 1) + 4 * ((k0 + q) & 1) + 8 * j) * (D_ * 2); };
    constexpr u32 D_W = 24 * 4096, D_H = D_W + 8 * 4352;
    u32x4 wb[56];
#pragma unroll
    for (int j = 0; j < 8; ++j) wb[j] = ld_nt_128(rsD, vd, drow(0, j));
    SB();
    if (wave >= 4) { BAR(); tag = (u32)rfl((int)lds_rd(L, L_TAG)); }
#pragma unroll
    for (int j = 8; j < 48; ++j) wb[j] = ld_nt_128(rsD, vd, drow(j >> 3, j & 7));
    SB();
    transpose_grp(wb);
#pragma unroll
    for (int e = 0; e < 4; ++e) *(LAS u32x4*)(L + D_W + wave * 4352 + e * 1024 + lane * 16) = wb[e];
    SB();
#pragma unroll
    for (int q = 1; q < 6; ++q) transpose_grp(wb + 8 * q);
#pragma unroll
    for (int j = 48; j < 56; ++j) wb[j] = ld_nt_128(rsD, vd, drow(6, j & 7));
    SB();
    __builtin_amdgcn_s_setprio(0);
    if (wave >= 4) { helper_offsets(); helper_fill(); }
    transpose_grp(wb + 48);
    VMCNT(0);
    STAMP(12);
    BAR();
    STAMP(11);
    {
      wait_until(tstart, T_H);
      const int qa = 2 * (wave & 3), qb = min(qa + 1, 6);
      u32x4 va, vb;
      _Pragma("nounroll") for (int sp = 0; sp < SPIN_CAP; ++sp) {
        va = ld_sc1_128(rsWS, lane * 16, WS_H + (k0 + qa) * 1024);
        vb = ld_sc1_128(rsWS, lane * 16, WS_H + (k0 + qb) * 1024);
        if (__all(va.y == tag && va.w == tag && vb.y == tag && vb.w == tag)) break;
        __builtin_amdgcn_s_sleep(1);
      }
      *(LAS u32x2*)(L + D_H + ((wave >> 2) * 7 + qa) * 512 + (lane & 31) * 16 + (lane >> 5) * 8) = u32x2{va.x, va.z};
      *(LAS u32x2*)(L + D_H + ((wave >> 2) * 7 + qb) * 512 + (lane & 31) * 16 + (lane >> 5) * 8) = u32x2{vb.x, vb.z};
      LGKM0();
      lds_wr(L, L_HR + wave * 4, 1);
      lds_wait_set(L, L_HR + (wave & 4) * 4 + (lane & 3) * 4);
    }
    STAMP(2);
    f32x4 ga[8];
#pragma unroll
    for (int e = 0; e < 8; ++e) ga[e] = f32x4{0.f, 0.f, 0.f, 0.f};
    {
      s16x8 hf = *(LAS s16x8*)(L + D_H + ((wave >> 2) * 7 + 6) * 512 + (g * 8 + (i & 7)) * 16);
#pragma unroll
      for (int q = 6; q >= 0; --q) {
        s16x8 hn = hf;
        if (q > 0) hn = *(LAS s16x8*)(L + D_H + ((wave >> 2) * 7 + q - 1) * 512 + (g * 8 + (i & 7)) * 16);
        mma_frags8(ga, wb + q * 8, hf);
        if (q == 5) {
#pragma unroll
          for (int e = 0; e < 4; ++e) wb[e] = *(LAS u32x4*)(L + D_W + wave * 4352 + e * 1024 + lane * 16);
        }
        SB();
        hf = hn;
      }
    }
    STAMP(4);
    {
      u32 ln = threadIdx.x & 63;
      PIN(ln);
      if ((ln & 15) < 8) {
        const u32 o = D_W + wave * 4352 + (ln & 7) * 64 + (((ln >> 4) ^ ((ln >> 1) & 3)) << 4);
#pragma unroll
        for (int e = 0; e < 8; ++e) *(LAS f32x4*)(L + o + e * 512) = ga[e];
      }
    }
    LGKM0();
    lds_wr(L, L_PW + 4 * wave, 1);
    if (wave >= 4) {
      {
        u32 ln = threadIdx.x & 63;
        PIN(ln);
        const u32 gq = 2 * (ln >> 5) + ((ln >> 1) & 1), o = lbase + D_W + (wave - 4) * 4352 + ((ln >> 2) & 7) * 512 + (ln & 1) * 8;
        u32 a[4], oy = WS_YS + (du >> 1) * 65536 + (du & 1) * 4096 + (wave - 4) * 1024 + ln * 16;
#pragma unroll
        for (int k = 0; k < 4; ++k) { a[k] = o + k * 128 + ((gq ^ k) << 4); PIN(a[k]); }
        PIN(oy);
        lds_wait_set(L, L_PW + (wave - 4) * 4);
        f32x2 p[8], q[8];
#pragma unroll
        for (int tk = 0; tk < 8; ++tk) { p[tk] = *(LAS f32x2*)(LP(a[tk >> 1]) + (tk & 1) * 64); q[tk] = *(LAS f32x2*)(LP(a[tk >> 1]) + (tk & 1) * 64 + 4 * 4352); }
        SB();
#pragma unroll
        for (int tk = 0; tk < 8; ++tk) {
          const f32x2 y = p[tk] + q[tk];
          st_sc1_128(u32x4{__float_as_uint(y.x), tag, __float_as_uint(y.y), tag}, rsWS, oy + tk * 8192);
        }
      }
      LGKM0();
      lds_wr(L, L_HS + 4 * (wave - 4), 1);
      lds_wait_set(L, L_HS + (lane & 3) * 4);
      u32 ln = threadIdx.x & 63;
      PIN(ln);
      vO = (ln >> 3) * 2048 + (((ln & 7) ^ (((ln >> 4) & 3) << 1)) << 4) + cg * 128;
      wo_grp(20 + wave); wo_grp(24 + wave);
      VMCNT(0);
      lds_wr(L, L_HS + 4 * (wave - 4), 2);
    }
    STAMP(8);
  } else {
    const int lr = lane >> 3, lc = lane & 7;
    const int fv = fu - (fu > 2);
    const bool two = fu != 2 && fv < 24;
    const int la = (fu == 2) ? 50 : (fv >= 24) ? (fv - 24) * 8 + 3 : (fv >= 21) ? (fv - 21) * 16 + 2 : (fv / 3) * 8 + ((fv % 3) ? 2 + 2 * (fv % 3) : 0);
    const int lb = la + ((fv >= 21) ? 8 : 1);
    const rsrc_t rsG = mk_rsrc(P.wg, D_ * F_ * 2), rsU = mk_rsrc(P.wu, D_ * F_ * 2), rsX = mk_rsrc(P.x, S_ * D_ * 2);
    const u32 vg = (i < 8 || two) ? ((wave * 128 + 8 * g) * F_ + ((i < 8) ? la : lb) * 64 + 8 * (i & 7)) * 2 : 0x7f000000u;
    u32x4 wb[8 * GWIN];
    auto ldgrp = [&](int q) {
#pragma unroll
      for (int j = 0; j < 8; ++j) wb[(q % GWIN) * 8 + j] = ld_nt_128((q & 1) ? rsU : rsG, vg, ((q >> 1) * 32 + j) * (F_ * 2));
      SB();
    };
    __builtin_amdgcn_s_setprio(0);
    ldgrp(0);
    ldgrp(1);
    if (wave >= 4) { BAR(); tag = (u32)rfl((int)lds_rd(L, L_TAG)); }
    if (fu == 2) {
      *(LAS u32x4*)(L + 32 + threadIdx.x * 16) = u32x4{0u, 0u, 0u, 0u};
      if (threadIdx.x < 4) *(LAS u32x4*)(L + 32 + 8192 + threadIdx.x * 16) = u32x4{0u, 0u, 0u, 0u};
      wait_until(tstart, T_GATHER);
      u32x4 v;
      _Pragma("nounroll") for (int sp = 0; sp < SPIN_CAP; ++sp) {
        v = ld_sc1_128(rsWS, lane * 16, WS_RG + wave * 1024);
        if (__all(v.y == tag && v.w == tag)) break;
        __builtin_amdgcn_s_sleep(2);
      }
      const u32 ib = 2047 - 256 * wave - 4 * lane;
      const u32 key[4] = {(v.x << 16) | ib, (v.x & 0xffff0000u) | (ib - 1), (v.z << 16) | (ib - 2), (v.z & 0xffff0000u) | (ib - 3)};
      lds_wr(L, wave * 4, wave_max_u32(max(max(key[0], key[1]), max(key[2], key[3]))));
      LGKM0();
      BAR();
      STAMP(9);
      const u32x4 m0 = *(LAS u32x4*)L, m1 = *(LAS u32x4*)(L + 16);
      const u32 thr = min(min(min(m0.x, m0.y), min(m0.z, m0.w)), min(min(m1.x, m1.y), min(m1.z, m1.w)));
      const u32 c0 = key[0] >= thr, c1 = key[1] >= thr, c2 = key[2] >= thr, c3 = key[3] >= thr;
      if (c0 | c1 | c2 | c3) {
        const u32 p = 64 + 4 * __hip_atomic_fetch_add((LAS u32*)(L + 32), c0 + c1 + c2 + c3, __ATOMIC_RELAXED, __HIP_MEMORY_SCOPE_WORKGROUP);
        lds_wr(L, c0 ? p : 36, key[0]); lds_wr(L, c1 ? p + 4 * c0 : 36, key[1]);
        lds_wr(L, c2 ? p + 4 * (c0 + c1) : 36, key[2]); lds_wr(L, c3 ? p + 4 * (c0 + c1 + c2) : 36, key[3]);
      }
      LGKM0();
      BAR();
      const int n = rfl((int)lds_rd(L, 32));
      _Pragma("nounroll") for (int b = threadIdx.x; b < n; b += 512) {
        const u32 my = lds_rd(L, 64 + 4 * b);
        int rank = 0;
        _Pragma("nounroll") for (int t = 0; t < n; t += 8) {
          const u32x4 o = *(LAS u32x4*)(L + 64 + 4 * t), q = *(LAS u32x4*)(L + 80 + 4 * t);
          rank += (o.x > my) + (o.y > my) + (o.z > my) + (o.w > my) + (q.x > my) + (q.y > my) + (q.z > my) + (q.w > my);
        }
        if (rank < 8) st_sc1_64(u32x2{my ^ 0x7ffu, tag}, rsWS, WS_SEL + rank * 8);
      }
      if (threadIdx.x == 0) st_sc1_32(tag, rsWS, WS_EPOCH);
      STAMP(10);
      LGKM0();
      BAR();
    }
    constexpr u32 F_XS = 81920;
    ldgrp(2);
    if (wave == 0) {
      wait_until(tstart, T_SEL);
      u32x2 sk;
      _Pragma("nounroll") for (int sp = 0; sp < SPIN_CAP; ++sp) {
        sk = ld_sc1_64(rsWS, lr * 8, WS_SEL);
        if (__all(sk.y == tag)) break;
        __builtin_amdgcn_s_sleep(4);
      }
      STAMP(2);
      u32x4 xr[16];
#pragma unroll
      for (int j = 0; j < 16; ++j) xr[j] = __builtin_amdgcn_raw_buffer_load_b128(rsX, (sk.x & 0x7ffu) * 2048 + lc * 16, j * 128, 0);
      u32x2 ms;
      _Pragma("nounroll") for (int sp = 0; sp < SPIN_CAP; ++sp) {
        ms = ld_sc1_64(rsWS, (sk.x & 0x7ffu) * 8, WS_SG);
        if (__all(ms.y == tag)) break;
      }
#pragma unroll
      for (int j = 0; j < 16; ++j) *(LAS u32x4*)(L + F_XS + lr * 2064 + j * 128 + lc * 16) = xr[j];
      *(LAS u32*)(L + F_XS + 8 * 2064 + lr * 4) = ms.x;
      LGKM0();
      lds_wr(L, L_HR, 1);
    }
#pragma unroll
    for (int q = 3; q < GWIN; ++q) ldgrp(q);
#pragma unroll
    for (int q = 0; q < GPRE; ++q) transpose_grp(wb + 8 * q);
    lds_wait_set(L, L_HR);
    f32x4 ga[16];
#pragma unroll
    for (int e = 0; e < 16; ++e) ga[e] = f32x4{0.f, 0.f, 0.f, 0.f};
    const u32 ms = *(LAS u32*)(L + F_XS + 8 * 2064 + (i & 7) * 4);
    const float rsn = __builtin_amdgcn_rcpf(bf_hi(ms)), mrs = -bf_lo(ms) * rsn;
    s16x8 xf;
#pragma unroll
    for (int q = 0; q < 8; ++q) {
      if ((q & 1) == 0) {
        const u32x4 xv = *(LAS u32x4*)(L + F_XS + (i & 7) * 2064 + (wave * 128 + (q >> 1) * 32 + 8 * g) * 2);
        u32x4 xn;
#pragma unroll
        for (int e = 0; e < 4; ++e) xn[e] = pack2(__builtin_fmaf(bf_lo(xv[e]), rsn, mrs), __builtin_fmaf(bf_hi(xv[e]), rsn, mrs));
        xf = as_frag(xn);
      }
      if (q < GPRE) mma_frags8(ga + 8 * (q & 1), wb + q * 8, xf); else mma_cols(ga + 8 * (q & 1), wb + (q % GWIN) * 8, xf);
      SB();
      if (q == 1 || q == 2) ldgrp(q + 5);
    }
    STAMP(4);
    {
      u32 ln = threadIdx.x & 63;
      PIN(ln);
      if ((ln & 15) < 8) {
        const u32 o = wave * 10240 + (ln & 7) * 80 + (ln >> 4) * 16;
#pragma unroll
        for (int e = 0; e < 16; ++e) *(LAS f32x4*)(L + o + e * 640) = ga[e];
      }
    }
    {
      u32 ln = threadIdx.x & 63;
      PIN(ln);
      const u32 e = 4 * ((wave >> 1) & 1) + (ln >> 4), tk = (ln >> 1) & 7, m = 8 * (wave >> 2) + 4 * (wave & 1) + 2 * (ln & 1);
      u32 o0 = lbase + (e * 8 + tk) * 80 + m * 4, o1 = o0 + 40960, oh = WS_H + ((wave < 4) ? la : lb) * 2048 + (wave & 3) * 512 + ln * 8;
      PIN(o0); PIN(o1); PIN(oh);
      LGKM0();
      BAR();
      f32x2 pg[8], pu[8];
#pragma unroll
      for (int w = 0; w < 8; ++w) { pg[w] = *(LAS f32x2*)(LP((w < 4) ? o0 : o1) + (w & 3) * 10240); pu[w] = *(LAS f32x2*)(LP((w < 4) ? o0 : o1) + (w & 3) * 10240 + 5120); }
      SB();
      const f32x2 gs = ((pg[0] + pg[1]) + (pg[2] + pg[3])) + ((pg[4] + pg[5]) + (pg[6] + pg[7])), us = ((pu[0] + pu[1]) + (pu[2] + pu[3])) + ((pu[4] + pu[5]) + (pu[6] + pu[7]));
      const float g0 = rbf(gs.x), g1 = rbf(gs.y);
      const u32 hv = pack2(rbf(g0 * sigm(g0)) * rbf(us.x), rbf(g1 * sigm(g1)) * rbf(us.y));
      if (wave < 4 || two) st_sc1_64(u32x2{hv, tag}, rsWS, oh);
    }
    LGKM0();
    BAR();
    STAMP(8);
    if (wave >= 4) { helper_offsets(); helper_fill(); }
  }
  }

  if (wave >= 4) {
    const int lid = wave - 4;
    if (!dwn) {
      VMCNT(0);
      STAMP(3);
      BAR();
    }
    if (lid >= 2) {
      const u32 sk = lds_rd(L, L_SEL + 4 * (lane & 7));
      u32 hits = 0;
#pragma unroll
      for (int k = 0; k < 4; ++k) hits |= (u32)((((u32)__builtin_amdgcn_readlane((int)sk, lid - 2 + 2 * k) & 0x7ffu) >> 7) == (u32)pan2) << k;
      if (hits) {
        u32 tgo = 0;
        _Pragma("nounroll") for (int sp = 0; sp < SPIN_CAP; ++sp) { tgo = (u32)rfl((int)lds_rd(L, L_GO)); if (tgo) break; __builtin_amdgcn_s_sleep(4); }
        while ((int)((u32)__builtin_amdgcn_s_memrealtime() - tgo) < T_Y) __builtin_amdgcn_s_sleep(4);
        u32x4 v[4][4];
        u32 todo = hits;
        _Pragma("nounroll") for (int sp = 0; sp < SPIN_CAP; ++sp) {
#pragma unroll
          for (int k = 0; k < 4; ++k)
            if ((todo >> k) & 1) {
#pragma unroll
              for (int j = 0; j < 4; ++j) v[k][j] = ld_sc1_128(rsWS, (lane >> 5) * 65536 + (lane & 31) * 16, WS_YS + j * 131072 + (lid - 2 + 2 * k) * 8192 + cg * 512);
            }
#pragma unroll
          for (int k = 0; k < 4; ++k)
            if ((todo >> k) & 1) {
              u32 bad = 0;
              float y0 = 0.f, y1 = 0.f;
#pragma unroll
              for (int j = 0; j < 4; ++j) { bad |= (v[k][j].y ^ tag) | (v[k][j].w ^ tag); y0 += __uint_as_float(v[k][j].x); y1 += __uint_as_float(v[k][j].z); }
              if (__all(bad == 0)) {
                y0 = sum32(y0); y1 = sum32(y1);
                if (lane < 32) {
                  const u32 o = L_UPD + ((lid - 2 + 2 * k) * 64 + 16 * (lane & 3) + (lane >> 2)) * 4;
                  *(LAS float*)(L + o) = y0; *(LAS float*)(L + o + 32) = y1;
                }
                todo &= ~(1u << k);
              }
            }
          if (!todo) break;
          __builtin_amdgcn_s_sleep(8);
        }
      }
      LGKM0();
      lds_wr(L, L_UPDF + 4 * (lid - 2), 1);
      STAMP(7);
    }
    return;
  }

  {
    const bool pol = wave == 0;
    const u32 fo = WS_OF + (((pan2 * 4 + 3 * (lane >> 5)) / 5) * 16 + (lane & 15)) * 128;
    u32 fv = ~tag;
    if (dwn && pol) fv = ld_sc1_32(rsWS, fo, 0);
    SB();
    if (!gemm && !dwn) BAR();
    const int par2 = wave >> 1, t2 = (wave & 1) * 2;
    u32 wa[4], wc[4];
    s16x8 wf[3][4];
    auto wo_addr = [&]() {
      u32 ln = threadIdx.x & 63;
      PIN(ln);
      const u32 g = ln >> 4, i = ln & 15, q4 = i >> 2, p4 = i & 3;
      const u32 wb = lbase + (4 * g + q4) * 128 + p4 * 8 + par2 * 2048, wsw = (2 * g + (q4 >> 1)) & 3;
#pragma unroll
      for (int c = 0; c < 4; ++c) { wa[c] = wb + ((c ^ wsw) << 5); wc[c] = wa[c] ^ 2048; PIN(wa[c]); PIN(wc[c]); }
    };
    auto wf_first = [&]() {
#pragma unroll
      for (int k = 0; k < 2; ++k)
#pragma unroll
        for (int c = 0; c < 4; ++c) wf[k][c] = tr_frag(LP(wa[c]), LP(wc[c]), k * 4096, k * 4096);
    };
    if (gemm) oc_init();
    SB();
    if (gemm) {
      VMCNT(0);
      if (lane == 0 && __hip_atomic_fetch_add((LAS u32*)(L + L_ARR), 1u, __ATOMIC_RELAXED, __HIP_MEMORY_SCOPE_WORKGROUP) == 3)
        st_sc1_32(tag, rsWS, WS_OF + (pan * 16 + hp) * 128);
      STAMP(4);
      SB();
      wo_addr();
      BAR();
      wf_first();
    }
    const int T0 = pan2 * 4 + t2;
    const u32 sk = lds_rd(L, L_SEL + 4 * (lane & 7));
    __builtin_amdgcn_s_waitcnt(0xc07f);
    SB();
    if (!gemm) wait_until(tstart, T_O);
    if (pol) {
      _Pragma("nounroll") for (int sp = 0; sp < SPIN_CAP; ++sp) {
        if (__all(fv == tag)) break;
        if (sp) __builtin_amdgcn_s_sleep(2);
        fv = ld_sc1_32(rsWS, fo, 0);
      }
      lds_wr(L, L_GO, (u32)__builtin_amdgcn_s_memrealtime() | 1u);
    } else {
      _Pragma("nounroll") for (int sp = 0; sp < SPIN_CAP; ++sp) if (rfl((int)lds_rd(L, L_GO))) break;
    }
    CBAR();
    STAMP(5);
    const u32 abase = WS_O + (par2 * 32 * 64 + T0) * 1024;
    u32x4 af[OWIN][2];
#pragma unroll
    for (int hh = 0; hh < OWIN; ++hh) {
#pragma unroll
      for (int tt = 0; tt < 2; ++tt) af[hh][tt] = ld_sc1_128(rsWS, lane * 16 + tt * 1024, abase + ((hh + hrot) & 31) * 65536);
      SB();
    }
    if (!gemm) {
      wo_addr(); wf_first();
      SB();
      oc_init();
      __builtin_amdgcn_s_waitcnt(0xc07f);
      SB();
    }
#pragma unroll
    for (int hh = 0; hh < 32; ++hh) {
      if (hh == 21 && dwn) {
        _Pragma("nounroll") for (int sp = 0; sp < SPIN_CAP; ++sp) if (__all(lds_rd(L, L_HS + (lane & 3) * 4) == 2u)) break;
        CBAR(); SB();
      }
      if (hh == 14) {
#pragma unroll
        for (int c = 0; c < 4; ++c) { wa[c] += 65536; wc[c] += 65536; PIN(wa[c]); PIN(wc[c]); }
        SB();
      }
      if (hh == 31) __builtin_amdgcn_s_waitcnt(0xc07f); else __builtin_amdgcn_s_waitcnt(0xc87f);
      SB();
      if (hh + 2 < 32) {
#pragma unroll
        for (int c = 0; c < 4; ++c) wf[(hh + 2) % 3][c] = tr_frag(LP(wa[c]), LP(wc[c]), ((hh + 2) & 15) * 4096, ((hh + 2) & 15) * 4096);
      }
#pragma unroll
      for (int tt = 0; tt < 2; ++tt) {
#pragma unroll
        for (int c = 0; c < 4; ++c) oc[tt][c] = mfma(wf[hh % 3][c], as_frag(af[hh % OWIN][tt]), oc[tt][c]);
        if (hh + OWIN < 32) af[hh % OWIN][tt] = ld_sc1_128(rsWS, lane * 16 + tt * 1024, abase + ((hh + OWIN + hrot) & 31) * 65536);
      }
#pragma unroll
      for (int k = 0; k < 2; ++k) {
        __builtin_amdgcn_sched_group_barrier(0x008, 1, 0); __builtin_amdgcn_sched_group_barrier(0x100, 2, 0);
        __builtin_amdgcn_sched_group_barrier(0x008, 1, 0); __builtin_amdgcn_sched_group_barrier(0x100, 1, 0);
        __builtin_amdgcn_sched_group_barrier(0x008, 1, 0); __builtin_amdgcn_sched_group_barrier(0x100, 1, 0);
        __builtin_amdgcn_sched_group_barrier(0x008, 1, 0); __builtin_amdgcn_sched_group_barrier(0x020, 1, 0);
      }
      SB();
    }
    STAMP(6);
    {
      const u32 stl = (sk & 0x7ffu) - (u32)(pan2 * 128 + par2 + 32 * t2);
      u64 hit = __ballot(stl < 64u && (stl & 1u) == 0u) & 0xffull;
      if (hit) {
        lds_wait_set(L, L_UPDF + (lane & 1) * 4);
        while (hit) {
          const int c = __builtin_ctzll(hit);
          hit &= hit - 1;
          const u32 tl = (u32)__builtin_amdgcn_readlane((int)stl, c), ob = (u32)__builtin_amdgcn_readlane((int)sk, c) >> 16;
          const float sg = rbf(sigm(bf_lo((ob & 0x8000u) ? (ob & 0x7fffu) : (~ob & 0xffffu))));
          const float f0 = ((tl >> 1) == (u32)i) ? sg : 0.f, f1 = ((tl >> 1) == (u32)i + 16u) ? sg : 0.f;
#pragma unroll
          for (int cc = 0; cc < 4; ++cc) {
            const f32x4 u = *(LAS f32x4*)(L + L_UPD + (c * 64 + 16 * cc + 4 * g) * 4);
            oc[0][cc] += u * f0; oc[1][cc] += u * f1;
          }
        }
      }
    }
    const rsrc_t rsO = mk_rsrc(P.out, S_ * D_ * 2);
    const u32 xo = ((pan2 * 128 + par2 + 32 * t2 + 2 * i) * D_ + cg * 64 + (g & 1) * 16 + (g >> 1) * 8) * 2;
#pragma unroll
    for (int tt = 0; tt < 2; ++tt)
#pragma unroll
      for (int cp = 0; cp < 2; ++cp) {
        const u32 a0 = pack2(oc[tt][2 * cp][0], oc[tt][2 * cp][1]), a1 = pack2(oc[tt][2 * cp][2], oc[tt][2 * cp][3]);
        const u32 b0 = pack2(oc[tt][2 * cp + 1][0], oc[tt][2 * cp + 1][1]), b1 = pack2(oc[tt][2 * cp + 1][2], oc[tt][2 * cp + 1][3]);
        const auto e0 = __builtin_amdgcn_permlane16_swap(a0, b0, false, false);
        const auto e1 = __builtin_amdgcn_permlane16_swap(a1, b1, false, false);
        st_sc1_128(u32x4{e0[0], e1[0], e0[1], e1[1]}, rsO, xo + tt * 65536 + cp * 64);
      }
    STAMP(7);
  }
}

static u64 k_order(int odd, int pl, int hl) {
  const int np = 7 - odd;
  int slot[16], used[16];
  for (int k = 0; k < 16; ++k) { slot[k] = -1; used[k] = 0; }
  for (int k = 2; k < 16; ++k) {
    const int h0 = k & 3, p0 = (k * (odd ? 5 : 3)) % np;
    int want = -1;
    if (hl == h0 && pl % np == p0) want = k - 2; else if (hl == h0 || pl % np == p0) want = k - 1;
    if (want >= 0) {
      if (want < 1) want = 1;
      while (want > 1 && used[want]) --want;
      if (!used[want]) { slot[k] = want; used[want] = 1; }
    }
  }
  int s = 0;
  for (int k = 0; k < 16; ++k) if (slot[k] < 0) { while (used[s]) ++s; slot[k] = s; used[s] = 1; }
  u64 r = 0;
  for (int k = 0; k < 16; ++k) r |= (u64)k << (4 * slot[k]);
  return r;
}

extern "C" void kernel_launch(void* const* d_in, const int* in_sizes, int n_in, void* d_out, int out_size, void* d_ws, size_t ws_size,
                              hipStream_t stream) {
  Args A;
  Params& P = A.p;
  P.x = (const u16*)d_in[0]; P.wqkv = (const u16*)d_in[1]; P.wo = (const u16*)d_in[2]; P.wr = (const u16*)d_in[4];
  P.wg = (const u16*)d_in[5]; P.wu = (const u16*)d_in[6]; P.wd = (const u16*)d_in[7];
  P.out = (u16*)d_out; P.ws = (char*)d_ws;
  for (int e = 0; e < 64; ++e) A.kt[e] = k_order(e >> 5, (e >> 2) & 7, e & 3);
  (void)hipFuncSetAttribute((const void*)fused_kernel, hipFuncAttributeMaxDynamicSharedMemorySize, L_END + 64);
  hipLaunchKernelGGL(fused_kernel, dim3(256), dim3(512), L_END + 64, stream, A);
}
```

```cpp
#include <hip/hip_runtime.h>
#include <stdint.h>

typedef unsigned short u16;
typedef unsigned u32;
typedef unsigned long long u64;
typedef __attribute__((ext_vector_type(4))) short s16x4;
typedef __attribute__((ext_vector_type(8))) short s16x8;
typedef __attribute__((ext_vector_type(2))) float f32x2;
typedef __attribute__((ext_vector_type(4))) float f32x4;
typedef __attribute__((ext_vector_type(4))) unsigned u32x4;
typedef __attribute__((ext_vector_type(2))) unsigned u32x2;
typedef __attribute__((ext_vector_type(16))) unsigned u32x16;
typedef __attribute__((ext_vector_type(2))) __bf16 bf16x2;
typedef __amdgpu_buffer_rsrc_t rsrc_t;
#define LAS __attribute__((address_space(3)))
#define GAS __attribute__((address_space(1)))
#define DEV __device__ __forceinline__

#define STAMPS 0
constexpr int OWIN = 6;
constexpr int GWIN = 6, GPRE = 4;
constexpr int T_GATHER = 350, T_SEL = 580, T_H = 1200, T_O = 1520;
constexpr int HITAT = 24;
constexpr int T_Y = 100;
constexpr int WARM_AT = 10;
constexpr u64 HELPK = 0x765432ull, ROLLK = 0xfedcba98ull;
constexpr int HELPN = 6, RS0 = 1, RSN = 8;
constexpr int CODE_LINES = 348;

constexpr int S_ = 2048, D_ = 1024, F_ = 3584;
constexpr u32 WS_EPOCH = 0;
constexpr u32 WS_SEL = 4096;
constexpr u32 WS_RG = 16384;
constexpr u32 WS_SG = 32768;
constexpr u32 WS_OF = 65536;
constexpr u32 WS_H = WS_OF + 131072;
constexpr u32 WS_YS = WS_H + 131072;
constexpr u32 WS_O = WS_YS + 524288;
constexpr u32 WS_END = WS_O + S_ * D_ * 2;
constexpr u32 WS_STAMP = 16u << 20;
constexpr u32 WOFF = 22528, SLOT = 47104;
constexpr u32 L_HX = 131072;
constexpr u32 L_XT = 3 * SLOT;
constexpr u32 L_STQ = L_XT + 16384;
constexpr u32 L_STO = L_STQ + 1408;
constexpr u32 L_UPD = L_STO + 1024;
constexpr u32 L_ONE = L_UPD + 2048;
constexpr u32 L_CTL = L_ONE + 128;
constexpr u32 L_TAG = L_CTL, L_ARR = L_CTL + 4, L_UPDF = L_CTL + 8  , L_HS = L_CTL + 16  , L_SEL = L_CTL + 32  ;
constexpr u32 L_PW = L_CTL + 64, L_HR = L_CTL + 96;
constexpr u32 L_GO = L_CTL + 128;
constexpr u32 L_END = L_CTL + 192;
constexpr int SPIN_CAP = 1 << 22;
static_assert(L_END <= 163840 && L_ONE % 128 == 0, "LDS");

struct Params {
  const u16 *x, *wqkv, *wo, *wr, *wg, *wu, *wd;
  u16* out;
  char* ws;
};
struct Args {
  Params p;
  u64 kt[64];
};

DEV float bf_lo(u32 v) { return __uint_as_float(v << 16); }
DEV float bf_hi(u32 v) { return __uint_as_float(v & 0xffff0000u); }
DEV u32 pack2(float a, float b) { bf16x2 p = {(__bf16)a, (__bf16)b}; return __builtin_bit_cast(u32, p); }
DEV float rbf(float a) { return bf_lo(pack2(a, a)); }
DEV float dot2(u32 a, u32 b, float c) { return __builtin_amdgcn_fdot2_f32_bf16(__builtin_bit_cast(bf16x2, a), __builtin_bit_cast(bf16x2, b), c, false); }
DEV s16x8 as_frag(u32x4 v) { return __builtin_bit_cast(s16x8, v); }
DEV f32x4 mfma(s16x8 a, s16x8 b, f32x4 c) { return __builtin_amdgcn_mfma_f32_16x16x32_bf16(a, b, c, 0, 0, 0); }
DEV int rfl(int v) { return __builtin_amdgcn_readfirstlane(v); }
template <int CTRL, int RM, bool BC> DEV float dppf(float v) { return __int_as_float(__builtin_amdgcn_update_dpp(0, __float_as_int(v), CTRL, RM, 0xf, BC)); }
template <int CTRL, int RM, bool BC> DEV u32 dppu(u32 v) { return (u32)__builtin_amdgcn_update_dpp(0, (int)v, CTRL, RM, 0xf, BC); }
DEV float half_sum(float v) {
  v += dppf<0x111, 0xf, true>(v); v += dppf<0x112, 0xf, true>(v); v += dppf<0x114, 0xf, true>(v); v += dppf<0x118, 0xf, true>(v);
  v += dppf<0x142, 0xa, false>(v);
  return v;
}
DEV u32 wave_max_u32(u32 v) {
  v = max(v, dppu<0x111, 0xf, true>(v)); v = max(v, dppu<0x112, 0xf, true>(v)); v = max(v, dppu<0x114, 0xf, true>(v)); v = max(v, dppu<0x118, 0xf, true>(v));
  v = max(v, dppu<0x142, 0xa, false>(v)); v = max(v, dppu<0x143, 0xc, false>(v));
  return (u32)__builtin_amdgcn_readlane((int)v, 63);
}
DEV float vmax(float a, float b) { return __builtin_elementwise_maximum(a, b); }
DEV float vmax3(float a, float b, float c) { return vmax(vmax(a, b), c); }
DEV float max16(float v) { u32 a = __float_as_uint(v); auto r = __builtin_amdgcn_permlane16_swap(a, a, false, false); u32 x = r[0], y = r[1]; return vmax(__uint_as_float(x), __uint_as_float(y)); }
DEV float max32(float v) { u32 a = __float_as_uint(v); auto r = __builtin_amdgcn_permlane32_swap(a, a, false, false); u32 x = r[0], y = r[1]; return vmax(__uint_as_float(x), __uint_as_float(y)); }
DEV float sum32(float v) { u32 a = __float_as_uint(v); auto r = __builtin_amdgcn_permlane32_swap(a, a, false, false); u32 x = r[0], y = r[1]; return __uint_as_float(x) + __uint_as_float(y); }
DEV u32 ord16(u32 b) { return (b & 0x8000u) ? (~b & 0xffffu) : (b | 0x8000u); }
DEV float sigm(float v) { return __builtin_amdgcn_rcpf(1.f + __builtin_amdgcn_exp2f(-v * 1.4426950408889634f)); }

DEV rsrc_t mk_rsrc(const void* p, u32 bytes) { return __builtin_amdgcn_make_buffer_rsrc((void*)p, 0, (int)bytes, 0x00020000); }
DEV u32x4 ld_nt_128(rsrc_t rs, u32 voff, u32 soff) { return __builtin_amdgcn_raw_buffer_load_b128(rs, voff, soff, 2); }
DEV u32x4 ld_sc1_128(rsrc_t rs, u32 voff, u32 soff) { return __builtin_amdgcn_raw_buffer_load_b128(rs, voff, soff, 16); }
DEV u32x2 ld_sc1_64(rsrc_t rs, u32 voff, u32 soff) { return __builtin_amdgcn_raw_buffer_load_b64(rs, voff, soff, 16); }
DEV u32 ld_sc1_32(rsrc_t rs, u32 voff, u32 soff) { return __builtin_amdgcn_raw_buffer_load_b32(rs, voff, soff, 16); }
DEV void st_sc1_128(u32x4 v, rsrc_t rs, u32 voff) { __builtin_amdgcn_raw_buffer_store_b128(v, rs, voff, 0, 16); }
DEV void st_sc1_64(u32x2 v, rsrc_t rs, u32 voff) { __builtin_amdgcn_raw_buffer_store_b64(v, rs, voff, 0, 16); }
DEV void st_sc1_32(u32 v, rsrc_t rs, u32 voff) { __builtin_amdgcn_raw_buffer_store_b32(v, rs, voff, 0, 16); }
#define VMCNT(n) asm volatile("s_waitcnt vmcnt(" #n ")" ::: "memory")
#define LGKM0() asm volatile("s_waitcnt lgkmcnt(0)" ::: "memory")
#define CBAR() asm volatile("" ::: "memory")
#define BAR() do { CBAR(); __builtin_amdgcn_s_barrier(); CBAR(); } while (0)
#define SB() __builtin_amdgcn_sched_barrier(0)
#define LP(a) ((LAS char*)(uintptr_t)(a))
#define PIN(x) asm volatile("" : "+v"(x))

#define TICK() 0ull
#define RT() 0ull
#define STAMP(k) do {} while (0)
#define CSTAMP(k) do {} while (0)
#define LSTAMP(k) do {} while (0)

DEV u32 lds_rd(LAS char* L, u32 off) { return *(volatile LAS u32*)(L + off); }
DEV void lds_wr(LAS char* L, u32 off, u32 v) { *(volatile LAS u32*)(L + off) = v; }
DEV void lds_wait_set(LAS char* L, u32 off) {
  _Pragma("nounroll") for (int sp = 0; sp < SPIN_CAP; ++sp) {
    if (__all(lds_rd(L, off) != 0u)) break;
    __builtin_amdgcn_s_sleep(1);
  }
  CBAR();
}
DEV void wait_until(u64 t0, u32 ticks) {
  while (__builtin_amdgcn_s_memrealtime() - t0 < ticks) __builtin_amdgcn_s_sleep(4);
}
#define DMA_HEAD "s_mov_b32 m0, %0\n\ts_nop 0\n\t"
DEV void dma16(u32 lds, u32 voff, rsrc_t rs, u32 soff) {
  asm volatile(DMA_HEAD "buffer_load_dwordx4 %1, %2, %3 offen lds" ::"s"(lds), "v"(voff), "s"(rs), "s"(soff) : "memory");
}
DEV void dma16x4(u32 lds, u32 v0, u32 v1, u32 v2, u32 v3, rsrc_t rs, u32 soff) {
  asm volatile(DMA_HEAD "buffer_load_dwordx4 %1, %5, %6 offen lds\n\tbuffer_load_dwordx4 %2, %5, %6 offen offset:1024 lds\n\t"
               "buffer_load_dwordx4 %3, %5, %6 offen offset:2048 lds\n\tbuffer_load_dwordx4 %4, %5, %6 offen offset:3072 lds"
               ::"s"(lds), "v"(v0), "v"(v1), "v"(v2), "v"(v3), "s"(rs), "s"(soff) : "memory");
}
DEV void dma16x2(u32 lds, u32 v0, u32 v1, rsrc_t rs, u32 soff) {
  asm volatile(DMA_HEAD "buffer_load_dwordx4 %1, %3, %4 offen lds\n\tbuffer_load_dwordx4 %2, %3, %4 offen offset:1024 lds"
               ::"s"(lds), "v"(v0), "v"(v1), "s"(rs), "s"(soff) : "memory");
}

struct Frags { s16x8 x[6]; s16x8 w[6]; };

DEV s16x8 tr_frag(LAS char* p, LAS char* q, u32 o0, u32 o1) {
  s16x4 a = __builtin_amdgcn_ds_read_tr16_b64_v4i16((LAS s16x4*)(p + o0));
  s16x4 b = __builtin_amdgcn_ds_read_tr16_b64_v4i16((LAS s16x4*)(q + o1));
  return __builtin_shufflevector(a, b, 0, 1, 2, 3, 4, 5, 6, 7);
}
template <int KK> DEV void load_frags(Frags& f, LAS char* aH, LAS char* aT, LAS char* w0, LAS char* w1) {
#pragma unroll
  for (int t = 0; t < 5; ++t) f.x[1 + t] = *(LAS s16x8*)(aT + t * 2048);
  f.x[0] = *(LAS s16x8*)aH;
#pragma unroll
  for (int sec = 0; sec < 3; ++sec) {
    f.w[2 * sec] = tr_frag(w0, w0, sec * 8192 + KK * 4096, sec * 8192 + KK * 4096 + 512);
    f.w[2 * sec + 1] = tr_frag(w1, w1, sec * 8192 + KK * 4096, sec * 8192 + KK * 4096 + 512);
  }
}
template <int A, int B> DEV void mma_range(f32x4 (&acc)[6][6], const Frags& f, s16x8 e0) {
#pragma unroll
  for (int n = A; n < B; ++n) {
    const int p = n / 11, r = n % 11, w = 2 * p + (r >= 6), t = (r >= 6) ? 11 - r : 5 - r;
    acc[w][t] = (w < 4) ? mfma(f.w[w], (w == 0 && t == 0) ? e0 : f.x[t], acc[w][t]) : mfma(f.x[t], f.w[w], acc[w][t]);
  }
}
DEV void interleave() {
#pragma unroll
  for (int k = 0; k < 6; ++k) { __builtin_amdgcn_sched_group_barrier(0x008, 1, 0); __builtin_amdgcn_sched_group_barrier(0x100, 1, 0); }
#pragma unroll
  for (int k = 0; k < 6; ++k) {
    __builtin_amdgcn_sched_group_barrier(0x008, 1, 0); __builtin_amdgcn_sched_group_barrier(0x100, 1, 0);
    __builtin_amdgcn_sched_group_barrier(0x008, 1, 0); __builtin_amdgcn_sched_group_barrier(0x100, 1, 0);
    __builtin_amdgcn_sched_group_barrier(0x008, 1, 0);
  }
}

template <int E> DEV s16x8 col_frag(const u32x4* L) {
  constexpr u32 sel = (E & 1) ? 0x07060302u : 0x05040100u;
  u32x4 r;
  r.x = __builtin_amdgcn_perm(L[1][E >> 1], L[0][E >> 1], sel);
  r.y = __builtin_amdgcn_perm(L[3][E >> 1], L[2][E >> 1], sel);
  r.z = __builtin_amdgcn_perm(L[5][E >> 1], L[4][E >> 1], sel);
  r.w = __builtin_amdgcn_perm(L[7][E >> 1], L[6][E >> 1], sel);
  return as_frag(r);
}
DEV void mma_cols(f32x4* acc, const u32x4* L, s16x8 b) {
  acc[0] = mfma(col_frag<0>(L), b, acc[0]); acc[1] = mfma(col_frag<1>(L), b, acc[1]);
  acc[2] = mfma(col_frag<2>(L), b, acc[2]); acc[3] = mfma(col_frag<3>(L), b, acc[3]);
  acc[4] = mfma(col_frag<4>(L), b, acc[4]); acc[5] = mfma(col_frag<5>(L), b, acc[5]);
  acc[6] = mfma(col_frag<6>(L), b, acc[6]); acc[7] = mfma(col_frag<7>(L), b, acc[7]);
}

DEV void transpose_grp(u32x4* L) {
  const s16x8 f0 = col_frag<0>(L), f1 = col_frag<1>(L), f2 = col_frag<2>(L), f3 = col_frag<3>(L), f4 = col_frag<4>(L), f5 = col_frag<5>(L), f6 = col_frag<6>(L), f7 = col_frag<7>(L);
  L[0] = __builtin_bit_cast(u32x4, f0); L[1] = __builtin_bit_cast(u32x4, f1); L[2] = __builtin_bit_cast(u32x4, f2); L[3] = __builtin_bit_cast(u32x4, f3);
  L[4] = __builtin_bit_cast(u32x4, f4); L[5] = __builtin_bit_cast(u32x4, f5); L[6] = __builtin_bit_cast(u32x4, f6); L[7] = __builtin_bit_cast(u32x4, f7);
#pragma unroll
  for (int e = 0; e < 8; ++e) PIN(L[e]);
  SB();
}
DEV void mma_frags8(f32x4* acc, const u32x4* L, s16x8 b) {
#pragma unroll
  for (int e = 0; e < 8; ++e) acc[e] = mfma(as_frag(L[e]), b, acc[e]);
}
DEV void warm_line(u32 lds, const char* a) {
  asm volatile(DMA_HEAD "global_load_lds_dword %1, off" ::"s"(lds), "v"(a) : "memory");
}

__global__ void __launch_bounds__(512, 2) fused_kernel(Args Parg) {
  extern __shared__ __attribute__((aligned(128))) char smem[];
  u32x16 ka;
  u64 kb;
  asm volatile("s_load_dwordx16 %0, %2, 0x0\n\ts_load_dwordx2 %1, %2, 0x40" : "=&s"(ka), "=&s"(kb) : "s"(__builtin_amdgcn_kernarg_segment_ptr()));
  Params P;
  const char* pc0;
  asm volatile("s_getpc_b64 %0" : "=s"(pc0));
  LAS char* L = (LAS char*)smem;
  const u32 lbase = (u32)(uintptr_t)L;
  const int lane = threadIdx.x & 63;
  const int wave = rfl(threadIdx.x >> 6);
  const int bid = blockIdx.x, xg = bid & 7, loc = bid >> 3;
  const int np = 7 - (xg & 1);
  const bool gemm = loc < 4 * np;
  const int pl = loc >> 2, hl = loc & 3;
  const int pan = (xg & 1) * 7 + pl, hp = (xg >> 1) * 4 + hl, r0 = pan * 160;
  const int fi = loc - 4 * np;
  const bool dwn = (u32)(fi - 2 - 3 * (xg & 1)) <= 2u * (xg & 1);
  const int fu = (xg & 1) * 12 + (xg >> 1) * (3 + 2 * (xg & 1)) + min(fi, 2 + 2 * (xg & 1));
  const int du = (xg & 1) ? 4 + (xg >> 1) * 3 + fi - 5 : (xg >> 1);
  const int pan2 = (xg & 1) * 8 + (xg >> 1) * 2 + (loc >> 4);
  const int cg = (int)((((loc & 16) ? 0xfedc0a987653421bull : 0xfedc8a9b76534210ull) >> (4 * (loc & 15))) & 15), hrot = 2 * cg;
  const int g = lane >> 4, i = lane & 15;
  u64 kord;
  asm volatile("s_load_dwordx2 %0, %1, %2" : "=&s"(kord) : "s"(__builtin_amdgcn_kernarg_segment_ptr()), "s"(72 + (((xg & 1) * 32 + loc) & 63) * 8));
  rsrc_t rsWS;
  const u64 tstart = __builtin_amdgcn_s_memrealtime();
  u32 tag = 0;
  f32x4 oc[2][4];
  auto oc_init = [&]() {
    const int tl = (wave >> 1) + 64 * (wave & 1) + 2 * i;
    f32x2 st[2];
    u32x2 v[2][4];
#pragma unroll
    for (int tt = 0; tt < 2; ++tt) {
      st[tt] = *(LAS f32x2*)(L + L_STO + tl * 8 + tt * 256);
#pragma unroll
      for (int c = 0; c < 4; ++c) v[tt][c] = *(LAS u32x2*)(L + L_XT + tl * 128 + (((2 * c + (g >> 1)) ^ (i & 7)) << 4) + (g & 1) * 8 + tt * 4096);
    }
    SB();
#pragma unroll
    for (int tt = 0; tt < 2; ++tt) {
      const float k1 = 1.f + st[tt].y, k0 = -st[tt].x * st[tt].y;
#pragma unroll
      for (int c = 0; c < 4; ++c) oc[tt][c] = f32x4{bf_lo(v[tt][c].x), bf_hi(v[tt][c].x), bf_lo(v[tt][c].y), bf_hi(v[tt][c].y)} * k1 + k0;
    }
#pragma unroll
    for (int q = 0; q < 8; ++q) PIN(oc[q >> 2][q & 3]);
  };

  u32 tq[3], vO, vx;
  auto helper_offsets = [&]() {
    u32 lane = threadIdx.x & 63;
    PIN(lane);
    const int lr = lane >> 3, lc = lane & 7;
#pragma unroll
    for (int j = 0; j < 3; ++j) {
      const int rho = min(64 * j + (int)lane, 175), pr = rho >= 96;
      const int tk = r0 + ((rho < 16) ? rho - 16 : pr + 2 * (rho - 16 - 80 * pr));
      const u32 a = (u32)(pan2 * 128 + (rho & 127)) * 8, b = (((u32)tk >= (u32)S_) | (rho == 0)) ? 1u : (u32)tk * 8;
      tq[j] = (wave == 7) ? a : b;
      PIN(tq[j]);
    }
    vO = lr * 2048 + ((u32)(lc ^ (((lr >> 1) & 3) << 1)) << 4) + cg * 128;
    vx = lr * 2048 + ((u32)(lc ^ (lr >> 1)) << 4) + cg * 128;
    PIN(vO); PIN(vx);
  };
  auto wo_grp = [&](int q) { dma16x4(lbase + q * 4096, vO, vO + 15 * 1024, vO + 30 * 1024, vO + 45 * 1024, mk_rsrc(P.wo, D_ * D_ * 2), ((q + hrot) & 31) * 65536); };
  auto helper_fill = [&]() {
    const int lid = wave - 4;
    tag = (u32)rfl((int)lds_rd(L, L_TAG));
    if (gemm) { VMCNT(11); BAR(); }
    if (lid >= 2) {
      u32x2 sv[3];
#pragma unroll
      for (int q = 2 * lid - 4; q < 2 * lid - 2; ++q) dma16x4(lbase + L_XT + q * 4096, vx, (vx ^ 64) + 15 * 1024, vx + 30 * 1024, (vx ^ 64) + 45 * 1024, mk_rsrc(P.x, S_ * D_ * 2), (pan2 * 128 + 32 * q) * 2048);
#pragma unroll
      for (int j = 0; j < 3; ++j) sv[j] = ld_sc1_64(rsWS, tq[j] & ~7u, WS_SG);
      SB();
      if (gemm) { VMCNT(11); BAR(); }
      _Pragma("nounroll") for (int sp = 0; sp < SPIN_CAP; ++sp) {
        if (__all(sv[0].y == tag && sv[1].y == tag && sv[2].y == tag)) break;
        __builtin_amdgcn_s_sleep(2);
#pragma unroll
        for (int j = 0; j < 3; ++j) sv[j] = ld_sc1_64(rsWS, tq[j] & ~7u, WS_SG);
      }
      f32x2 st[3];
      u32 ta = lbase + ((lid == 3) ? L_STO + lane * 8 : L_STQ + lane * 4), tb2 = ta + ((lid == 3) ? 4 : 704);
      u32 tc = (lid == 2 && lane < 48) ? ta + 512 : lbase + L_UPD + lane * 4, td = (lid == 2 && lane < 48) ? tb2 + 512 : tc;
      const u32 ts = (lid == 3) ? 512 : 256;
#pragma unroll
      for (int j = 0; j < 3; ++j) {
        st[j] = f32x2{__uint_as_float((sv[j].x << 16) ^ ((lid == 3) ? 0u : 0x80000000u)), __builtin_amdgcn_rcpf(bf_hi(sv[j].x))};
        if (tq[j] & 1u) st[j] = f32x2{0.f, 0.f};
        PIN(st[j]);
      }
      u32 te = ta + ts, tf = tb2 + ts;
      PIN(ta); PIN(tb2); PIN(tc); PIN(td); PIN(te); PIN(tf);
      VMCNT(0);
      if (gemm) BAR();
      *(LAS float*)LP(ta) = st[0].x; *(LAS float*)LP(tb2) = st[0].y;
      *(LAS float*)LP(te) = st[1].x; *(LAS float*)LP(tf) = st[1].y;
      *(LAS float*)LP(tc) = st[2].x; *(LAS float*)LP(td) = st[2].y;
      LGKM0();
    } else {
      if (gemm) { VMCNT(0); BAR(); }
      wo_grp(12 + lid); wo_grp(14 + lid);
#pragma unroll
      for (int q = 0; q < 4; ++q) wo_grp(16 + 2 * q + lid);
      if (gemm) BAR();
    }
    if (gemm) BAR();
    int l2 = lid;
    asm volatile("" : "+s"(l2));
    wo_grp(l2); wo_grp(4 + l2); wo_grp(8 + l2);
    if (!dwn) { wo_grp(24 + l2); wo_grp(28 + l2); }
    if (lid == 2) {
      u32x2 sk;
      _Pragma("nounroll") for (int sp = 0; sp < SPIN_CAP; ++sp) {
        sk = ld_sc1_64(rsWS, (lane & 7) * 8, WS_SEL);
        if (__all(sk.y == tag)) break;
        __builtin_amdgcn_s_sleep(8);
      }
      if (lane < 8) lds_wr(L, L_SEL + 4 * lane, sk.x);
      LGKM0();
    }
  };
  {
    u32 ln = threadIdx.x & 63;
    PIN(ln);
    if (ln < 2) warm_line(lbase + L_UPD, pc0 + min((loc * 8 + wave) * 2 + (int)ln, CODE_LINES - 1) * 128);
  }
  const int lid = wave - 4;
  const bool isx = lid < 2;
  u32 v0 = 0, v1 = 0, v2 = 0, v3 = 0, vh = 0, g0 = 0, g1 = 0, g2 = 0, kstep = 0, d0 = 0;
  {
    int wave_a = wave;
    asm volatile("" : "+s"(wave_a));
    if (gemm && wave_a >= 4) {
      u32 ln = threadIdx.x & 63;
      PIN(ln);
      const int lr = ln >> 3, lc = ln & 7;
      const u32 sw = (u32)(lc ^ (isx ? lr >> 1 : lr & 2)) << 4;
      const u32 vb = lr * (isx ? 4096 : 6144) + sw + (isx ? (r0 + lid) * 2048 : hp * 128);
      const u32 pp = (isx ? 32768 : 49152) - 1024;
      v0 = vb; v1 = (vb ^ 64) + pp; v2 = vb + 2 * pp; v3 = (vb ^ 64) + 3 * pp;
      g0 = (lid == 3) ? 198656 : 0; g1 = isx ? 131072 : (lid == 3) ? 4096 : 196608; g2 = isx ? 262144 : (lid == 3) ? 200704 : 2048;
      vh = max(r0 - 16 + 8 * lid + lr, 0) * 2048 + (sw ^ (lid << 6));
      kstep = isx ? 128u : 64u * 6144u; d0 = lbase + (isx ? 2 + 10 * lid : 22 + 12 * (lid - 2)) * 1024;
      PIN(v0); PIN(v1); PIN(v2); PIN(v3); PIN(vh);
      asm volatile("" : "+s"(g0), "+s"(g1), "+s"(g2), "+s"(kstep), "+s"(d0));
    }
  }
  u32 ev;
  asm volatile("s_waitcnt lgkmcnt(0)" : "+s"(ka), "+s"(kb), "+s"(kord));
  {
    auto ptr = [&](int k) { return (char*)(GAS char*)(((u64)ka[2 * k + 1] << 32) | ka[2 * k]); };
    P.x = (const u16*)ptr(0); P.wqkv = (const u16*)ptr(1); P.wo = (const u16*)ptr(2); P.wr = (const u16*)ptr(3);
    P.wg = (const u16*)ptr(4); P.wu = (const u16*)ptr(5); P.wd = (const u16*)ptr(6); P.out = (u16*)ptr(7);
    P.ws = (char*)(GAS char*)kb;
  }
  rsWS = mk_rsrc(P.ws, WS_END);
  STAMP(0);
  if (wave < 4) {
    asm volatile("s_load_dword %0, %1, 0x0" : "=s"(ev) : "s"(P.ws + WS_EPOCH) : "memory");
    u32 ln = threadIdx.x & 63;
    PIN(ln);
    const u32 d = lbase + 2 * SLOT + wave * 6144, v = ln * 16;
    dma16x4(d, v, v, v, v, mk_rsrc(P.x, S_ * D_ * 2), (pan2 * 128 + cg * 8 + wave * 2) * 2048);
    dma16x2(d + 4096, v, v, mk_rsrc(P.wr, D_ * 2), 0);
  }
  SB();
  int wave_b = wave;
  asm volatile("" : "+s"(wave_b));
  if (gemm && wave_b >= 4) {
    const rsrc_t rs = mk_rsrc(isx ? P.x : P.wqkv, isx ? S_ * D_ * 2 : D_ * 3 * D_ * 2);
    auto issue_k = [&](u32 ko, u32 so) {
      dma16x4(d0 + so, v0, v1, v2, v3, rs, ko + g0);
      dma16x4(d0 + so + 4096, v0, v1, v2, v3, rs, ko + g1);
      if (isx) { dma16x2(d0 + so + 8192, v0, v1, rs, ko + g2); dma16(lbase + so + lid * 1024, vh, rs, ko); }
      else dma16x4(d0 + so + 8192, v0, v1, v2, v3, rs, ko + g2);
    };
    auto issue = [&](int s, u32 so) { issue_k(((u32)(kord >> (4 * s)) & 15u) * kstep, so); };
    LSTAMP(20);
    issue(0, 0);
    LSTAMP(21);
    helper_offsets();
    issue(1, SLOT);
    LSTAMP(22);
    if (isx) VMCNT(11); else VMCNT(12);
    LSTAMP(23);
    BAR();
    const bool helps = !isx && !(xg & 2) && hl != 3;
    if (helps) {
      constexpr int HL = (HELPN * 192 + 35) / 36;
      const int n = min(((pl * 3 + hl) * 2 + lid - 2) * HL + min(lane, HL - 1), HELPN * 192 - 1), row = n / 3;
      warm_line(lbase + L_UPD, (const char*)P.wqkv + (size_t)((u32)((HELPK >> (4 * (row >> 6))) & 15) * 64 + (row & 63)) * 6144 + (n % 3) * 2048 + (hp | 3) * 128);
    }
    issue(2, 2 * SLOT);
    u32 so = 0, sdum = 0;
    const int sn = min((pl * 4 + hl) * 4 + lid, 95) * 2;
    const u32 so0 = (sn / 3) * 6144 + (sn % 3) * 2048, so1 = ((sn + 1) / 3) * 6144 + ((sn + 1) % 3) * 2048;
    const char* sbase = (const char*)P.wqkv + (hp | 3) * 128;
    _Pragma("nounroll") for (int s = 0; s < 13; ++s) {
      if (helps && s == 0) VMCNT(13); else if (!isx || s == WARM_AT + 1 || s == WARM_AT + 2) VMCNT(12); else VMCNT(11);
      if (s == 0) LSTAMP(24);
      if (s == 1) LSTAMP(25);
      if (s == 2) LSTAMP(26);
      BAR();
      issue(s + 3, so);
      if (!(xg & 2) && (u32)(s - RS0) < RSN) {
        const u32 ko = (u32)((ROLLK >> (4 * (s - RS0))) & 15) * (64u * 6144u), a0 = ko + so0, a1 = ko + so1;
        asm volatile("s_load_dword %0, %1, %2\n\ts_load_dword %0, %1, %2 offset:64\n\ts_load_dword %0, %1, %3\n\ts_load_dword %0, %1, %3 offset:64"
                     : "+s"(sdum) : "s"(sbase), "s"(a0), "s"(a1) : "memory");
      }
      if (s == WARM_AT && isx) warm_line(lbase + L_UPD, (const char*)P.wo + (size_t)min((pan * 16 + hp) * 80 + lid * 40 + min(lane, 39), 16383) * 128);
      so = (so == 2 * SLOT) ? 0 : so + SLOT;
    }
    asm volatile("s_waitcnt lgkmcnt(0)" : "+s"(sdum));
    STAMP(2);
    helper_fill();
  }
  if (wave < 4) {
    __builtin_amdgcn_s_setprio(1);
    if (wave == 1) lds_wr(L, L_ONE + lane * 4, lane < 32 ? 0x3f803f80u : 0u);
    if (wave == 2) lds_wr(L, L_GO, 0u);
    f32x4 acc[6][6];
#pragma unroll
    for (int w = 0; w < 6; ++w)
#pragma unroll
      for (int t = (w & 1); t < 6; ++t) { acc[w][t] = f32x4{0.f, 0.f, 0.f, 0.f}; PIN(acc[w][t]); }
    f32x4 bprev, bhalo;
    u32 mc[2];
    u64 cur[4];
#pragma unroll
    for (int r = 0; r < 4; ++r) {
      const int kap = 4 * g + r, d = 16 + (wave & 1) + 2 * i - kap;
      bprev[r] = (kap - i >= 9) ? 0.f : -1e30f;
      bhalo[r] = (((d & 1) == 0) & (d <= 14) & (pan > 0)) ? 0.f : -1e30f;
      cur[r] = __ballot((u32)(i - kap) <= 7u);
      asm volatile("" : "+s"(cur[r]));
    }
    mc[0] = (((u32)(i - 4 * g) <= 7u) ? 0xffffu : 0u) | (((u32)(i - 4 * g - 1) <= 7u) ? 0xffff0000u : 0u);
    mc[1] = (((u32)(i - 4 * g - 2) <= 7u) ? 0xffffu : 0u) | (((u32)(i - 4 * g - 3) <= 7u) ? 0xffff0000u : 0u);
    u32 hxl = lbase + L_HX + (wave & 2) * 2304 + lane * 16;
    u32 obase = WS_O + (((wave & 1) * 32 + hp * 2 + (wave >> 1)) * 64 + pan * 5) * 1024 + lane * 16;
    PIN(bprev); PIN(bhalo); PIN(mc[0]); PIN(mc[1]); PIN(hxl); PIN(obase);
    SB();
    {
      const int tok = pan2 * 128 + cg * 8 + wave * 2 + (lane >> 5);
      u32x4 xv[4], wv[4];
      VMCNT(0);
      {
        LAS char* sp = L + 2 * SLOT + wave * 6144 + (lane & 31) * 16;
#pragma unroll
        for (int j = 0; j < 4; ++j) { xv[j] = *(LAS u32x4*)(sp + (lane >> 5) * 2048 + j * 512); wv[j] = *(LAS u32x4*)(sp + 4096 + j * 512); }
      }
      asm volatile("s_waitcnt lgkmcnt(0)" : "+s"(ev) :: "memory");
      const u32 e = ev;
      tag = e * 0x9E3779B1u + 0x7F4A7C15u; tag ^= tag >> 15; tag = (u32)rfl((int)(tag ^ (u32)(tag == e)));
      if (wave == 1) lds_wr(L, L_TAG, tag);
      float sx = 0.f, sxx = 0.f, sxw = 0.f, sw = 0.f;
#pragma unroll
      for (int j = 0; j < 4; ++j)
#pragma unroll
        for (int e2 = 0; e2 < 4; ++e2) {
          const u32 a = xv[j][e2], b = wv[j][e2];
          sx = dot2(a, 0x3f803f80u, sx); sxx = dot2(a, a, sxx); sxw = dot2(a, b, sxw); sw = dot2(b, 0x3f803f80u, sw);
        }
      const float mean = half_sum(sx) * (1.f / D_);
      const float var = fmaxf(half_sum(sxx) * (1.f / D_) - mean * mean, 0.f);
      const float mb = rbf(mean), sb = rbf(__builtin_amdgcn_sqrtf(rbf(rbf(var) + 1e-5f)));
      const u32 ro = ord16(pack2((half_sum(sxw) - mb * half_sum(sw)) * __builtin_amdgcn_rcpf(sb), 0.f) & 0xffffu);
      const u32 rlo = (u32)__builtin_amdgcn_readlane((int)ro, 31);
      if ((lane & 31) == 31) st_sc1_64(u32x2{pack2(mb, sb), tag}, rsWS, WS_SG + tok * 8);
      if (lane == 63) st_sc1_64(u32x2{rlo | (ro << 16), tag}, rsWS, WS_RG + (tok >> 1) * 8);
      if (!gemm) { LGKM0(); BAR(); }
    }
    STAMP(1);
    if (gemm) {
      const int h = wave >> 1, par = wave & 1;
        const int q4 = i >> 2, p4 = i & 3;
        u32 a0 = lbase + i * 128 + ((g ^ (i >> 1)) << 4);
        const u32 wsw = ((q4 >> 1) & 1) | ((g & 1) << 1);
        u32 w0 = lbase + WOFF + (8 * g + q4) * 128 + (((2 * h + par) ^ wsw) << 5) + p4 * 8;
        const u32 tb = (1 + 5 * par) * 2048;
        const u32 one = lbase + L_ONE;
        u32 pH = (i == 0) ? one : a0, pT = a0 + tb, pW0 = w0, pW1 = w0 ^ 32;
        PIN(a0); PIN(w0); PIN(pH); PIN(pT); PIN(pW0); PIN(pW1);
        Frags f0, f1;
        const u32 e1 = (i == 0) ? 0x3f803f80u : 0u;
        s16x8 e0 = as_frag(u32x4{e1, e1, e1, e1});
        PIN(e0);
        __builtin_amdgcn_s_waitcnt(0xc07f);
        BAR();
        STAMP(2); CSTAMP(14);
        load_frags<0>(f0, LP(pH), LP(pT), LP(pW0), LP(pW1));
        pH ^= 64; pT ^= 64;
        __builtin_amdgcn_s_waitcnt(0xc07f);
        u32 so = 0, trc = 0;
        auto half1 = [&]() {
          SB();
          load_frags<1>(f1, LP(pH), LP(pT), LP(pW0), LP(pW1));
          mma_range<0, 24>(acc, f0, e0);
          interleave();
          SB();
          so = (so == 2 * SLOT) ? 0 : so + SLOT;
          mma_range<24, 25>(acc, f0, e0);
          SB();
          pT = so + a0; pW0 = so + w0; PIN(pT); PIN(pW0);
          mma_range<25, 26>(acc, f0, e0);
          SB();
          pH = (i == 0) ? one : pT; pW1 = pW0 ^ 32; PIN(pH); PIN(pW1);
          mma_range<26, 27>(acc, f0, e0);
          SB();
          pT += tb; PIN(pT);
          mma_range<27, 28>(acc, f0, e0);
          SB();
        };
        auto half1b = [&](int s) {
          const u64 tnow = TICK(), rnow = RT();
          mma_range<28, 30>(acc, f0, e0);
          SB();
          __builtin_amdgcn_s_waitcnt(0xc07f);
          if (STAMPS) { trc = (lane == s) ? (u32)tnow : trc; trc = (lane == s + 16) ? (u32)rnow : trc; }
          mma_range<30, 31>(acc, f0, e0);
          SB();
          BAR();
          SB();
          mma_range<31, 33>(acc, f0, e0);
          SB();
        };
        half1();
        for (int s = 0; s < 15; ++s) {
          half1b(s);
          load_frags<0>(f0, LP(pH), LP(pT), LP(pW0), LP(pW1));
          mma_range<0, 24>(acc, f1, e0);
          interleave();
          SB();
          pH ^= 64; pT ^= 64; PIN(pH); PIN(pT);
          mma_range<24, 31>(acc, f1, e0);
          SB();
          __builtin_amdgcn_s_waitcnt(0xc07f);
          mma_range<31, 33>(acc, f1, e0);
          half1();
        }
        half1b(15);
        if (STAMPS && lane < 32) ((GAS u32*)(P.ws + WS_STAMP + (1 << 20)))[(bid * 4 + wave) * 32 + lane] = trc;
      mma_range<5, 6>(acc, f1, e0); mma_range<16, 17>(acc, f1, e0); mma_range<27, 28>(acc, f1, e0);
      SB();
      mma_range<11, 16>(acc, f1, e0);
      u32 tz = threadIdx.x;
      PIN(tz);
      const u32 i = tz & 15, g = (tz >> 4) & 3;
      u32 sqi = lbase + L_STQ + i * 4, sqg = lbase + L_STQ + g * 16;
      PIN(sqi); PIN(sqg);
      const u32 hx = hxl + par * 2304, hy = hxl + (par ^ 1) * 2304;
      u32 hxg = hx - i * 16, hyg = hy - i * 16, hxi = hx - g * 256, hyi = hy - g * 256, sqi2 = sqi + par * 320, sqg2 = sqg + par * 320;
      const u32 hqa = hx + 2048 - g * 240, hqb = hxl + (L_UPD - L_HX) - (wave & 2) * 2304;
      u32 hq = __builtin_amdgcn_inverse_ballot_w64(0x0001000100010001ull) ? hqa : hqb;
      PIN(hxg); PIN(hyg); PIN(hxi); PIN(hyi); PIN(sqi2); PIN(sqg2); PIN(hq);
#pragma unroll
      for (int k = 0; k < 5; ++k) { __builtin_amdgcn_sched_group_barrier(0x008, 1, 0); __builtin_amdgcn_sched_group_barrier(0x002, 2, 0); }
      SB();
      *(LAS f32x4*)LP(hx) = acc[2][0];
      *(LAS f32x4*)LP(hx + 1024) = acc[4][0];
      *(LAS f32x4*)LP(hq) = acc[0][0];
      mma_range<17, 22>(acc, f1, e0);
#pragma unroll
      for (int k = 0; k < 3; ++k) { __builtin_amdgcn_sched_group_barrier(0x008, 1, 0); __builtin_amdgcn_sched_group_barrier(0x200, 1, 0); }
      SB();
      mma_range<0, 3>(acc, f1, e0);
      SB();
      __builtin_amdgcn_s_waitcnt(0xc07f);
      BAR();
      SB();
      f32x4 cs[4];
      cs[2] = *(LAS f32x4*)LP(hxg); cs[3] = *(LAS f32x4*)LP(hyg);
      cs[0] = *(LAS f32x4*)LP(hxg + 2048 - g * 240); cs[1] = *(LAS f32x4*)LP(hyg + 2048 - g * 240);
      float m[6], rs[6];
#pragma unroll
      for (int t = 0; t < 6; ++t) { m[t] = *(LAS float*)LP((t ? sqi2 : sqi) + t * 64); rs[t] = *(LAS float*)LP((t ? sqi2 : sqi) + t * 64 + 704); }
      acc[3][0] = *(LAS f32x4*)LP(hy);
      const float cv[2] = {*(LAS float*)LP(hxi + 1024), *(LAS float*)LP(hyi + 1024)};
      acc[5][0] = *(LAS f32x4*)LP(hy + 1024);
      mma_range<3, 5>(acc, f1, e0); mma_range<6, 11>(acc, f1, e0);
#pragma unroll
      for (int k = 0; k < 7; ++k) { __builtin_amdgcn_sched_group_barrier(0x008, 1, 0); __builtin_amdgcn_sched_group_barrier(0x100, 2, 0); }
      SB();
      STAMP(3); CSTAMP(15);
      const float QS = 0.17677669529663687f * 1.4426950408889634f;
      const f32x4 z4 = {0.f, 0.f, 0.f, 0.f};
      f32x4 s0[5], s1[5], m4[6], r4[6];
      {
        s16x8 kf[6];
#pragma unroll
        for (int t = 5; t >= 0; --t) {
          const f32x4 k0 = (acc[2][t] + m[t] * cs[2]) * rs[t], k1 = (acc[3][t] + m[t] * cs[3]) * rs[t];
          kf[t] = as_frag(u32x4{pack2(k0[0], k0[1]), pack2(k0[2], k0[3]), pack2(k1[0], k1[1]), pack2(k1[2], k1[3])});
        }
        mma_range<22, 27>(acc, f1, e0); mma_range<28, 33>(acc, f1, e0);
#pragma unroll
        for (int k = 0; k < 10; ++k) { __builtin_amdgcn_sched_group_barrier(0x008, 1, 0); __builtin_amdgcn_sched_group_barrier(0x002, 3, 0); }
        SB();
#pragma unroll
        for (int t = 0; t < 6; ++t) { m4[t] = *(LAS f32x4*)LP((t ? sqg2 : sqg) + t * 64); r4[t] = *(LAS f32x4*)LP((t ? sqg2 : sqg) + t * 64 + 704); }
#pragma unroll
        for (int t = 0; t < 5; ++t) {
          const f32x4 q0 = acc[0][t + 1] + m[t + 1] * cs[0], q1 = acc[1][t + 1] + m[t + 1] * cs[1];
          const s16x8 qf = as_frag(u32x4{pack2(q0[0], q0[1]), pack2(q0[2], q0[3]), pack2(q1[0], q1[1]), pack2(q1[2], q1[3])});
          s0[t] = mfma(kf[t], qf, t == 0 ? bhalo : bprev); s1[t] = mfma(kf[t + 1], qf, z4);
        }
      }
      SB();
      u32x16 vp[2];
#pragma unroll
      for (int t = 0; t < 6; ++t) {
#pragma unroll
        for (int c = 0; c < 2; ++c) {
          const f32x4 v = (acc[4 + c][t] + m4[t] * cv[c]) * r4[t];
          vp[c][2 * t] = pack2(v[0], v[1]); vp[c][2 * t + 1] = pack2(v[2], v[3]);
        }
      }
      PIN(vp[0]); PIN(vp[1]);
      float l[5][4], mx[5], rq[5];
#pragma unroll
      for (int t = 0; t < 5; ++t) {
#pragma unroll
        for (int r = 0; r < 4; ++r) l[t][r] = __builtin_amdgcn_inverse_ballot_w64(cur[r]) ? s1[t][r] : s0[t][r];
        mx[t] = vmax3(vmax(l[t][0], l[t][1]), l[t][2], l[t][3]);
        rq[t] = rs[t + 1] * QS;
      }
#pragma unroll
      for (int t = 0; t < 4; t += 2) {
        const auto r = __builtin_amdgcn_permlane16_swap(__float_as_uint(mx[t]), __float_as_uint(mx[t + 1]), false, false);
        const u32 r0 = r[0], r1 = r[1];
        const float z = max32(vmax(__uint_as_float(r0), __uint_as_float(r1)));
        const auto b = __builtin_amdgcn_permlane16_swap(__float_as_uint(z), __float_as_uint(z), false, false);
        const u32 b0 = b[0], b1 = b[1];
        mx[t] = -__uint_as_float(b0) * rq[t]; mx[t + 1] = -__uint_as_float(b1) * rq[t + 1];
      }
      mx[4] = -max32(max16(mx[4])) * rq[4];
      const s16x8 ones = as_frag(u32x4{0x3f803f80u, 0x3f803f80u, 0x3f803f80u, 0x3f803f80u});
      f32x4 sm[5], o0[5], o1[5];
#pragma unroll
      for (int t = 0; t < 5; ++t) {
#pragma unroll
        for (int r = 0; r < 4; ++r) l[t][r] = __builtin_amdgcn_exp2f(__builtin_fmaf(l[t][r], rq[t], mx[t]));
        const u32 p01 = pack2(l[t][0], l[t][1]), p23 = pack2(l[t][2], l[t][3]);
        const s16x8 pp = as_frag(u32x4{p01 & ~mc[0], p23 & ~mc[1], p01 & mc[0], p23 & mc[1]});
        sm[t] = mfma(ones, pp, z4);
        o0[t] = mfma(as_frag(u32x4{vp[0][2 * t], vp[0][2 * t + 1], vp[0][2 * t + 2], vp[0][2 * t + 3]}), pp, z4);
        o1[t] = mfma(as_frag(u32x4{vp[1][2 * t], vp[1][2 * t + 1], vp[1][2 * t + 2], vp[1][2 * t + 3]}), pp, z4);
      }
#pragma unroll
      for (int t = 0; t < 5; ++t) {
        const float inv = __builtin_amdgcn_rcpf(sm[t][0]);
        o0[t] *= inv; o1[t] *= inv;
        st_sc1_128(u32x4{pack2(o0[t][0], o0[t][1]), pack2(o0[t][2], o0[t][3]), pack2(o1[t][0], o1[t][1]), pack2(o1[t][2], o1[t][3])}, rsWS,
                   (t == 4) ? obase + ((pan == 12) ? 0x40000000u : 4096u) : obase + t * 1024);
      }
      STAMP(8);
    }
  }
  if (!gemm) {
  if (dwn) {
    const rsrc_t rsD = mk_rsrc(P.wd, F_ * D_ * 2);
    const int k0 = (du >> 1) * 14 + (wave >> 2) * 7;
    const u32 vd = (g * D_ + (du & 1) * 512 + (wave & 3) * 128 + 8 * i) * 2;
    auto drow = [&](int q, int j) -> u32 { return (u32)(64 * ((k0 + q) >> 1) + 4 * ((k0 + q) & 1) + 8 * j) * (D_ * 2); };
    constexpr u32 D_W = 24 * 4096, D_H = D_W + 8 * 4352;
    u32x4 wb[56];
#pragma unroll
    for (int j = 0; j < 8; ++j) wb[j] = ld_nt_128(rsD, vd, drow(0, j));
    SB();
    if (wave >= 4) { BAR(); tag = (u32)rfl((int)lds_rd(L, L_TAG)); }
#pragma unroll
    for (int j = 8; j < 48; ++j) wb[j] = ld_nt_128(rsD, vd, drow(j >> 3, j & 7));
    SB();
    transpose_grp(wb);
#pragma unroll
    for (int e = 0; e < 4; ++e) *(LAS u32x4*)(L + D_W + wave * 4352 + e * 1024 + lane * 16) = wb[e];
    SB();
#pragma unroll
    for (int q = 1; q < 6; ++q) transpose_grp(wb + 8 * q);
#pragma unroll
    for (int j = 48; j < 56; ++j) wb[j] = ld_nt_128(rsD, vd, drow(6, j & 7));
    SB();
    __builtin_amdgcn_s_setprio(0);
    if (wave >= 4) { helper_offsets(); helper_fill(); }
    transpose_grp(wb + 48);
    VMCNT(0);
    STAMP(12);
    BAR();
    STAMP(11);
    {
      wait_until(tstart, T_H);
      const int qa = 2 * (wave & 3), qb = min(qa + 1, 6);
      u32x4 va, vb;
      _Pragma("nounroll") for (int sp = 0; sp < SPIN_CAP; ++sp) {
        va = ld_sc1_128(rsWS, lane * 16, WS_H + (k0 + qa) * 1024);
        vb = ld_sc1_128(rsWS, lane * 16, WS_H + (k0 + qb) * 1024);
        if (__all(va.y == tag && va.w == tag && vb.y == tag && vb.w == tag)) break;
        __builtin_amdgcn_s_sleep(1);
      }
      *(LAS u32x2*)(L + D_H + ((wave >> 2) * 7 + qa) * 512 + (lane & 31) * 16 + (lane >> 5) * 8) = u32x2{va.x, va.z};
      *(LAS u32x2*)(L + D_H + ((wave >> 2) * 7 + qb) * 512 + (lane & 31) * 16 + (lane >> 5) * 8) = u32x2{vb.x, vb.z};
      LGKM0();
      lds_wr(L, L_HR + wave * 4, 1);
      lds_wait_set(L, L_HR + (wave & 4) * 4 + (lane & 3) * 4);
    }
    STAMP(2);
    f32x4 ga[8];
#pragma unroll
    for (int e = 0; e < 8; ++e) ga[e] = f32x4{0.f, 0.f, 0.f, 0.f};
    {
      s16x8 hf = *(LAS s16x8*)(L + D_H + ((wave >> 2) * 7 + 6) * 512 + (g * 8 + (i & 7)) * 16);
#pragma unroll
      for (int q = 6; q >= 0; --q) {
        s16x8 hn = hf;
        if (q > 0) hn = *(LAS s16x8*)(L + D_H + ((wave >> 2) * 7 + q - 1) * 512 + (g * 8 + (i & 7)) * 16);
        mma_frags8(ga, wb + q * 8, hf);
        if (q == 5) {
#pragma unroll
          for (int e = 0; e < 4; ++e) wb[e] = *(LAS u32x4*)(L + D_W + wave * 4352 + e * 1024 + lane * 16);
        }
        SB();
        hf = hn;
      }
    }
    STAMP(4);
    {
      u32 ln = threadIdx.x & 63;
      PIN(ln);
      if ((ln & 15) < 8) {
        const u32 o = D_W + wave * 4352 + (ln & 7) * 64 + (((ln >> 4) ^ ((ln >> 1) & 3)) << 4);
#pragma unroll
        for (int e = 0; e < 8; ++e) *(LAS f32x4*)(L + o + e * 512) = ga[e];
      }
    }
    LGKM0();
    lds_wr(L, L_PW + 4 * wave, 1);
    if (wave >= 4) {
      {
        u32 ln = threadIdx.x & 63;
        PIN(ln);
        const u32 gq = 2 * (ln >> 5) + ((ln >> 1) & 1), o = lbase + D_W + (wave - 4) * 4352 + ((ln >> 2) & 7) * 512 + (ln & 1) * 8;
        u32 a[4], oy = WS_YS + (du >> 1) * 65536 + (du & 1) * 4096 + (wave - 4) * 1024 + ln * 16;
#pragma unroll
        for (int k = 0; k < 4; ++k) { a[k] = o + k * 128 + ((gq ^ k) << 4); PIN(a[k]); }
        PIN(oy);
        lds_wait_set(L, L_PW + (wave - 4) * 4);
        f32x2 p[8], q[8];
#pragma unroll
        for (int tk = 0; tk < 8; ++tk) { p[tk] = *(LAS f32x2*)(LP(a[tk >> 1]) + (tk & 1) * 64); q[tk] = *(LAS f32x2*)(LP(a[tk >> 1]) + (tk & 1) * 64 + 4 * 4352); }
        SB();
#pragma unroll
        for (int tk = 0; tk < 8; ++tk) {
          const f32x2 y = p[tk] + q[tk];
          st_sc1_128(u32x4{__float_as_uint(y.x), tag, __float_as_uint(y.y), tag}, rsWS, oy + tk * 8192);
        }
      }
      LGKM0();
      lds_wr(L, L_HS + 4 * (wave - 4), 1);
      lds_wait_set(L, L_HS + (lane & 3) * 4);
      u32 ln = threadIdx.x & 63;
      PIN(ln);
      vO = (ln >> 3) * 2048 + (((ln & 7) ^ (((ln >> 4) & 3) << 1)) << 4) + cg * 128;
      wo_grp(20 + wave); wo_grp(24 + wave);
      VMCNT(0);
      lds_wr(L, L_HS + 4 * (wave - 4), 2);
    }
    STAMP(8);
  } else {
    const int lr = lane >> 3, lc = lane & 7;
    const int fv = fu - (fu > 2);
    const bool two = fu != 2 && fv < 24;
    const int la = (fu == 2) ? 50 : (fv >= 24) ? (fv - 24) * 8 + 3 : (fv >= 21) ? (fv - 21) * 16 + 2 : (fv / 3) * 8 + ((fv % 3) ? 2 + 2 * (fv % 3) : 0);
    const int lb = la + ((fv >= 21) ? 8 : 1);
    const rsrc_t rsG = mk_rsrc(P.wg, D_ * F_ * 2), rsU = mk_rsrc(P.wu, D_ * F_ * 2), rsX = mk_rsrc(P.x, S_ * D_ * 2);
    const u32 vg = (i < 8 || two) ? ((wave * 128 + 8 * g) * F_ + ((i < 8) ? la : lb) * 64 + 8 * (i & 7)) * 2 : 0x7f000000u;
    u32x4 wb[8 * GWIN];
    auto ldgrp = [&](int q) {
#pragma unroll
      for (int j = 0; j < 8; ++j) wb[(q % GWIN) * 8 + j] = ld_nt_128((q & 1) ? rsU : rsG, vg, ((q >> 1) * 32 + j) * (F_ * 2));
      SB();
    };
    __builtin_amdgcn_s_setprio(0);
    ldgrp(0);
    ldgrp(1);
    if (wave >= 4) { BAR(); tag = (u32)rfl((int)lds_rd(L, L_TAG)); }
    if (fu == 2) {
      *(LAS u32x4*)(L + 32 + threadIdx.x * 16) = u32x4{0u, 0u, 0u, 0u};
      if (threadIdx.x < 4) *(LAS u32x4*)(L + 32 + 8192 + threadIdx.x * 16) = u32x4{0u, 0u, 0u, 0u};
      wait_until(tstart, T_GATHER);
      u32x4 v;
      _Pragma("nounroll") for (int sp = 0; sp < SPIN_CAP; ++sp) {
        v = ld_sc1_128(rsWS, lane * 16, WS_RG + wave * 1024);
        if (__all(v.y == tag && v.w == tag)) break;
        __builtin_amdgcn_s_sleep(2);
      }
      const u32 ib = 2047 - 256 * wave - 4 * lane;
      const u32 key[4] = {(v.x << 16) | ib, (v.x & 0xffff0000u) | (ib - 1), (v.z << 16) | (ib - 2), (v.z & 0xffff0000u) | (ib - 3)};
      lds_wr(L, wave * 4, wave_max_u32(max(max(key[0], key[1]), max(key[2], key[3]))));
      LGKM0();
      BAR();
      STAMP(9);
      const u32x4 m0 = *(LAS u32x4*)L, m1 = *(LAS u32x4*)(L + 16);
      const u32 thr = min(min(min(m0.x, m0.y), min(m0.z, m0.w)), min(min(m1.x, m1.y), min(m1.z, m1.w)));
      const u32 c0 = key[0] >= thr, c1 = key[1] >= thr, c2 = key[2] >= thr, c3 = key[3] >= thr;
      if (c0 | c1 | c2 | c3) {
        const u32 p = 64 + 4 * __hip_atomic_fetch_add((LAS u32*)(L + 32), c0 + c1 + c2 + c3, __ATOMIC_RELAXED, __HIP_MEMORY_SCOPE_WORKGROUP);
        lds_wr(L, c0 ? p : 36, key[0]); lds_wr(L, c1 ? p + 4 * c0 : 36, key[1]);
        lds_wr(L, c2 ? p + 4 * (c0 + c1) : 36, key[2]); lds_wr(L, c3 ? p + 4 * (c0 + c1 + c2) : 36, key[3]);
      }
      LGKM0();
      BAR();
      const int n = rfl((int)lds_rd(L, 32));
      _Pragma("nounroll") for (int b = threadIdx.x; b < n; b += 512) {
        const u32 my = lds_rd(L, 64 + 4 * b);
        int rank = 0;
        _Pragma("nounroll") for (int t = 0; t < n; t += 8) {
          const u32x4 o = *(LAS u32x4*)(L + 64 + 4 * t), q = *(LAS u32x4*)(L + 80 + 4 * t);
          rank += (o.x > my) + (o.y > my) + (o.z > my) + (o.w > my) + (q.x > my) + (q.y > my) + (q.z > my) + (q.w > my);
        }
        if (rank < 8) st_sc1_64(u32x2{my ^ 0x7ffu, tag}, rsWS, WS_SEL + rank * 8);
      }
      if (threadIdx.x == 0) st_sc1_32(tag, rsWS, WS_EPOCH);
      STAMP(10);
      LGKM0();
      BAR();
    }
    constexpr u32 F_XS = 81920;
    ldgrp(2);
    if (wave == 0) {
      wait_until(tstart, T_SEL);
      u32x2 sk;
      _Pragma("nounroll") for (int sp = 0; sp < SPIN_CAP; ++sp) {
        sk = ld_sc1_64(rsWS, lr * 8, WS_SEL);
        if (__all(sk.y == tag)) break;
        __builtin_amdgcn_s_sleep(4);
      }
      STAMP(2);
      u32x4 xr[16];
#pragma unroll
      for (int j = 0; j < 16; ++j) xr[j] = __builtin_amdgcn_raw_buffer_load_b128(rsX, (sk.x & 0x7ffu) * 2048 + lc * 16, j * 128, 0);
      u32x2 ms;
      _Pragma("nounroll") for (int sp = 0; sp < SPIN_CAP; ++sp) {
        ms = ld_sc1_64(rsWS, (sk.x & 0x7ffu) * 8, WS_SG);
        if (__all(ms.y == tag)) break;
      }
#pragma unroll
      for (int j = 0; j < 16; ++j) *(LAS u32x4*)(L + F_XS + lr * 2064 + j * 128 + lc * 16) = xr[j];
      *(LAS u32*)(L + F_XS + 8 * 2064 + lr * 4) = ms.x;
      LGKM0();
      lds_wr(L, L_HR, 1);
    }
#pragma unroll
    for (int q = 3; q < GWIN; ++q) ldgrp(q);
#pragma unroll
    for (int q = 0; q < GPRE; ++q) transpose_grp(wb + 8 * q);
    lds_wait_set(L, L_HR);
    f32x4 ga[16];
#pragma unroll
    for (int e = 0; e < 16; ++e) ga[e] = f32x4{0.f, 0.f, 0.f, 0.f};
    const u32 ms = *(LAS u32*)(L + F_XS + 8 * 2064 + (i & 7) * 4);
    const float rsn = __builtin_amdgcn_rcpf(bf_hi(ms)), mrs = -bf_lo(ms) * rsn;
    s16x8 xf;
#pragma unroll
    for (int q = 0; q < 8; ++q) {
      if ((q & 1) == 0) {
        const u32x4 xv = *(LAS u32x4*)(L + F_XS + (i & 7) * 2064 + (wave * 128 + (q >> 1) * 32 + 8 * g) * 2);
        u32x4 xn;
#pragma unroll
        for (int e = 0; e < 4; ++e) xn[e] = pack2(__builtin_fmaf(bf_lo(xv[e]), rsn, mrs), __builtin_fmaf(bf_hi(xv[e]), rsn, mrs));
        xf = as_frag(xn);
      }
      if (q < GPRE) mma_frags8(ga + 8 * (q & 1), wb + q * 8, xf); else mma_cols(ga + 8 * (q & 1), wb + (q % GWIN) * 8, xf);
      SB();
      if (q == 1 || q == 2) ldgrp(q + 5);
    }
    STAMP(4);
    {
      u32 ln = threadIdx.x & 63;
      PIN(ln);
      if ((ln & 15) < 8) {
        const u32 o = wave * 10240 + (ln & 7) * 80 + (ln >> 4) * 16;
#pragma unroll
        for (int e = 0; e < 16; ++e) *(LAS f32x4*)(L + o + e * 640) = ga[e];
      }
    }
    {
      u32 ln = threadIdx.x & 63;
      PIN(ln);
      const u32 e = 4 * ((wave >> 1) & 1) + (ln >> 4), tk = (ln >> 1) & 7, m = 8 * (wave >> 2) + 4 * (wave & 1) + 2 * (ln & 1);
      u32 o0 = lbase + (e * 8 + tk) * 80 + m * 4, o1 = o0 + 40960, oh = WS_H + ((wave < 4) ? la : lb) * 2048 + (wave & 3) * 512 + ln * 8;
      PIN(o0); PIN(o1); PIN(oh);
      LGKM0();
      BAR();
      f32x2 pg[8], pu[8];
#pragma unroll
      for (int w = 0; w < 8; ++w) { pg[w] = *(LAS f32x2*)(LP((w < 4) ? o0 : o1) + (w & 3) * 10240); pu[w] = *(LAS f32x2*)(LP((w < 4) ? o0 : o1) + (w & 3) * 10240 + 5120); }
      SB();
      const f32x2 gs = ((pg[0] + pg[1]) + (pg[2] + pg[3])) + ((pg[4] + pg[5]) + (pg[6] + pg[7])), us = ((pu[0] + pu[1]) + (pu[2] + pu[3])) + ((pu[4] + pu[5]) + (pu[6] + pu[7]));
      const float g0 = rbf(gs.x), g1 = rbf(gs.y);
      const u32 hv = pack2(rbf(g0 * sigm(g0)) * rbf(us.x), rbf(g1 * sigm(g1)) * rbf(us.y));
      if (wave < 4 || two) st_sc1_64(u32x2{hv, tag}, rsWS, oh);
    }
    LGKM0();
    BAR();
    STAMP(8);
    if (wave >= 4) { helper_offsets(); helper_fill(); }
  }
  }

  if (wave >= 4) {
    const int lid = wave - 4;
    if (!dwn) {
      VMCNT(0);
      STAMP(3);
      BAR();
    }
    if (lid >= 2) {
      const u32 sk = lds_rd(L, L_SEL + 4 * (lane & 7));
      u32 hits = 0;
#pragma unroll
      for (int k = 0; k < 4; ++k) hits |= (u32)((((u32)__builtin_amdgcn_readlane((int)sk, lid - 2 + 2 * k) & 0x7ffu) >> 7) == (u32)pan2) << k;
      if (hits) {
        u32 tgo = 0;
        _Pragma("nounroll") for (int sp = 0; sp < SPIN_CAP; ++sp) { tgo = (u32)rfl((int)lds_rd(L, L_GO)); if (tgo) break; __builtin_amdgcn_s_sleep(4); }
        while ((int)((u32)__builtin_amdgcn_s_memrealtime() - tgo) < T_Y) __builtin_amdgcn_s_sleep(4);
        u32x4 v[4][4];
        u32 todo = hits;
        _Pragma("nounroll") for (int sp = 0; sp < SPIN_CAP; ++sp) {
#pragma unroll
          for (int k = 0; k < 4; ++k)
            if ((todo >> k) & 1) {
#pragma unroll
              for (int j = 0; j < 4; ++j) v[k][j] = ld_sc1_128(rsWS, (lane >> 5) * 65536 + (lane & 31) * 16, WS_YS + j * 131072 + (lid - 2 + 2 * k) * 8192 + cg * 512);
            }
#pragma unroll
          for (int k = 0; k < 4; ++k)
            if ((todo >> k) & 1) {
              u32 bad = 0;
              float y0 = 0.f, y1 = 0.f;
#pragma unroll
              for (int j = 0; j < 4; ++j) { bad |= (v[k][j].y ^ tag) | (v[k][j].w ^ tag); y0 += __uint_as_float(v[k][j].x); y1 += __uint_as_float(v[k][j].z); }
              if (__all(bad == 0)) {
                y0 = sum32(y0); y1 = sum32(y1);
                if (lane < 32) {
                  const u32 o = L_UPD + ((lid - 2 + 2 * k) * 64 + 16 * (lane & 3) + (lane >> 2)) * 4;
                  *(LAS float*)(L + o) = y0; *(LAS float*)(L + o + 32) = y1;
                }
                todo &= ~(1u << k);
              }
            }
          if (!todo) break;
          __builtin_amdgcn_s_sleep(8);
        }
      }
      LGKM0();
      lds_wr(L, L_UPDF + 4 * (lid - 2), 1);
      STAMP(7);
    }
    return;
  }

  {
    const bool pol = wave == 0;
    const u32 fo = WS_OF + (((pan2 * 4 + 3 * (lane >> 5)) / 5) * 16 + (lane & 15)) * 128;
    u32 fv = ~tag;
    if (dwn && pol) fv = ld_sc1_32(rsWS, fo, 0);
    SB();
    if (!gemm && !dwn) BAR();
    const int par2 = wave >> 1, t2 = (wave & 1) * 2;
    u32 wa[4], wc[4];
    s16x8 wf[3][4];
    auto wo_addr = [&]() {
      u32 ln = threadIdx.x & 63;
      PIN(ln);
      const u32 g = ln >> 4, i = ln & 15, q4 = i >> 2, p4 = i & 3;
      const u32 wb = lbase + (4 * g + q4) * 128 + p4 * 8 + par2 * 2048, wsw = (2 * g + (q4 >> 1)) & 3;
#pragma unroll
      for (int c = 0; c < 4; ++c) { wa[c] = wb + ((c ^ wsw) << 5); wc[c] = wa[c] ^ 2048; PIN(wa[c]); PIN(wc[c]); }
    };
    auto wf_first = [&]() {
#pragma unroll
      for (int k = 0; k < 2; ++k)
#pragma unroll
        for (int c = 0; c < 4; ++c) wf[k][c] = tr_frag(LP(wa[c]), LP(wc[c]), k * 4096, k * 4096);
    };
    if (gemm) oc_init();
    SB();
    if (gemm) {
      VMCNT(0);
      if (lane == 0 && __hip_atomic_fetch_add((LAS u32*)(L + L_ARR), 1u, __ATOMIC_RELAXED, __HIP_MEMORY_SCOPE_WORKGROUP) == 3)
        st_sc1_32(tag, rsWS, WS_OF + (pan * 16 + hp) * 128);
      STAMP(4);
      SB();
      wo_addr();
      BAR();
      wf_first();
    }
    const int T0 = pan2 * 4 + t2;
    const u32 sk = lds_rd(L, L_SEL + 4 * (lane & 7));
    __builtin_amdgcn_s_waitcnt(0xc07f);
    SB();
    if (!gemm) wait_until(tstart, T_O);
    if (pol) {
      _Pragma("nounroll") for (int sp = 0; sp < SPIN_CAP; ++sp) {
        if (__all(fv == tag)) break;
        if (sp) __builtin_amdgcn_s_sleep(2);
        fv = ld_sc1_32(rsWS, fo, 0);
      }
      lds_wr(L, L_GO, (u32)__builtin_amdgcn_s_memrealtime() | 1u);
    } else {
      _Pragma("nounroll") for (int sp = 0; sp < SPIN_CAP; ++sp) if (rfl((int)lds_rd(L, L_GO))) break;
    }
    CBAR();
    STAMP(5);
    const u32 abase = WS_O + (par2 * 32 * 64 + T0) * 1024;
    u32x4 af[OWIN][2];
#pragma unroll
    for (int hh = 0; hh < OWIN; ++hh) {
#pragma unroll
      for (int tt = 0; tt < 2; ++tt) af[hh][tt] = ld_sc1_128(rsWS, lane * 16 + tt * 1024, abase + ((hh + hrot) & 31) * 65536);
      SB();
    }
    if (!gemm) {
      wo_addr(); wf_first();
      SB();
      oc_init();
      __builtin_amdgcn_s_waitcnt(0xc07f);
      SB();
    }
    auto add_routed = [&]() {
      const u32 stl = (sk & 0x7ffu) - (u32)(pan2 * 128 + par2 + 32 * t2);
      u64 hit = __ballot(stl < 64u && (stl & 1u) == 0u) & 0xffull;
      if (hit) {
        lds_wait_set(L, L_UPDF + (lane & 1) * 4);
        while (hit) {
          const int c = __builtin_ctzll(hit);
          hit &= hit - 1;
          const u32 tl = (u32)__builtin_amdgcn_readlane((int)stl, c), ob = (u32)__builtin_amdgcn_readlane((int)sk, c) >> 16;
          const float sg = rbf(sigm(bf_lo((ob & 0x8000u) ? (ob & 0x7fffu) : (~ob & 0xffffu))));
          const float f0 = ((tl >> 1) == (u32)i) ? sg : 0.f, f1 = ((tl >> 1) == (u32)i + 16u) ? sg : 0.f;
#pragma unroll
          for (int cc = 0; cc < 4; ++cc) {
            const f32x4 u = *(LAS f32x4*)(L + L_UPD + (c * 64 + 16 * cc + 4 * g) * 4);
            oc[0][cc] += u * f0; oc[1][cc] += u * f1;
          }
        }
      }
    };
#pragma unroll
    for (int hh = 0; hh < 32; ++hh) {
      if (hh == 21 && dwn) {
        _Pragma("nounroll") for (int sp = 0; sp < SPIN_CAP; ++sp) if (__all(lds_rd(L, L_HS + (lane & 3) * 4) == 2u)) break;
        CBAR(); SB();
      }
      if (hh == 14) {
#pragma unroll
        for (int c = 0; c < 4; ++c) { wa[c] += 65536; wc[c] += 65536; PIN(wa[c]); PIN(wc[c]); }
        SB();
      }
      if (hh == HITAT) { add_routed(); SB(); }
      if (hh == 31) __builtin_amdgcn_s_waitcnt(0xc07f); else __builtin_amdgcn_s_waitcnt(0xc87f);
      SB();
      if (hh + 2 < 32) {
#pragma unroll
        for (int c = 0; c < 4; ++c) wf[(hh + 2) % 3][c] = tr_frag(LP(wa[c]), LP(wc[c]), ((hh + 2) & 15) * 4096, ((hh + 2) & 15) * 4096);
      }
#pragma unroll
      for (int tt = 0; tt < 2; ++tt) {
#pragma unroll
        for (int c = 0; c < 4; ++c) oc[tt][c] = mfma(wf[hh % 3][c], as_frag(af[hh % OWIN][tt]), oc[tt][c]);
        if (hh + OWIN < 32) af[hh % OWIN][tt] = ld_sc1_128(rsWS, lane * 16 + tt * 1024, abase + ((hh + OWIN + hrot) & 31) * 65536);
      }
#pragma unroll
      for (int k = 0; k < 2; ++k) {
        __builtin_amdgcn_sched_group_barrier(0x008, 1, 0); __builtin_amdgcn_sched_group_barrier(0x100, 2, 0);
        __builtin_amdgcn_sched_group_barrier(0x008, 1, 0); __builtin_amdgcn_sched_group_barrier(0x100, 1, 0);
        __builtin_amdgcn_sched_group_barrier(0x008, 1, 0); __builtin_amdgcn_sched_group_barrier(0x100, 1, 0);
        __builtin_amdgcn_sched_group_barrier(0x008, 1, 0); __builtin_amdgcn_sched_group_barrier(0x020, 1, 0);
      }
      SB();
    }
    STAMP(6);
    const rsrc_t rsO = mk_rsrc(P.out, S_ * D_ * 2);
    const u32 xo = ((pan2 * 128 + par2 + 32 * t2 + 2 * i) * D_ + cg * 64 + (g & 1) * 16 + (g >> 1) * 8) * 2;
#pragma unroll
    for (int tt = 0; tt < 2; ++tt)
#pragma unroll
      for (int cp = 0; cp < 2; ++cp) {
        const u32 a0 = pack2(oc[tt][2 * cp][0], oc[tt][2 * cp][1]), a1 = pack2(oc[tt][2 * cp][2], oc[tt][2 * cp][3]);
        const u32 b0 = pack2(oc[tt][2 * cp + 1][0], oc[tt][2 * cp + 1][1]), b1 = pack2(oc[tt][2 * cp + 1][2], oc[tt][2 * cp + 1][3]);
        const auto e0 = __builtin_amdgcn_permlane16_swap(a0, b0, false, false);
        const auto e1 = __builtin_amdgcn_permlane16_swap(a1, b1, false, false);
        st_sc1_128(u32x4{e0[0], e1[0], e0[1], e1[1]}, rsO, xo + tt * 65536 + cp * 64);
      }
    STAMP(7);
  }
}

static u64 k_order(int odd, int pl, int hl) {
  const int np = 7 - odd;
  int slot[16], used[16];
  for (int k = 0; k < 16; ++k) { slot[k] = -1; used[k] = 0; }
  for (int k = 2; k < 16; ++k) {
    const int h0 = k & 3, p0 = (k * (odd ? 5 : 3)) % np;
    int want = -1;
    if (hl == h0 && pl % np == p0) want = k - 2; else if (hl == h0 || pl % np == p0) want = k - 1;
    if (want >= 0) {
      if (want < 1) want = 1;
      while (want > 1 && used[want]) --want;
      if (!used[want]) { slot[k] = want; used[want] = 1; }
    }
  }
  int s = 0;
  for (int k = 0; k < 16; ++k) if (slot[k] < 0) { while (used[s]) ++s; slot[k] = s; used[s] = 1; }
  u64 r = 0;
  for (int k = 0; k < 16; ++k) r |= (u64)k << (4 * slot[k]);
  return r;
}

extern "C" void kernel_launch(void* const* d_in, const int* in_sizes, int n_in, void* d_out, int out_size, void* d_ws, size_t ws_size,
                              hipStream_t stream) {
  Args A;
  Params& P = A.p;
  P.x = (const u16*)d_in[0]; P.wqkv = (const u16*)d_in[1]; P.wo = (const u16*)d_in[2]; P.wr = (const u16*)d_in[4];
  P.wg = (const u16*)d_in[5]; P.wu = (const u16*)d_in[6]; P.wd = (const u16*)d_in[7];
  P.out = (u16*)d_out; P.ws = (char*)d_ws;
  for (int e = 0; e < 64; ++e) A.kt[e] = k_order(e >> 5, (e >> 2) & 7, e & 3);
  (void)hipFuncSetAttribute((const void*)fused_kernel, hipFuncAttributeMaxDynamicSharedMemorySize, L_END + 64);
  hipLaunchKernelGGL(fused_kernel, dim3(256), dim3(512), L_END + 64, stream, A);
}
```
